```python
import torch

def _softmax(x, axis=-1):
    return torch.softmax(x, dim=axis)

def _gelu(x):
    return torch.nn.functional.gelu(x, approximate='tanh')

def _silu(x):
    return torch.nn.functional.silu(x)

def _sigmoid(x):
    return torch.sigmoid(x)

def _layernorm(x, axis=-1, eps=1e-05):
    m = x.mean(dim=axis, keepdim=True)
    v = x.var(dim=axis, keepdim=True, unbiased=False)
    return (x - m) / torch.sqrt(v + eps)

def _scatter_add(tgt, idx, src, axis=0):
    return tgt.clone().index_add_(axis, idx, src)

def _segment_sum(src, seg, n):
    out = torch.zeros((n,) + src.shape[1:], dtype=src.dtype)
    return out.index_add_(0, seg, src)

def _pad(x, pw):
    flat = tuple((v for pair in reversed(pw) for v in pair))
    return torch.nn.functional.pad(x, flat)
kernel_name = 'sequential_preLN__memory_aug_recurrent_mem_tr__glu_gated_bilinear__prompt'

def setup_inputs():

    _w = lambda a: (lambda t: t.to(torch.bfloat16) if t.is_floating_point() else t)(torch.as_tensor(a))

    def _A_setup():
        B, S, Dm = (1, 2048, 576)
        H, Dh = (32, 128)
        M, seg = (8, 32)
        seg = min(seg, S)
        S = S // seg * seg
        return {'x': _w((lambda *s: torch.randn(*s, dtype=torch.bfloat16) * 0.05)(B, S, Dm)), 'mem0': _w((lambda *s: torch.randn(*s, dtype=torch.bfloat16) * 0.2)(B, M, Dm)), 'Wq': _w((lambda *s: torch.randn(*s, dtype=torch.bfloat16) * 1.6)(Dm, H, Dh) * Dm ** (-0.5)), 'Wk': _w((lambda *s: torch.randn(*s, dtype=torch.bfloat16) * 1.6)(Dm, H, Dh) * Dm ** (-0.5)), 'Wv': _w((lambda *s: torch.randn(*s, dtype=torch.bfloat16) * 0.2)(Dm, H, Dh) * Dm ** (-0.5)), 'Wo': _w((lambda *s: torch.randn(*s, dtype=torch.bfloat16) * 0.2)(H, Dh, Dm) * (H * Dh) ** (-0.5)), 'seg_len': seg}
    _A_in = _A_setup()

    def _B_setup():
        B, S, D, F = (1, 2048, 576, 4608)
        return {'x': _w((lambda *s: torch.randn(*s, dtype=torch.bfloat16) * 0.05)(B, S, D)), 'w_gate': _w((lambda *s: torch.randn(*s, dtype=torch.bfloat16) * 0.2)(D, F) * D ** (-0.5)), 'w_up': _w((lambda *s: torch.randn(*s, dtype=torch.bfloat16) * 0.2)(D, F) * D ** (-0.5)), 'w_down': _w((lambda *s: torch.randn(*s, dtype=torch.bfloat16) * 0.2)(F, D) * F ** (-0.5))}
    _B_in = _B_setup()
    return {'A_x': _A_in['x'], 'A_mem0': _A_in['mem0'], 'A_Wq': _A_in['Wq'], 'A_Wk': _A_in['Wk'], 'A_Wv': _A_in['Wv'], 'A_Wo': _A_in['Wo'], 'A_seg_len': _A_in['seg_len'], 'B_x': _B_in['x'], 'B_w_gate': _B_in['w_gate'], 'B_w_up': _B_in['w_up'], 'B_w_down': _B_in['w_down']}

def reference(A_x, A_mem0, A_Wq, A_Wk, A_Wv, A_Wo, A_seg_len, B_x, B_w_gate, B_w_up, B_w_down):

    def _A_ref(x, mem0, Wq, Wk, Wv, Wo, seg_len):
        B, S, Dm = x.shape
        H, Dh = (Wq.shape[1], Wq.shape[2])
        M = mem0.shape[1]
        scale = Dh ** (-0.5)
        n_seg = S // seg_len
        L = M + seg_len
        idx = torch.arange(L)
        mask = (idx[:, None] < M) | (idx[:, None] >= idx[None, :])
        mem = mem0
        outs = []
        for s in range(n_seg):
            xs = x[:, s * seg_len:(s + 1) * seg_len, :].clone()
            h = torch.concatenate([mem, xs], axis=1)
            q = torch.einsum('btd,dhe->bhte', h, Wq)
            k = torch.einsum('btd,dhe->bhte', h, Wk)
            v = torch.einsum('btd,dhe->bhte', h, Wv)
            scores = torch.einsum('bhqe,bhke->bhqk', q, k) * scale
            scores = torch.where(mask[None, None], scores, -1000000000.0)
            p = _softmax(scores, axis=-1)
            o = torch.einsum('bhqk,bhke->bhqe', p, v)
            o = torch.einsum('bhte,hed->btd', o, Wo)
            mem = o[:, :M, :].clone()
            outs.append(o[:, M:, :])
        return torch.concatenate(outs, axis=1)

    def _B_ref(x, w_gate, w_up, w_down):
        g = torch.einsum('bsd,df->bsf', x, w_gate)
        u = torch.einsum('bsd,df->bsf', x, w_up)
        h = g * u
        return torch.einsum('bsf,fd->bsd', h, w_down)
    x = A_x
    h = x + _A_ref(_layernorm(x, axis=-1), A_mem0, A_Wq, A_Wk, A_Wv, A_Wo, A_seg_len)
    h2 = h + _B_ref(_layernorm(h, axis=-1), B_w_gate, B_w_up, B_w_down)
    return h2
```

```cpp
#include <hip/hip_runtime.h>
#include <hip/hip_bf16.h>
#include <stdint.h>

typedef unsigned u32;
typedef u32 u2 __attribute__((ext_vector_type(2)));
typedef u32 u4 __attribute__((ext_vector_type(4)));
typedef u32 u8v __attribute__((ext_vector_type(8)));
typedef float f2 __attribute__((ext_vector_type(2)));
typedef float f4 __attribute__((ext_vector_type(4)));
typedef short s4 __attribute__((ext_vector_type(4)));
typedef __bf16 bf2 __attribute__((ext_vector_type(2)));
typedef __bf16 bf8 __attribute__((ext_vector_type(8)));
typedef __bf16 bf16v __attribute__((ext_vector_type(16)));
typedef __amdgpu_buffer_rsrc_t rsrc_t;
#define LAS __attribute__((address_space(3)))
#define DEV __device__ __forceinline__

constexpr int SEQ = 2048, DM = 576, NH = 32, DH = 128, NSEG = 64, FF = 4608;
constexpr u32 ROWB = DM * 2;
constexpr u32 TILEB = 32 * ROWB;
constexpr u32 OOB = 0x7fffffffu;
constexpr int SC1 = 16;

constexpr u32 HOP_PROBE = 104u * 16384u;
constexpr u32 OFF_CFG = 0x200000;
constexpr u32 OFF_MBP1 = 0x201000;
constexpr u32 OFF_MBKV = 106u * 16384u;
constexpr u32 OFF_MBAB = OFF_MBP1 + 768 * 64;
constexpr u32 OFF_MBC = OFF_MBAB + 36 * 32 * 64;
constexpr u32 OFF_MBDE = OFF_MBC + 72 * 128 * 64;
constexpr u32 CTL_END = OFF_MBDE + 36 * 72 * 64;
constexpr u32 CTL_BYTES = 0x300000;
static_assert(CTL_END <= CTL_BYTES, "control region");
constexpr size_t OFF_XF = CTL_BYTES;
constexpr size_t OFF_QKV = OFF_XF + (size_t)NSEG * TILEB;
constexpr u32 QKV_M = NSEG * NH * 8192;
constexpr size_t OFF_KM = OFF_QKV + 3 * (size_t)QKV_M;
constexpr size_t OFF_VM = OFF_KM + (size_t)NSEG * NH * 4096;
constexpr size_t OFF_PAB = OFF_VM + (size_t)NSEG * NH * 4096;
constexpr size_t OFF_HN = OFF_PAB + (size_t)NSEG * NH * TILEB;
constexpr u32 HSEG = TILEB + 4608;
constexpr size_t OFF_PDE = OFF_HN + (size_t)NSEG * HSEG;
constexpr size_t WS_END = OFF_PDE + (size_t)NSEG * 36 * TILEB;
static_assert(WS_END + (2u << 20) <= (256u << 20), "workspace");

constexpr int LDS_BYTES = 163840;
constexpr u32 SPIN_CAP = 1u << 18;

struct Params {
    const char* x; const char* mem0; const char* wq; const char* wk; const char* wv; const char* wo;
    const char* wg; const char* wu; const char* wd; char* out; char* ws;
    int mask, d1, d2, d3, map, ring;
};

DEV rsrc_t mk(const void* p, u32 bytes) { return __builtin_amdgcn_make_buffer_rsrc((void*)p, (short)0, (int)bytes, 0x00020000); }
DEV u4 ld4(rsrc_t r, u32 v, u32 s) { return __builtin_amdgcn_raw_buffer_load_b128(r, v, s, SC1); }
DEV u2 ld2(rsrc_t r, u32 v, u32 s) { return __builtin_amdgcn_raw_buffer_load_b64(r, v, s, SC1); }
DEV u32 ld1(rsrc_t r, u32 v, u32 s) { return __builtin_amdgcn_raw_buffer_load_b32(r, v, s, SC1); }
template <int AUX = SC1> DEV void st4(u4 d, rsrc_t r, u32 v, u32 s) {
    __builtin_amdgcn_raw_buffer_store_b128(d, r, v, s, AUX);
    asm volatile("s_nop 1" :: "v"(d) : "memory");
}
DEV void st2(u2 d, rsrc_t r, u32 v, u32 s) { __builtin_amdgcn_raw_buffer_store_b64(d, r, v, s, SC1); }
DEV void st1(u32 d, rsrc_t r, u32 v, u32 s) { __builtin_amdgcn_raw_buffer_store_b32(d, r, v, s, SC1); }
DEV f4 mma32(u4 a, u4 b, f4 c) { return __builtin_amdgcn_mfma_f32_16x16x32_bf16(__builtin_bit_cast(bf8, a), __builtin_bit_cast(bf8, b), c, 0, 0, 0); }
DEV f4 mma16(u2 a, u2 b, f4 c) { return __builtin_amdgcn_mfma_f32_16x16x16bf16_1k(__builtin_bit_cast(s4, a), __builtin_bit_cast(s4, b), c, 0, 0, 0); }
DEV f4 smma(u4 a, u8v b, f4 c, u32 idx) { return __builtin_amdgcn_smfmac_f32_16x16x64_bf16(__builtin_bit_cast(bf8, a), __builtin_bit_cast(bf16v, b), c, (int)idx, 0, 0); }
DEV u32 pk(float a, float b) { f2 v = {a, b}; return __builtin_bit_cast(u32, __builtin_convertvector(v, bf2)); }
DEV u2 pk4(f4 v) { u2 r = {pk(v[0], v[1]), pk(v[2], v[3])}; return r; }
DEV u4 cat(u2 a, u2 b) { u4 r = {a[0], a[1], b[0], b[1]}; return r; }
DEV float blo(u32 v) { return __builtin_bit_cast(float, v << 16); }
DEV float bhi(u32 v) { return __builtin_bit_cast(float, v & 0xffff0000u); }
DEV u32 fb(float v) { return __builtin_bit_cast(u32, v); }
DEV float bf(u32 v) { return __builtin_bit_cast(float, v); }
DEV f4 halfsum(f4 x, f4 y) {
    f4 r;
#pragma unroll
    for (int i = 0; i < 4; ++i) { auto t = __builtin_amdgcn_permlane32_swap(fb(x[i]), fb(y[i]), false, false); r[i] = bf(t[0]) + bf(t[1]); }
    return r;
}
DEV u4 merge16(u2 a, u2 b) {
    auto t0 = __builtin_amdgcn_permlane16_swap(a[0], b[0], false, false);
    auto t1 = __builtin_amdgcn_permlane16_swap(a[1], b[1], false, false);
    u4 r = {t0[0], t1[0], t0[1], t1[1]};
    return r;
}
DEV float max_x16_x32(float m) {
    auto a = __builtin_amdgcn_permlane16_swap(fb(m), fb(m), false, false);
    m = __builtin_fmaxf(bf(a[0]), bf(a[1]));
    auto b = __builtin_amdgcn_permlane32_swap(fb(m), fb(m), false, false);
    return __builtin_fmaxf(bf(b[0]), bf(b[1]));
}
DEV float sum_x16(float m) { auto a = __builtin_amdgcn_permlane16_swap(fb(m), fb(m), false, false); return bf(a[0]) + bf(a[1]); }
DEV float sum_x32(float m) { auto a = __builtin_amdgcn_permlane32_swap(fb(m), fb(m), false, false); return bf(a[0]) + bf(a[1]); }
template <int N> DEV float ror(float v) { return bf(__builtin_amdgcn_update_dpp(0u, fb(v), 0x120 + N, 0xf, 0xf, false)); }
DEV void bar() { asm volatile("s_waitcnt lgkmcnt(0)\n\ts_barrier" ::: "memory"); }
template <int N> DEV void waitv() { asm volatile("s_waitcnt vmcnt(%0)" :: "i"(N) : "memory"); }
DEV void fence() { asm volatile("" ::: "memory"); }
DEV void sched() { __builtin_amdgcn_sched_barrier(0); }
DEV void dma(rsrc_t r, u32 voff, u32 soff, u32 ldsoff) {
    asm volatile("s_mov_b32 m0, %0\n\ts_nop 4\n\tbuffer_load_dwordx4 %1, %2, %3 offen lds" :: "s"(ldsoff), "v"(voff), "s"(r), "s"(soff) : "memory");
}
DEV void delay(int n) { for (int i = 0; i < n; ++i) asm volatile("s_nop 0" ::: "memory"); }
DEV u4 ldsr(const LAS char* p) { return *(const LAS u4*)p; }
DEV u2 ldstr(const LAS char* p) { return __builtin_bit_cast(u2, __builtin_amdgcn_ds_read_tr16_b64_v4i16((LAS s4*)p)); }

DEV void wait_words(rsrc_t ctl, u32 voff, bool act, u32 need, bool& dead) {
    u32 spins = 0;
    while (!dead) {
        fence();
        const u32 v = ld1(ctl, act ? voff : OOB, 0);
        if (__all(!act || v >= need)) break;
        if (++spins > SPIN_CAP) dead = true;
        __builtin_amdgcn_s_sleep(1);
    }
    fence();
}
DEV bool test_words(u32 v, bool act, u32 need) { return __all(!act || v >= need); }

#define STAMP(id)
DEV void role_chain(u32 ids) {
    asm volatile(
    "s_nop 4\n"
    "s_load_dwordx16 s[40:55], %0, 0x0\n"
    "s_load_dwordx2 s[56:57], %0, 0x50\n"
    "s_load_dwordx4 s[60:63], %0, 0x58\n"
    "s_and_b32 s64, %1, 0xff\n"
    "s_bfe_u32 s65, %1, 0x40008\n"
    "s_bfe_u32 s66, %1, 0x4000c\n"
    "s_bfe_u32 s67, %1, 0x80010\n"
    "s_lshr_b32 s68, %1, 24\n"
    "s_waitcnt lgkmcnt(0)\n"
    "s_add_u32 s20, s56, 0\n"
    "s_addc_u32 s21, s57, 0\n"
    "s_and_b32 s21, s21, 0xffff\n"
    "s_mov_b32 s22, 0x300000\n"
    "s_mov_b32 s23, 0x20000\n"
    "s_add_u32 s24, s56, 0x540000\n"
    "s_addc_u32 s25, s57, 0\n"
    "s_and_b32 s25, s25, 0xffff\n"
    "s_mov_b32 s26, 0x3000000\n"
    "s_mov_b32 s27, 0x20000\n"
    "s_add_u32 s28, s56, 0x3540000\n"
    "s_addc_u32 s29, s57, 0\n"
    "s_and_b32 s29, s29, 0xffff\n"
    "s_mov_b32 s30, 0x1000000\n"
    "s_mov_b32 s31, 0x20000\n"
    "v_mbcnt_lo_u32_b32 v150, -1, 0\n"
    "v_mbcnt_hi_u32_b32 v150, -1, v150\n"
    "s_lshr_b32 s79, s68, 1\n"
    "v_lshrrev_b32 v151, 3, v150\n"
    "v_and_b32 v152, 6, v151\n"
    "v_and_b32 v148, 7, v150\n"
    "v_xor_b32 v148, v148, v152\n"
    "s_mul_i32 s80, s79, 6\n"
    "v_xor_b32 v148, s80, v148\n"
    "v_lshlrev_b32 v148, 4, v148\n"
    "s_mov_b32 s80, 8192\n"
    "v_mad_u32_u24 v148, v151, s80, v148\n"
    "s_and_b32 s79, s68, 1\n"
    "v_lshrrev_b32 v151, 3, v150\n"
    "v_and_b32 v152, 6, v151\n"
    "v_and_b32 v149, 7, v150\n"
    "v_xor_b32 v149, v149, v152\n"
    "s_mul_i32 s80, s79, 6\n"
    "v_xor_b32 v149, s80, v149\n"
    "v_lshlrev_b32 v149, 4, v149\n"
    "s_mov_b32 s80, 1152\n"
    "v_mad_u32_u24 v149, v151, s80, v149\n"
    "s_add_u32 s36, s44, 0\n"
    "s_addc_u32 s37, s45, 0\n"
    "s_and_b32 s37, s37, 0xffff\n"
    "s_mov_b32 s38, 0x480000\n"
    "s_mov_b32 s39, 0x20000\n"
    "s_lshr_b32 s79, s68, 1\n"
    "s_add_u32 s79, s79, 0\n"
    "s_lshl_b32 s79, s79, 16\n"
    "s_and_b32 s80, s68, 1\n"
    "s_lshl_b32 s80, s80, 7\n"
    "s_add_u32 s79, s79, s80\n"
    "s_lshl_b32 s80, s64, 8\n"
    "s_add_u32 s79, s79, s80\n"
    "s_lshl_b32 s80, s68, 10\n"
    "s_add_u32 m0, s80, 0\n"
    "s_nop 0\n"
    "buffer_load_dwordx4 v148, s[36:39], s79 offen lds\n"
    "s_add_u32 s79, s79, 131072\n"
    "s_add_u32 m0, m0, 4096\n"
    "s_nop 0\n"
    "buffer_load_dwordx4 v148, s[36:39], s79 offen lds\n"
    "s_add_u32 s79, s79, 131072\n"
    "s_add_u32 m0, m0, 4096\n"
    "s_nop 0\n"
    "buffer_load_dwordx4 v148, s[36:39], s79 offen lds\n"
    "s_add_u32 s79, s79, 131072\n"
    "s_add_u32 m0, m0, 4096\n"
    "s_nop 0\n"
    "buffer_load_dwordx4 v148, s[36:39], s79 offen lds\n"
    "s_add_u32 s79, s79, 131072\n"
    "s_add_u32 m0, m0, 4096\n"
    "s_nop 0\n"
    "buffer_load_dwordx4 v148, s[36:39], s79 offen lds\n"
    "s_add_u32 s79, s79, 131072\n"
    "s_add_u32 m0, m0, 4096\n"
    "s_nop 0\n"
    "buffer_load_dwordx4 v148, s[36:39], s79 offen lds\n"
    "s_add_u32 s79, s79, 131072\n"
    "s_add_u32 m0, m0, 4096\n"
    "s_nop 0\n"
    "buffer_load_dwordx4 v148, s[36:39], s79 offen lds\n"
    "s_add_u32 s79, s79, 131072\n"
    "s_add_u32 m0, m0, 4096\n"
    "s_nop 0\n"
    "buffer_load_dwordx4 v148, s[36:39], s79 offen lds\n"
    "s_add_u32 s79, s79, 131072\n"
    "s_add_u32 m0, m0, 4096\n"
    "s_nop 0\n"
    "buffer_load_dwordx4 v148, s[36:39], s79 offen lds\n"
    "s_add_u32 s79, s79, 131072\n"
    "s_add_u32 m0, m0, 4096\n"
    "s_nop 0\n"
    "buffer_load_dwordx4 v148, s[36:39], s79 offen lds\n"
    "s_add_u32 s79, s79, 131072\n"
    "s_add_u32 m0, m0, 4096\n"
    "s_nop 0\n"
    "buffer_load_dwordx4 v148, s[36:39], s79 offen lds\n"
    "s_add_u32 s79, s79, 131072\n"
    "s_add_u32 m0, m0, 4096\n"
    "s_nop 0\n"
    "buffer_load_dwordx4 v148, s[36:39], s79 offen lds\n"
    "s_add_u32 s79, s79, 131072\n"
    "s_add_u32 m0, m0, 4096\n"
    "s_nop 0\n"
    "buffer_load_dwordx4 v148, s[36:39], s79 offen lds\n"
    "s_add_u32 s79, s79, 131072\n"
    "s_add_u32 m0, m0, 4096\n"
    "s_nop 0\n"
    "buffer_load_dwordx4 v148, s[36:39], s79 offen lds\n"
    "s_add_u32 s79, s79, 131072\n"
    "s_add_u32 m0, m0, 4096\n"
    "s_nop 0\n"
    "buffer_load_dwordx4 v148, s[36:39], s79 offen lds\n"
    "s_add_u32 s79, s79, 131072\n"
    "s_add_u32 m0, m0, 4096\n"
    "s_nop 0\n"
    "buffer_load_dwordx4 v148, s[36:39], s79 offen lds\n"
    "s_add_u32 s79, s79, 131072\n"
    "s_add_u32 m0, m0, 4096\n"
    "s_nop 0\n"
    "buffer_load_dwordx4 v148, s[36:39], s79 offen lds\n"
    "s_add_u32 s79, s79, 131072\n"
    "s_add_u32 m0, m0, 4096\n"
    "s_nop 0\n"
    "buffer_load_dwordx4 v148, s[36:39], s79 offen lds\n"
    "s_add_u32 s79, s79, 131072\n"
    "s_add_u32 m0, m0, 4096\n"
    "s_nop 0\n"
    "buffer_load_dwordx4 v148, s[36:39], s79 offen lds\n"
    "s_add_u32 s79, s79, 131072\n"
    "s_add_u32 m0, m0, 4096\n"
    "s_nop 0\n"
    "buffer_load_dwordx4 v148, s[36:39], s79 offen lds\n"
    "s_add_u32 s79, s79, 131072\n"
    "s_add_u32 m0, m0, 4096\n"
    "s_add_u32 s36, s44, 0\n"
    "s_addc_u32 s37, s45, 0\n"
    "s_and_b32 s37, s37, 0xffff\n"
    "s_mov_b32 s38, 0x480000\n"
    "s_mov_b32 s39, 0x20000\n"
    "s_lshr_b32 s79, s68, 1\n"
    "s_add_u32 s79, s79, 40\n"
    "s_lshl_b32 s79, s79, 16\n"
    "s_and_b32 s80, s68, 1\n"
    "s_lshl_b32 s80, s80, 7\n"
    "s_add_u32 s79, s79, s80\n"
    "s_lshl_b32 s80, s64, 8\n"
    "s_add_u32 s79, s79, s80\n"
    "s_lshl_b32 s80, s68, 10\n"
    "s_add_u32 m0, s80, 81920\n"
    "s_nop 0\n"
    "buffer_load_dwordx4 v148, s[36:39], s79 offen lds\n"
    "s_add_u32 s79, s79, 131072\n"
    "s_add_u32 m0, m0, 4096\n"
    "s_nop 0\n"
    "buffer_load_dwordx4 v148, s[36:39], s79 offen lds\n"
    "s_add_u32 s79, s79, 131072\n"
    "s_add_u32 m0, m0, 4096\n"
    "s_nop 0\n"
    "buffer_load_dwordx4 v148, s[36:39], s79 offen lds\n"
    "s_add_u32 s79, s79, 131072\n"
    "s_add_u32 m0, m0, 4096\n"
    "s_nop 0\n"
    "buffer_load_dwordx4 v148, s[36:39], s79 offen lds\n"
    "s_add_u32 s79, s79, 131072\n"
    "s_add_u32 m0, m0, 4096\n"
    "s_nop 0\n"
    "buffer_load_dwordx4 v148, s[36:39], s79 offen lds\n"
    "s_add_u32 s79, s79, 131072\n"
    "s_add_u32 m0, m0, 4096\n"
    "s_nop 0\n"
    "buffer_load_dwordx4 v148, s[36:39], s79 offen lds\n"
    "s_add_u32 s79, s79, 131072\n"
    "s_add_u32 m0, m0, 4096\n"
    "s_nop 0\n"
    "buffer_load_dwordx4 v148, s[36:39], s79 offen lds\n"
    "s_add_u32 s79, s79, 131072\n"
    "s_add_u32 m0, m0, 4096\n"
    "s_nop 0\n"
    "buffer_load_dwordx4 v148, s[36:39], s79 offen lds\n"
    "s_add_u32 s79, s79, 131072\n"
    "s_add_u32 m0, m0, 4096\n"
    "s_nop 0\n"
    "buffer_load_dwordx4 v148, s[36:39], s79 offen lds\n"
    "s_add_u32 s79, s79, 131072\n"
    "s_add_u32 m0, m0, 4096\n"
    "s_nop 0\n"
    "buffer_load_dwordx4 v148, s[36:39], s79 offen lds\n"
    "s_add_u32 s79, s79, 131072\n"
    "s_add_u32 m0, m0, 4096\n"
    "s_nop 0\n"
    "buffer_load_dwordx4 v148, s[36:39], s79 offen lds\n"
    "s_add_u32 s79, s79, 131072\n"
    "s_add_u32 m0, m0, 4096\n"
    "s_nop 0\n"
    "buffer_load_dwordx4 v148, s[36:39], s79 offen lds\n"
    "s_add_u32 s79, s79, 131072\n"
    "s_add_u32 m0, m0, 4096\n"
    "s_nop 0\n"
    "buffer_load_dwordx4 v148, s[36:39], s79 offen lds\n"
    "s_add_u32 s79, s79, 131072\n"
    "s_add_u32 m0, m0, 4096\n"
    "s_nop 0\n"
    "buffer_load_dwordx4 v148, s[36:39], s79 offen lds\n"
    "s_add_u32 s79, s79, 131072\n"
    "s_add_u32 m0, m0, 4096\n"
    "s_nop 0\n"
    "buffer_load_dwordx4 v148, s[36:39], s79 offen lds\n"
    "s_add_u32 s79, s79, 131072\n"
    "s_add_u32 m0, m0, 4096\n"
    "s_nop 0\n"
    "buffer_load_dwordx4 v148, s[36:39], s79 offen lds\n"
    "s_add_u32 s79, s79, 131072\n"
    "s_add_u32 m0, m0, 4096\n"
    "v_bfe_u32 v157, v150, 4, 1\n"
    "v_bfe_u32 v158, v150, 3, 1\n"
    "v_xor_b32 v151, v157, v158\n"
    "v_lshlrev_b32 v151, 6, v151\n"
    "v_lshl_or_b32 v153, v158, 5, v151\n"
    "v_xor_b32 v154, 32, v153\n"
    "v_lshrrev_b32 v151, 5, v150\n"
    "v_lshl_or_b32 v151, v151, 1, v158\n"
    "s_mov_b32 s79, 2048\n"
    "v_mul_u32_u24 v155, s79, v151\n"
    "v_bfe_u32 v151, v150, 2, 1\n"
    "v_lshl_or_b32 v151, v157, 2, v151\n"
    "v_lshl_add_u32 v155, v151, 7, v155\n"
    "v_and_b32 v151, 3, v150\n"
    "v_lshl_add_u32 v155, v151, 3, v155\n"
    "v_add_u32 v156, 256, v155\n"
    "s_lshl_b32 s79, s68, 1\n"
    "s_add_u32 s79, s79, 0\n"
    "s_and_b32 s80, s79, 3\n"
    "s_lshl_b32 s80, s80, 5\n"
    "v_xor_b32 v144, s80, v153\n"
    "s_lshr_b32 s80, s79, 2\n"
    "s_lshl_b32 s80, s80, 10\n"
    "s_add_u32 s80, s80, 0\n"
    "v_add3_u32 v144, v144, v155, s80\n"
    "s_and_b32 s80, s79, 3\n"
    "s_lshl_b32 s80, s80, 5\n"
    "v_xor_b32 v145, s80, v154\n"
    "s_lshr_b32 s80, s79, 2\n"
    "s_lshl_b32 s80, s80, 10\n"
    "s_add_u32 s80, s80, 0\n"
    "v_add3_u32 v145, v145, v156, s80\n"
    "s_lshl_b32 s79, s68, 1\n"
    "s_add_u32 s79, s79, 1\n"
    "s_and_b32 s80, s79, 3\n"
    "s_lshl_b32 s80, s80, 5\n"
    "v_xor_b32 v146, s80, v153\n"
    "s_lshr_b32 s80, s79, 2\n"
    "s_lshl_b32 s80, s80, 10\n"
    "s_add_u32 s80, s80, 0\n"
    "v_add3_u32 v146, v146, v155, s80\n"
    "s_and_b32 s80, s79, 3\n"
    "s_lshl_b32 s80, s80, 5\n"
    "v_xor_b32 v147, s80, v154\n"
    "s_lshr_b32 s80, s79, 2\n"
    "s_lshl_b32 s80, s80, 10\n"
    "s_add_u32 s80, s80, 0\n"
    "v_add3_u32 v147, v147, v156, s80\n"
    "s_waitcnt vmcnt(16)\n"
    "s_waitcnt lgkmcnt(0)\n"
    "s_barrier\n"
    "ds_read_b64_tr_b16 a[0:1], v144\n"
    "ds_read_b64_tr_b16 a[2:3], v145\n"
    "ds_read_b64_tr_b16 a[4:5], v144 offset:8192\n"
    "ds_read_b64_tr_b16 a[6:7], v145 offset:8192\n"
    "ds_read_b64_tr_b16 a[72:73], v146\n"
    "ds_read_b64_tr_b16 a[74:75], v147\n"
    "ds_read_b64_tr_b16 a[76:77], v146 offset:8192\n"
    "ds_read_b64_tr_b16 a[78:79], v147 offset:8192\n"
    "ds_read_b64_tr_b16 a[8:9], v144 offset:16384\n"
    "ds_read_b64_tr_b16 a[10:11], v145 offset:16384\n"
    "ds_read_b64_tr_b16 a[12:13], v144 offset:24576\n"
    "ds_read_b64_tr_b16 a[14:15], v145 offset:24576\n"
    "ds_read_b64_tr_b16 a[80:81], v146 offset:16384\n"
    "ds_read_b64_tr_b16 a[82:83], v147 offset:16384\n"
    "ds_read_b64_tr_b16 a[84:85], v146 offset:24576\n"
    "ds_read_b64_tr_b16 a[86:87], v147 offset:24576\n"
    "ds_read_b64_tr_b16 a[16:17], v144 offset:32768\n"
    "ds_read_b64_tr_b16 a[18:19], v145 offset:32768\n"
    "ds_read_b64_tr_b16 a[20:21], v144 offset:40960\n"
    "ds_read_b64_tr_b16 a[22:23], v145 offset:40960\n"
    "ds_read_b64_tr_b16 a[88:89], v146 offset:32768\n"
    "ds_read_b64_tr_b16 a[90:91], v147 offset:32768\n"
    "ds_read_b64_tr_b16 a[92:93], v146 offset:40960\n"
    "ds_read_b64_tr_b16 a[94:95], v147 offset:40960\n"
    "ds_read_b64_tr_b16 a[24:25], v144 offset:49152\n"
    "ds_read_b64_tr_b16 a[26:27], v145 offset:49152\n"
    "ds_read_b64_tr_b16 a[28:29], v144 offset:57344\n"
    "ds_read_b64_tr_b16 a[30:31], v145 offset:57344\n"
    "ds_read_b64_tr_b16 a[96:97], v146 offset:49152\n"
    "ds_read_b64_tr_b16 a[98:99], v147 offset:49152\n"
    "ds_read_b64_tr_b16 a[100:101], v146 offset:57344\n"
    "ds_read_b64_tr_b16 a[102:103], v147 offset:57344\n"
    "v_add_u32 v144, 0x10000, v144\n"
    "v_add_u32 v145, 0x10000, v145\n"
    "v_add_u32 v146, 0x10000, v146\n"
    "v_add_u32 v147, 0x10000, v147\n"
    "ds_read_b64_tr_b16 a[32:33], v144\n"
    "ds_read_b64_tr_b16 a[34:35], v145\n"
    "ds_read_b64_tr_b16 a[36:37], v144 offset:8192\n"
    "ds_read_b64_tr_b16 a[38:39], v145 offset:8192\n"
    "ds_read_b64_tr_b16 a[104:105], v146\n"
    "ds_read_b64_tr_b16 a[106:107], v147\n"
    "ds_read_b64_tr_b16 a[108:109], v146 offset:8192\n"
    "ds_read_b64_tr_b16 a[110:111], v147 offset:8192\n"
    "s_waitcnt vmcnt(0)\n"
    "s_waitcnt lgkmcnt(0)\n"
    "s_barrier\n"
    "s_add_u32 s36, s46, 0\n"
    "s_addc_u32 s37, s47, 0\n"
    "s_and_b32 s37, s37, 0xffff\n"
    "s_mov_b32 s38, 0x480000\n"
    "s_mov_b32 s39, 0x20000\n"
    "s_lshr_b32 s79, s68, 1\n"
    "s_add_u32 s79, s79, 0\n"
    "s_lshl_b32 s79, s79, 16\n"
    "s_and_b32 s80, s68, 1\n"
    "s_lshl_b32 s80, s80, 7\n"
    "s_add_u32 s79, s79, s80\n"
    "s_lshl_b32 s80, s64, 8\n"
    "s_add_u32 s79, s79, s80\n"
    "s_lshl_b32 s80, s68, 10\n"
    "s_add_u32 m0, s80, 0\n"
    "s_nop 0\n"
    "buffer_load_dwordx4 v148, s[36:39], s79 offen lds\n"
    "s_add_u32 s79, s79, 131072\n"
    "s_add_u32 m0, m0, 4096\n"
    "s_nop 0\n"
    "buffer_load_dwordx4 v148, s[36:39], s79 offen lds\n"
    "s_add_u32 s79, s79, 131072\n"
    "s_add_u32 m0, m0, 4096\n"
    "s_nop 0\n"
    "buffer_load_dwordx4 v148, s[36:39], s79 offen lds\n"
    "s_add_u32 s79, s79, 131072\n"
    "s_add_u32 m0, m0, 4096\n"
    "s_nop 0\n"
    "buffer_load_dwordx4 v148, s[36:39], s79 offen lds\n"
    "s_add_u32 s79, s79, 131072\n"
    "s_add_u32 m0, m0, 4096\n"
    "s_nop 0\n"
    "buffer_load_dwordx4 v148, s[36:39], s79 offen lds\n"
    "s_add_u32 s79, s79, 131072\n"
    "s_add_u32 m0, m0, 4096\n"
    "s_nop 0\n"
    "buffer_load_dwordx4 v148, s[36:39], s79 offen lds\n"
    "s_add_u32 s79, s79, 131072\n"
    "s_add_u32 m0, m0, 4096\n"
    "s_nop 0\n"
    "buffer_load_dwordx4 v148, s[36:39], s79 offen lds\n"
    "s_add_u32 s79, s79, 131072\n"
    "s_add_u32 m0, m0, 4096\n"
    "s_nop 0\n"
    "buffer_load_dwordx4 v148, s[36:39], s79 offen lds\n"
    "s_add_u32 s79, s79, 131072\n"
    "s_add_u32 m0, m0, 4096\n"
    "s_nop 0\n"
    "buffer_load_dwordx4 v148, s[36:39], s79 offen lds\n"
    "s_add_u32 s79, s79, 131072\n"
    "s_add_u32 m0, m0, 4096\n"
    "s_nop 0\n"
    "buffer_load_dwordx4 v148, s[36:39], s79 offen lds\n"
    "s_add_u32 s79, s79, 131072\n"
    "s_add_u32 m0, m0, 4096\n"
    "s_nop 0\n"
    "buffer_load_dwordx4 v148, s[36:39], s79 offen lds\n"
    "s_add_u32 s79, s79, 131072\n"
    "s_add_u32 m0, m0, 4096\n"
    "s_nop 0\n"
    "buffer_load_dwordx4 v148, s[36:39], s79 offen lds\n"
    "s_add_u32 s79, s79, 131072\n"
    "s_add_u32 m0, m0, 4096\n"
    "s_nop 0\n"
    "buffer_load_dwordx4 v148, s[36:39], s79 offen lds\n"
    "s_add_u32 s79, s79, 131072\n"
    "s_add_u32 m0, m0, 4096\n"
    "s_nop 0\n"
    "buffer_load_dwordx4 v148, s[36:39], s79 offen lds\n"
    "s_add_u32 s79, s79, 131072\n"
    "s_add_u32 m0, m0, 4096\n"
    "s_nop 0\n"
    "buffer_load_dwordx4 v148, s[36:39], s79 offen lds\n"
    "s_add_u32 s79, s79, 131072\n"
    "s_add_u32 m0, m0, 4096\n"
    "s_nop 0\n"
    "buffer_load_dwordx4 v148, s[36:39], s79 offen lds\n"
    "s_add_u32 s79, s79, 131072\n"
    "s_add_u32 m0, m0, 4096\n"
    "s_nop 0\n"
    "buffer_load_dwordx4 v148, s[36:39], s79 offen lds\n"
    "s_add_u32 s79, s79, 131072\n"
    "s_add_u32 m0, m0, 4096\n"
    "s_nop 0\n"
    "buffer_load_dwordx4 v148, s[36:39], s79 offen lds\n"
    "s_add_u32 s79, s79, 131072\n"
    "s_add_u32 m0, m0, 4096\n"
    "s_nop 0\n"
    "buffer_load_dwordx4 v148, s[36:39], s79 offen lds\n"
    "s_add_u32 s79, s79, 131072\n"
    "s_add_u32 m0, m0, 4096\n"
    "s_nop 0\n"
    "buffer_load_dwordx4 v148, s[36:39], s79 offen lds\n"
    "s_add_u32 s79, s79, 131072\n"
    "s_add_u32 m0, m0, 4096\n"
    "ds_read_b64_tr_b16 a[40:41], v144 offset:16384\n"
    "ds_read_b64_tr_b16 a[42:43], v145 offset:16384\n"
    "ds_read_b64_tr_b16 a[44:45], v144 offset:24576\n"
    "ds_read_b64_tr_b16 a[46:47], v145 offset:24576\n"
    "ds_read_b64_tr_b16 a[112:113], v146 offset:16384\n"
    "ds_read_b64_tr_b16 a[114:115], v147 offset:16384\n"
    "ds_read_b64_tr_b16 a[116:117], v146 offset:24576\n"
    "ds_read_b64_tr_b16 a[118:119], v147 offset:24576\n"
    "ds_read_b64_tr_b16 a[48:49], v144 offset:32768\n"
    "ds_read_b64_tr_b16 a[50:51], v145 offset:32768\n"
    "ds_read_b64_tr_b16 a[52:53], v144 offset:40960\n"
    "ds_read_b64_tr_b16 a[54:55], v145 offset:40960\n"
    "ds_read_b64_tr_b16 a[120:121], v146 offset:32768\n"
    "ds_read_b64_tr_b16 a[122:123], v147 offset:32768\n"
    "ds_read_b64_tr_b16 a[124:125], v146 offset:40960\n"
    "ds_read_b64_tr_b16 a[126:127], v147 offset:40960\n"
    "ds_read_b64_tr_b16 a[56:57], v144 offset:49152\n"
    "ds_read_b64_tr_b16 a[58:59], v145 offset:49152\n"
    "ds_read_b64_tr_b16 a[60:61], v144 offset:57344\n"
    "ds_read_b64_tr_b16 a[62:63], v145 offset:57344\n"
    "ds_read_b64_tr_b16 a[128:129], v146 offset:49152\n"
    "ds_read_b64_tr_b16 a[130:131], v147 offset:49152\n"
    "ds_read_b64_tr_b16 a[132:133], v146 offset:57344\n"
    "ds_read_b64_tr_b16 a[134:135], v147 offset:57344\n"
    "v_add_u32 v144, 0x10000, v144\n"
    "v_add_u32 v145, 0x10000, v145\n"
    "v_add_u32 v146, 0x10000, v146\n"
    "v_add_u32 v147, 0x10000, v147\n"
    "ds_read_b64_tr_b16 a[64:65], v144\n"
    "ds_read_b64_tr_b16 a[66:67], v145\n"
    "ds_read_b64_tr_b16 a[68:69], v144 offset:8192\n"
    "ds_read_b64_tr_b16 a[70:71], v145 offset:8192\n"
    "ds_read_b64_tr_b16 a[136:137], v146\n"
    "ds_read_b64_tr_b16 a[138:139], v147\n"
    "ds_read_b64_tr_b16 a[140:141], v146 offset:8192\n"
    "ds_read_b64_tr_b16 a[142:143], v147 offset:8192\n"
    "s_waitcnt vmcnt(0)\n"
    "s_waitcnt lgkmcnt(0)\n"
    "s_barrier\n"
    "s_add_u32 s36, s46, 0\n"
    "s_addc_u32 s37, s47, 0\n"
    "s_and_b32 s37, s37, 0xffff\n"
    "s_mov_b32 s38, 0x480000\n"
    "s_mov_b32 s39, 0x20000\n"
    "s_lshr_b32 s79, s68, 1\n"
    "s_add_u32 s79, s79, 40\n"
    "s_lshl_b32 s79, s79, 16\n"
    "s_and_b32 s80, s68, 1\n"
    "s_lshl_b32 s80, s80, 7\n"
    "s_add_u32 s79, s79, s80\n"
    "s_lshl_b32 s80, s64, 8\n"
    "s_add_u32 s79, s79, s80\n"
    "s_lshl_b32 s80, s68, 10\n"
    "s_add_u32 m0, s80, 81920\n"
    "s_nop 0\n"
    "buffer_load_dwordx4 v148, s[36:39], s79 offen lds\n"
    "s_add_u32 s79, s79, 131072\n"
    "s_add_u32 m0, m0, 4096\n"
    "s_nop 0\n"
    "buffer_load_dwordx4 v148, s[36:39], s79 offen lds\n"
    "s_add_u32 s79, s79, 131072\n"
    "s_add_u32 m0, m0, 4096\n"
    "s_nop 0\n"
    "buffer_load_dwordx4 v148, s[36:39], s79 offen lds\n"
    "s_add_u32 s79, s79, 131072\n"
    "s_add_u32 m0, m0, 4096\n"
    "s_nop 0\n"
    "buffer_load_dwordx4 v148, s[36:39], s79 offen lds\n"
    "s_add_u32 s79, s79, 131072\n"
    "s_add_u32 m0, m0, 4096\n"
    "s_nop 0\n"
    "buffer_load_dwordx4 v148, s[36:39], s79 offen lds\n"
    "s_add_u32 s79, s79, 131072\n"
    "s_add_u32 m0, m0, 4096\n"
    "s_nop 0\n"
    "buffer_load_dwordx4 v148, s[36:39], s79 offen lds\n"
    "s_add_u32 s79, s79, 131072\n"
    "s_add_u32 m0, m0, 4096\n"
    "s_nop 0\n"
    "buffer_load_dwordx4 v148, s[36:39], s79 offen lds\n"
    "s_add_u32 s79, s79, 131072\n"
    "s_add_u32 m0, m0, 4096\n"
    "s_nop 0\n"
    "buffer_load_dwordx4 v148, s[36:39], s79 offen lds\n"
    "s_add_u32 s79, s79, 131072\n"
    "s_add_u32 m0, m0, 4096\n"
    "s_nop 0\n"
    "buffer_load_dwordx4 v148, s[36:39], s79 offen lds\n"
    "s_add_u32 s79, s79, 131072\n"
    "s_add_u32 m0, m0, 4096\n"
    "s_nop 0\n"
    "buffer_load_dwordx4 v148, s[36:39], s79 offen lds\n"
    "s_add_u32 s79, s79, 131072\n"
    "s_add_u32 m0, m0, 4096\n"
    "s_nop 0\n"
    "buffer_load_dwordx4 v148, s[36:39], s79 offen lds\n"
    "s_add_u32 s79, s79, 131072\n"
    "s_add_u32 m0, m0, 4096\n"
    "s_nop 0\n"
    "buffer_load_dwordx4 v148, s[36:39], s79 offen lds\n"
    "s_add_u32 s79, s79, 131072\n"
    "s_add_u32 m0, m0, 4096\n"
    "s_nop 0\n"
    "buffer_load_dwordx4 v148, s[36:39], s79 offen lds\n"
    "s_add_u32 s79, s79, 131072\n"
    "s_add_u32 m0, m0, 4096\n"
    "s_nop 0\n"
    "buffer_load_dwordx4 v148, s[36:39], s79 offen lds\n"
    "s_add_u32 s79, s79, 131072\n"
    "s_add_u32 m0, m0, 4096\n"
    "s_nop 0\n"
    "buffer_load_dwordx4 v148, s[36:39], s79 offen lds\n"
    "s_add_u32 s79, s79, 131072\n"
    "s_add_u32 m0, m0, 4096\n"
    "s_nop 0\n"
    "buffer_load_dwordx4 v148, s[36:39], s79 offen lds\n"
    "s_add_u32 s79, s79, 131072\n"
    "s_add_u32 m0, m0, 4096\n"
    "v_add_u32 v144, 0xfffe0000, v144\n"
    "v_add_u32 v145, 0xfffe0000, v145\n"
    "v_add_u32 v146, 0xfffe0000, v146\n"
    "v_add_u32 v147, 0xfffe0000, v147\n"
    "ds_read_b64_tr_b16 a[144:145], v144\n"
    "ds_read_b64_tr_b16 a[146:147], v145\n"
    "ds_read_b64_tr_b16 a[148:149], v144 offset:8192\n"
    "ds_read_b64_tr_b16 a[150:151], v145 offset:8192\n"
    "ds_read_b64_tr_b16 v[0:1], v146\n"
    "ds_read_b64_tr_b16 v[2:3], v147\n"
    "ds_read_b64_tr_b16 v[4:5], v146 offset:8192\n"
    "ds_read_b64_tr_b16 v[6:7], v147 offset:8192\n"
    "ds_read_b64_tr_b16 a[152:153], v144 offset:16384\n"
    "ds_read_b64_tr_b16 a[154:155], v145 offset:16384\n"
    "ds_read_b64_tr_b16 a[156:157], v144 offset:24576\n"
    "ds_read_b64_tr_b16 a[158:159], v145 offset:24576\n"
    "ds_read_b64_tr_b16 v[8:9], v146 offset:16384\n"
    "ds_read_b64_tr_b16 v[10:11], v147 offset:16384\n"
    "ds_read_b64_tr_b16 v[12:13], v146 offset:24576\n"
    "ds_read_b64_tr_b16 v[14:15], v147 offset:24576\n"
    "ds_read_b64_tr_b16 a[160:161], v144 offset:32768\n"
    "ds_read_b64_tr_b16 a[162:163], v145 offset:32768\n"
    "ds_read_b64_tr_b16 a[164:165], v144 offset:40960\n"
    "ds_read_b64_tr_b16 a[166:167], v145 offset:40960\n"
    "ds_read_b64_tr_b16 v[16:17], v146 offset:32768\n"
    "ds_read_b64_tr_b16 v[18:19], v147 offset:32768\n"
    "ds_read_b64_tr_b16 v[20:21], v146 offset:40960\n"
    "ds_read_b64_tr_b16 v[22:23], v147 offset:40960\n"
    "ds_read_b64_tr_b16 a[168:169], v144 offset:49152\n"
    "ds_read_b64_tr_b16 a[170:171], v145 offset:49152\n"
    "ds_read_b64_tr_b16 a[172:173], v144 offset:57344\n"
    "ds_read_b64_tr_b16 a[174:175], v145 offset:57344\n"
    "ds_read_b64_tr_b16 v[24:25], v146 offset:49152\n"
    "ds_read_b64_tr_b16 v[26:27], v147 offset:49152\n"
    "ds_read_b64_tr_b16 v[28:29], v146 offset:57344\n"
    "ds_read_b64_tr_b16 v[30:31], v147 offset:57344\n"
    "v_add_u32 v144, 0x10000, v144\n"
    "v_add_u32 v145, 0x10000, v145\n"
    "v_add_u32 v146, 0x10000, v146\n"
    "v_add_u32 v147, 0x10000, v147\n"
    "ds_read_b64_tr_b16 a[176:177], v144\n"
    "ds_read_b64_tr_b16 a[178:179], v145\n"
    "ds_read_b64_tr_b16 a[180:181], v144 offset:8192\n"
    "ds_read_b64_tr_b16 a[182:183], v145 offset:8192\n"
    "ds_read_b64_tr_b16 v[32:33], v146\n"
    "ds_read_b64_tr_b16 v[34:35], v147\n"
    "ds_read_b64_tr_b16 v[36:37], v146 offset:8192\n"
    "ds_read_b64_tr_b16 v[38:39], v147 offset:8192\n"
    "s_waitcnt vmcnt(0)\n"
    "s_waitcnt lgkmcnt(0)\n"
    "s_barrier\n"
    "s_add_u32 s36, s48, 0\n"
    "s_addc_u32 s37, s49, 0\n"
    "s_and_b32 s37, s37, 0xffff\n"
    "s_mov_b32 s38, 0x480000\n"
    "s_mov_b32 s39, 0x20000\n"
    "s_lshr_b32 s79, s68, 1\n"
    "s_add_u32 s79, s79, 0\n"
    "s_lshl_b32 s79, s79, 16\n"
    "s_and_b32 s80, s68, 1\n"
    "s_lshl_b32 s80, s80, 7\n"
    "s_add_u32 s79, s79, s80\n"
    "s_lshl_b32 s80, s64, 8\n"
    "s_add_u32 s79, s79, s80\n"
    "s_lshl_b32 s80, s68, 10\n"
    "s_add_u32 m0, s80, 0\n"
    "s_nop 0\n"
    "buffer_load_dwordx4 v148, s[36:39], s79 offen lds\n"
    "s_add_u32 s79, s79, 131072\n"
    "s_add_u32 m0, m0, 4096\n"
    "s_nop 0\n"
    "buffer_load_dwordx4 v148, s[36:39], s79 offen lds\n"
    "s_add_u32 s79, s79, 131072\n"
    "s_add_u32 m0, m0, 4096\n"
    "s_nop 0\n"
    "buffer_load_dwordx4 v148, s[36:39], s79 offen lds\n"
    "s_add_u32 s79, s79, 131072\n"
    "s_add_u32 m0, m0, 4096\n"
    "s_nop 0\n"
    "buffer_load_dwordx4 v148, s[36:39], s79 offen lds\n"
    "s_add_u32 s79, s79, 131072\n"
    "s_add_u32 m0, m0, 4096\n"
    "s_nop 0\n"
    "buffer_load_dwordx4 v148, s[36:39], s79 offen lds\n"
    "s_add_u32 s79, s79, 131072\n"
    "s_add_u32 m0, m0, 4096\n"
    "s_nop 0\n"
    "buffer_load_dwordx4 v148, s[36:39], s79 offen lds\n"
    "s_add_u32 s79, s79, 131072\n"
    "s_add_u32 m0, m0, 4096\n"
    "s_nop 0\n"
    "buffer_load_dwordx4 v148, s[36:39], s79 offen lds\n"
    "s_add_u32 s79, s79, 131072\n"
    "s_add_u32 m0, m0, 4096\n"
    "s_nop 0\n"
    "buffer_load_dwordx4 v148, s[36:39], s79 offen lds\n"
    "s_add_u32 s79, s79, 131072\n"
    "s_add_u32 m0, m0, 4096\n"
    "s_nop 0\n"
    "buffer_load_dwordx4 v148, s[36:39], s79 offen lds\n"
    "s_add_u32 s79, s79, 131072\n"
    "s_add_u32 m0, m0, 4096\n"
    "s_nop 0\n"
    "buffer_load_dwordx4 v148, s[36:39], s79 offen lds\n"
    "s_add_u32 s79, s79, 131072\n"
    "s_add_u32 m0, m0, 4096\n"
    "s_nop 0\n"
    "buffer_load_dwordx4 v148, s[36:39], s79 offen lds\n"
    "s_add_u32 s79, s79, 131072\n"
    "s_add_u32 m0, m0, 4096\n"
    "s_nop 0\n"
    "buffer_load_dwordx4 v148, s[36:39], s79 offen lds\n"
    "s_add_u32 s79, s79, 131072\n"
    "s_add_u32 m0, m0, 4096\n"
    "s_nop 0\n"
    "buffer_load_dwordx4 v148, s[36:39], s79 offen lds\n"
    "s_add_u32 s79, s79, 131072\n"
    "s_add_u32 m0, m0, 4096\n"
    "s_nop 0\n"
    "buffer_load_dwordx4 v148, s[36:39], s79 offen lds\n"
    "s_add_u32 s79, s79, 131072\n"
    "s_add_u32 m0, m0, 4096\n"
    "s_nop 0\n"
    "buffer_load_dwordx4 v148, s[36:39], s79 offen lds\n"
    "s_add_u32 s79, s79, 131072\n"
    "s_add_u32 m0, m0, 4096\n"
    "s_nop 0\n"
    "buffer_load_dwordx4 v148, s[36:39], s79 offen lds\n"
    "s_add_u32 s79, s79, 131072\n"
    "s_add_u32 m0, m0, 4096\n"
    "s_nop 0\n"
    "buffer_load_dwordx4 v148, s[36:39], s79 offen lds\n"
    "s_add_u32 s79, s79, 131072\n"
    "s_add_u32 m0, m0, 4096\n"
    "s_nop 0\n"
    "buffer_load_dwordx4 v148, s[36:39], s79 offen lds\n"
    "s_add_u32 s79, s79, 131072\n"
    "s_add_u32 m0, m0, 4096\n"
    "s_nop 0\n"
    "buffer_load_dwordx4 v148, s[36:39], s79 offen lds\n"
    "s_add_u32 s79, s79, 131072\n"
    "s_add_u32 m0, m0, 4096\n"
    "s_nop 0\n"
    "buffer_load_dwordx4 v148, s[36:39], s79 offen lds\n"
    "s_add_u32 s79, s79, 131072\n"
    "s_add_u32 m0, m0, 4096\n"
    "ds_read_b64_tr_b16 a[184:185], v144 offset:16384\n"
    "ds_read_b64_tr_b16 a[186:187], v145 offset:16384\n"
    "ds_read_b64_tr_b16 a[188:189], v144 offset:24576\n"
    "ds_read_b64_tr_b16 a[190:191], v145 offset:24576\n"
    "ds_read_b64_tr_b16 v[40:41], v146 offset:16384\n"
    "ds_read_b64_tr_b16 v[42:43], v147 offset:16384\n"
    "ds_read_b64_tr_b16 v[44:45], v146 offset:24576\n"
    "ds_read_b64_tr_b16 v[46:47], v147 offset:24576\n"
    "ds_read_b64_tr_b16 a[192:193], v144 offset:32768\n"
    "ds_read_b64_tr_b16 a[194:195], v145 offset:32768\n"
    "ds_read_b64_tr_b16 a[196:197], v144 offset:40960\n"
    "ds_read_b64_tr_b16 a[198:199], v145 offset:40960\n"
    "ds_read_b64_tr_b16 v[48:49], v146 offset:32768\n"
    "ds_read_b64_tr_b16 v[50:51], v147 offset:32768\n"
    "ds_read_b64_tr_b16 v[52:53], v146 offset:40960\n"
    "ds_read_b64_tr_b16 v[54:55], v147 offset:40960\n"
    "ds_read_b64_tr_b16 a[200:201], v144 offset:49152\n"
    "ds_read_b64_tr_b16 a[202:203], v145 offset:49152\n"
    "ds_read_b64_tr_b16 a[204:205], v144 offset:57344\n"
    "ds_read_b64_tr_b16 a[206:207], v145 offset:57344\n"
    "ds_read_b64_tr_b16 v[56:57], v146 offset:49152\n"
    "ds_read_b64_tr_b16 v[58:59], v147 offset:49152\n"
    "ds_read_b64_tr_b16 v[60:61], v146 offset:57344\n"
    "ds_read_b64_tr_b16 v[62:63], v147 offset:57344\n"
    "v_add_u32 v144, 0x10000, v144\n"
    "v_add_u32 v145, 0x10000, v145\n"
    "v_add_u32 v146, 0x10000, v146\n"
    "v_add_u32 v147, 0x10000, v147\n"
    "ds_read_b64_tr_b16 a[208:209], v144\n"
    "ds_read_b64_tr_b16 a[210:211], v145\n"
    "ds_read_b64_tr_b16 a[212:213], v144 offset:8192\n"
    "ds_read_b64_tr_b16 a[214:215], v145 offset:8192\n"
    "ds_read_b64_tr_b16 v[64:65], v146\n"
    "ds_read_b64_tr_b16 v[66:67], v147\n"
    "ds_read_b64_tr_b16 v[68:69], v146 offset:8192\n"
    "ds_read_b64_tr_b16 v[70:71], v147 offset:8192\n"
    "s_waitcnt vmcnt(0)\n"
    "s_waitcnt lgkmcnt(0)\n"
    "s_barrier\n"
    "s_add_u32 s36, s48, 0\n"
    "s_addc_u32 s37, s49, 0\n"
    "s_and_b32 s37, s37, 0xffff\n"
    "s_mov_b32 s38, 0x480000\n"
    "s_mov_b32 s39, 0x20000\n"
    "s_lshr_b32 s79, s68, 1\n"
    "s_add_u32 s79, s79, 40\n"
    "s_lshl_b32 s79, s79, 16\n"
    "s_and_b32 s80, s68, 1\n"
    "s_lshl_b32 s80, s80, 7\n"
    "s_add_u32 s79, s79, s80\n"
    "s_lshl_b32 s80, s64, 8\n"
    "s_add_u32 s79, s79, s80\n"
    "s_lshl_b32 s80, s68, 10\n"
    "s_add_u32 m0, s80, 81920\n"
    "s_nop 0\n"
    "buffer_load_dwordx4 v148, s[36:39], s79 offen lds\n"
    "s_add_u32 s79, s79, 131072\n"
    "s_add_u32 m0, m0, 4096\n"
    "s_nop 0\n"
    "buffer_load_dwordx4 v148, s[36:39], s79 offen lds\n"
    "s_add_u32 s79, s79, 131072\n"
    "s_add_u32 m0, m0, 4096\n"
    "s_nop 0\n"
    "buffer_load_dwordx4 v148, s[36:39], s79 offen lds\n"
    "s_add_u32 s79, s79, 131072\n"
    "s_add_u32 m0, m0, 4096\n"
    "s_nop 0\n"
    "buffer_load_dwordx4 v148, s[36:39], s79 offen lds\n"
    "s_add_u32 s79, s79, 131072\n"
    "s_add_u32 m0, m0, 4096\n"
    "s_nop 0\n"
    "buffer_load_dwordx4 v148, s[36:39], s79 offen lds\n"
    "s_add_u32 s79, s79, 131072\n"
    "s_add_u32 m0, m0, 4096\n"
    "s_nop 0\n"
    "buffer_load_dwordx4 v148, s[36:39], s79 offen lds\n"
    "s_add_u32 s79, s79, 131072\n"
    "s_add_u32 m0, m0, 4096\n"
    "s_nop 0\n"
    "buffer_load_dwordx4 v148, s[36:39], s79 offen lds\n"
    "s_add_u32 s79, s79, 131072\n"
    "s_add_u32 m0, m0, 4096\n"
    "s_nop 0\n"
    "buffer_load_dwordx4 v148, s[36:39], s79 offen lds\n"
    "s_add_u32 s79, s79, 131072\n"
    "s_add_u32 m0, m0, 4096\n"
    "s_nop 0\n"
    "buffer_load_dwordx4 v148, s[36:39], s79 offen lds\n"
    "s_add_u32 s79, s79, 131072\n"
    "s_add_u32 m0, m0, 4096\n"
    "s_nop 0\n"
    "buffer_load_dwordx4 v148, s[36:39], s79 offen lds\n"
    "s_add_u32 s79, s79, 131072\n"
    "s_add_u32 m0, m0, 4096\n"
    "s_nop 0\n"
    "buffer_load_dwordx4 v148, s[36:39], s79 offen lds\n"
    "s_add_u32 s79, s79, 131072\n"
    "s_add_u32 m0, m0, 4096\n"
    "s_nop 0\n"
    "buffer_load_dwordx4 v148, s[36:39], s79 offen lds\n"
    "s_add_u32 s79, s79, 131072\n"
    "s_add_u32 m0, m0, 4096\n"
    "s_nop 0\n"
    "buffer_load_dwordx4 v148, s[36:39], s79 offen lds\n"
    "s_add_u32 s79, s79, 131072\n"
    "s_add_u32 m0, m0, 4096\n"
    "s_nop 0\n"
    "buffer_load_dwordx4 v148, s[36:39], s79 offen lds\n"
    "s_add_u32 s79, s79, 131072\n"
    "s_add_u32 m0, m0, 4096\n"
    "s_nop 0\n"
    "buffer_load_dwordx4 v148, s[36:39], s79 offen lds\n"
    "s_add_u32 s79, s79, 131072\n"
    "s_add_u32 m0, m0, 4096\n"
    "s_nop 0\n"
    "buffer_load_dwordx4 v148, s[36:39], s79 offen lds\n"
    "s_add_u32 s79, s79, 131072\n"
    "s_add_u32 m0, m0, 4096\n"
    "v_add_u32 v144, 0xfffe0000, v144\n"
    "v_add_u32 v145, 0xfffe0000, v145\n"
    "v_add_u32 v146, 0xfffe0000, v146\n"
    "v_add_u32 v147, 0xfffe0000, v147\n"
    "ds_read_b64_tr_b16 a[216:217], v144\n"
    "ds_read_b64_tr_b16 a[218:219], v145\n"
    "ds_read_b64_tr_b16 a[220:221], v144 offset:8192\n"
    "ds_read_b64_tr_b16 a[222:223], v145 offset:8192\n"
    "ds_read_b64_tr_b16 v[72:73], v146\n"
    "ds_read_b64_tr_b16 v[74:75], v147\n"
    "ds_read_b64_tr_b16 v[76:77], v146 offset:8192\n"
    "ds_read_b64_tr_b16 v[78:79], v147 offset:8192\n"
    "ds_read_b64_tr_b16 a[224:225], v144 offset:16384\n"
    "ds_read_b64_tr_b16 a[226:227], v145 offset:16384\n"
    "ds_read_b64_tr_b16 a[228:229], v144 offset:24576\n"
    "ds_read_b64_tr_b16 a[230:231], v145 offset:24576\n"
    "ds_read_b64_tr_b16 v[80:81], v146 offset:16384\n"
    "ds_read_b64_tr_b16 v[82:83], v147 offset:16384\n"
    "ds_read_b64_tr_b16 v[84:85], v146 offset:24576\n"
    "ds_read_b64_tr_b16 v[86:87], v147 offset:24576\n"
    "ds_read_b64_tr_b16 a[232:233], v144 offset:32768\n"
    "ds_read_b64_tr_b16 a[234:235], v145 offset:32768\n"
    "ds_read_b64_tr_b16 a[236:237], v144 offset:40960\n"
    "ds_read_b64_tr_b16 a[238:239], v145 offset:40960\n"
    "ds_read_b64_tr_b16 v[88:89], v146 offset:32768\n"
    "ds_read_b64_tr_b16 v[90:91], v147 offset:32768\n"
    "ds_read_b64_tr_b16 v[92:93], v146 offset:40960\n"
    "ds_read_b64_tr_b16 v[94:95], v147 offset:40960\n"
    "ds_read_b64_tr_b16 a[240:241], v144 offset:49152\n"
    "ds_read_b64_tr_b16 a[242:243], v145 offset:49152\n"
    "ds_read_b64_tr_b16 a[244:245], v144 offset:57344\n"
    "ds_read_b64_tr_b16 a[246:247], v145 offset:57344\n"
    "ds_read_b64_tr_b16 v[96:97], v146 offset:49152\n"
    "ds_read_b64_tr_b16 v[98:99], v147 offset:49152\n"
    "ds_read_b64_tr_b16 v[100:101], v146 offset:57344\n"
    "ds_read_b64_tr_b16 v[102:103], v147 offset:57344\n"
    "v_add_u32 v144, 0x10000, v144\n"
    "v_add_u32 v145, 0x10000, v145\n"
    "v_add_u32 v146, 0x10000, v146\n"
    "v_add_u32 v147, 0x10000, v147\n"
    "ds_read_b64_tr_b16 a[248:249], v144\n"
    "ds_read_b64_tr_b16 a[250:251], v145\n"
    "ds_read_b64_tr_b16 a[252:253], v144 offset:8192\n"
    "ds_read_b64_tr_b16 a[254:255], v145 offset:8192\n"
    "ds_read_b64_tr_b16 v[104:105], v146\n"
    "ds_read_b64_tr_b16 v[106:107], v147\n"
    "ds_read_b64_tr_b16 v[108:109], v146 offset:8192\n"
    "ds_read_b64_tr_b16 v[110:111], v147 offset:8192\n"
    "s_waitcnt vmcnt(0)\n"
    "s_waitcnt lgkmcnt(0)\n"
    "s_barrier\n"
    "s_add_u32 s36, s50, 0\n"
    "s_addc_u32 s37, s51, 0\n"
    "s_and_b32 s37, s37, 0xffff\n"
    "s_mov_b32 s38, 0x480000\n"
    "s_mov_b32 s39, 0x20000\n"
    "s_mul_i32 s79, s64, 147456\n"
    "s_lshl_b32 s80, s65, 9\n"
    "s_add_u32 s79, s79, s80\n"
    "s_mul_i32 s80, s68, 9216\n"
    "s_add_u32 s79, s79, s80\n"
    "s_mul_i32 s80, s68, 5120\n"
    "s_add_u32 m0, s80, 0\n"
    "s_nop 0\n"
    "buffer_load_dwordx4 v149, s[36:39], s79 offen lds\n"
    "s_add_u32 s79, s79, 128\n"
    "s_add_u32 m0, m0, 1024\n"
    "s_nop 0\n"
    "buffer_load_dwordx4 v149, s[36:39], s79 offen lds\n"
    "s_add_u32 s79, s79, 128\n"
    "s_add_u32 m0, m0, 1024\n"
    "s_nop 0\n"
    "buffer_load_dwordx4 v149, s[36:39], s79 offen lds\n"
    "s_add_u32 s79, s79, 128\n"
    "s_add_u32 m0, m0, 1024\n"
    "s_nop 0\n"
    "buffer_load_dwordx4 v149, s[36:39], s79 offen lds\n"
    "s_add_u32 s79, s79, 128\n"
    "s_add_u32 m0, m0, 1024\n"
    "s_nop 0\n"
    "buffer_load_dwordx4 v149, s[36:39], s79 offen lds\n"
    "s_add_u32 s79, s79, 36352\n"
    "s_add_u32 m0, m0, 16384\n"
    "s_nop 0\n"
    "buffer_load_dwordx4 v149, s[36:39], s79 offen lds\n"
    "s_add_u32 s79, s79, 128\n"
    "s_add_u32 m0, m0, 1024\n"
    "s_nop 0\n"
    "buffer_load_dwordx4 v149, s[36:39], s79 offen lds\n"
    "s_add_u32 s79, s79, 128\n"
    "s_add_u32 m0, m0, 1024\n"
    "s_nop 0\n"
    "buffer_load_dwordx4 v149, s[36:39], s79 offen lds\n"
    "s_add_u32 s79, s79, 128\n"
    "s_add_u32 m0, m0, 1024\n"
    "s_nop 0\n"
    "buffer_load_dwordx4 v149, s[36:39], s79 offen lds\n"
    "s_add_u32 s79, s79, 128\n"
    "s_add_u32 m0, m0, 1024\n"
    "s_nop 0\n"
    "buffer_load_dwordx4 v149, s[36:39], s79 offen lds\n"
    "s_add_u32 s79, s79, 36352\n"
    "s_add_u32 m0, m0, 16384\n"
    "s_nop 0\n"
    "buffer_load_dwordx4 v149, s[36:39], s79 offen lds\n"
    "s_add_u32 s79, s79, 128\n"
    "s_add_u32 m0, m0, 1024\n"
    "s_nop 0\n"
    "buffer_load_dwordx4 v149, s[36:39], s79 offen lds\n"
    "s_add_u32 s79, s79, 128\n"
    "s_add_u32 m0, m0, 1024\n"
    "s_nop 0\n"
    "buffer_load_dwordx4 v149, s[36:39], s79 offen lds\n"
    "s_add_u32 s79, s79, 128\n"
    "s_add_u32 m0, m0, 1024\n"
    "s_nop 0\n"
    "buffer_load_dwordx4 v149, s[36:39], s79 offen lds\n"
    "s_add_u32 s79, s79, 128\n"
    "s_add_u32 m0, m0, 1024\n"
    "s_nop 0\n"
    "buffer_load_dwordx4 v149, s[36:39], s79 offen lds\n"
    "s_add_u32 s79, s79, 36352\n"
    "s_add_u32 m0, m0, 16384\n"
    "s_nop 0\n"
    "buffer_load_dwordx4 v149, s[36:39], s79 offen lds\n"
    "s_add_u32 s79, s79, 128\n"
    "s_add_u32 m0, m0, 1024\n"
    "s_nop 0\n"
    "buffer_load_dwordx4 v149, s[36:39], s79 offen lds\n"
    "s_add_u32 s79, s79, 128\n"
    "s_add_u32 m0, m0, 1024\n"
    "s_nop 0\n"
    "buffer_load_dwordx4 v149, s[36:39], s79 offen lds\n"
    "s_add_u32 s79, s79, 128\n"
    "s_add_u32 m0, m0, 1024\n"
    "s_nop 0\n"
    "buffer_load_dwordx4 v149, s[36:39], s79 offen lds\n"
    "s_add_u32 s79, s79, 128\n"
    "s_add_u32 m0, m0, 1024\n"
    "s_nop 0\n"
    "buffer_load_dwordx4 v149, s[36:39], s79 offen lds\n"
    "s_add_u32 s79, s79, 36352\n"
    "s_add_u32 m0, m0, 16384\n"
    "ds_read_b64_tr_b16 v[160:161], v144 offset:16384\n"
    "ds_read_b64_tr_b16 v[162:163], v145 offset:16384\n"
    "ds_read_b64_tr_b16 v[164:165], v144 offset:24576\n"
    "ds_read_b64_tr_b16 v[166:167], v145 offset:24576\n"
    "ds_read_b64_tr_b16 v[112:113], v146 offset:16384\n"
    "ds_read_b64_tr_b16 v[114:115], v147 offset:16384\n"
    "ds_read_b64_tr_b16 v[116:117], v146 offset:24576\n"
    "ds_read_b64_tr_b16 v[118:119], v147 offset:24576\n"
    "ds_read_b64_tr_b16 v[168:169], v144 offset:32768\n"
    "ds_read_b64_tr_b16 v[170:171], v145 offset:32768\n"
    "ds_read_b64_tr_b16 v[172:173], v144 offset:40960\n"
    "ds_read_b64_tr_b16 v[174:175], v145 offset:40960\n"
    "ds_read_b64_tr_b16 v[120:121], v146 offset:32768\n"
    "ds_read_b64_tr_b16 v[122:123], v147 offset:32768\n"
    "ds_read_b64_tr_b16 v[124:125], v146 offset:40960\n"
    "ds_read_b64_tr_b16 v[126:127], v147 offset:40960\n"
    "ds_read_b64_tr_b16 v[176:177], v144 offset:49152\n"
    "ds_read_b64_tr_b16 v[178:179], v145 offset:49152\n"
    "ds_read_b64_tr_b16 v[180:181], v144 offset:57344\n"
    "ds_read_b64_tr_b16 v[182:183], v145 offset:57344\n"
    "ds_read_b64_tr_b16 v[128:129], v146 offset:49152\n"
    "ds_read_b64_tr_b16 v[130:131], v147 offset:49152\n"
    "ds_read_b64_tr_b16 v[132:133], v146 offset:57344\n"
    "ds_read_b64_tr_b16 v[134:135], v147 offset:57344\n"
    "v_add_u32 v144, 0x10000, v144\n"
    "v_add_u32 v145, 0x10000, v145\n"
    "v_add_u32 v146, 0x10000, v146\n"
    "v_add_u32 v147, 0x10000, v147\n"
    "ds_read_b64_tr_b16 v[184:185], v144\n"
    "ds_read_b64_tr_b16 v[186:187], v145\n"
    "ds_read_b64_tr_b16 v[188:189], v144 offset:8192\n"
    "ds_read_b64_tr_b16 v[190:191], v145 offset:8192\n"
    "ds_read_b64_tr_b16 v[136:137], v146\n"
    "ds_read_b64_tr_b16 v[138:139], v147\n"
    "ds_read_b64_tr_b16 v[140:141], v146 offset:8192\n"
    "ds_read_b64_tr_b16 v[142:143], v147 offset:8192\n"
    "s_waitcnt vmcnt(0)\n"
    "s_waitcnt lgkmcnt(0)\n"
    "s_barrier\n"
    "v_bfe_u32 v157, v150, 4, 1\n"
    "v_bfe_u32 v158, v150, 3, 1\n"
    "v_xor_b32 v151, v157, v158\n"
    "v_lshlrev_b32 v151, 6, v151\n"
    "v_lshl_or_b32 v153, v158, 5, v151\n"
    "v_xor_b32 v154, 32, v153\n"
    "v_lshrrev_b32 v151, 5, v150\n"
    "v_lshl_or_b32 v151, v151, 1, v158\n"
    "s_mov_b32 s79, 5120\n"
    "v_mul_u32_u24 v155, s79, v151\n"
    "v_bfe_u32 v151, v150, 2, 1\n"
    "v_lshl_or_b32 v151, v157, 2, v151\n"
    "v_lshl_add_u32 v155, v151, 7, v155\n"
    "v_and_b32 v151, 3, v150\n"
    "v_lshl_add_u32 v155, v151, 3, v155\n"
    "v_add_u32 v156, 256, v155\n"
    "s_mul_i32 s79, s68, 5\n"
    "s_sub_u32 s79, s79, 2\n"
    "s_lshl_b32 s81, s68, 2\n"
    "s_cmp_lt_u32 s68, 2\n"
    "s_cselect_b32 s79, s81, s79\n"
    "s_mul_i32 s81, s65, 18\n"
    "s_add_u32 s92, s79, s81\n"
    "s_lshl_b32 s81, s65, 1\n"
    "s_add_u32 s79, s79, s81\n"
    "s_sub_u32 s83, s68, 2\n"
    "s_lshl_b32 s83, s83, 12\n"
    "s_add_u32 s83, s83, 154112\n"
    "s_and_b32 s80, s79, 3\n"
    "s_lshl_b32 s80, s80, 5\n"
    "v_xor_b32 v144, s80, v153\n"
    "s_lshr_b32 s80, s79, 2\n"
    "s_lshl_b32 s80, s80, 10\n"
    "s_add_u32 s80, s80, 0\n"
    "v_add3_u32 v144, v144, v155, s80\n"
    "s_and_b32 s80, s79, 3\n"
    "s_lshl_b32 s80, s80, 5\n"
    "v_xor_b32 v145, s80, v154\n"
    "s_lshr_b32 s80, s79, 2\n"
    "s_lshl_b32 s80, s80, 10\n"
    "s_add_u32 s80, s80, 0\n"
    "v_add3_u32 v145, v145, v156, s80\n"
    "ds_read_b64_tr_b16 v[192:193], v144\n"
    "ds_read_b64_tr_b16 v[194:195], v145\n"
    "ds_read_b64_tr_b16 v[196:197], v144 offset:20480\n"
    "ds_read_b64_tr_b16 v[198:199], v145 offset:20480\n"
    "ds_read_b64_tr_b16 v[200:201], v144 offset:40960\n"
    "ds_read_b64_tr_b16 v[202:203], v145 offset:40960\n"
    "ds_read_b64_tr_b16 v[204:205], v144 offset:61440\n"
    "ds_read_b64_tr_b16 v[206:207], v145 offset:61440\n"
    "s_add_u32 s79, s79, 1\n"
    "s_and_b32 s80, s79, 3\n"
    "s_lshl_b32 s80, s80, 5\n"
    "v_xor_b32 v144, s80, v153\n"
    "s_lshr_b32 s80, s79, 2\n"
    "s_lshl_b32 s80, s80, 10\n"
    "s_add_u32 s80, s80, 0\n"
    "v_add3_u32 v144, v144, v155, s80\n"
    "s_and_b32 s80, s79, 3\n"
    "s_lshl_b32 s80, s80, 5\n"
    "v_xor_b32 v145, s80, v154\n"
    "s_lshr_b32 s80, s79, 2\n"
    "s_lshl_b32 s80, s80, 10\n"
    "s_add_u32 s80, s80, 0\n"
    "v_add3_u32 v145, v145, v156, s80\n"
    "ds_read_b64_tr_b16 v[208:209], v144\n"
    "ds_read_b64_tr_b16 v[210:211], v145\n"
    "ds_read_b64_tr_b16 v[212:213], v144 offset:20480\n"
    "ds_read_b64_tr_b16 v[214:215], v145 offset:20480\n"
    "ds_read_b64_tr_b16 v[216:217], v144 offset:40960\n"
    "ds_read_b64_tr_b16 v[218:219], v145 offset:40960\n"
    "ds_read_b64_tr_b16 v[220:221], v144 offset:61440\n"
    "ds_read_b64_tr_b16 v[222:223], v145 offset:61440\n"
    "s_add_u32 s79, s79, 1\n"
    "s_and_b32 s80, s79, 3\n"
    "s_lshl_b32 s80, s80, 5\n"
    "v_xor_b32 v144, s80, v153\n"
    "s_lshr_b32 s80, s79, 2\n"
    "s_lshl_b32 s80, s80, 10\n"
    "s_add_u32 s80, s80, 0\n"
    "v_add3_u32 v144, v144, v155, s80\n"
    "s_and_b32 s80, s79, 3\n"
    "s_lshl_b32 s80, s80, 5\n"
    "v_xor_b32 v145, s80, v154\n"
    "s_lshr_b32 s80, s79, 2\n"
    "s_lshl_b32 s80, s80, 10\n"
    "s_add_u32 s80, s80, 0\n"
    "v_add3_u32 v145, v145, v156, s80\n"
    "ds_read_b64_tr_b16 v[224:225], v144\n"
    "ds_read_b64_tr_b16 v[226:227], v145\n"
    "ds_read_b64_tr_b16 v[228:229], v144 offset:20480\n"
    "ds_read_b64_tr_b16 v[230:231], v145 offset:20480\n"
    "ds_read_b64_tr_b16 v[232:233], v144 offset:40960\n"
    "ds_read_b64_tr_b16 v[234:235], v145 offset:40960\n"
    "ds_read_b64_tr_b16 v[236:237], v144 offset:61440\n"
    "ds_read_b64_tr_b16 v[238:239], v145 offset:61440\n"
    "s_add_u32 s79, s79, 1\n"
    "s_and_b32 s80, s79, 3\n"
    "s_lshl_b32 s80, s80, 5\n"
    "v_xor_b32 v144, s80, v153\n"
    "s_lshr_b32 s80, s79, 2\n"
    "s_lshl_b32 s80, s80, 10\n"
    "s_add_u32 s80, s80, 0\n"
    "v_add3_u32 v144, v144, v155, s80\n"
    "s_and_b32 s80, s79, 3\n"
    "s_lshl_b32 s80, s80, 5\n"
    "v_xor_b32 v145, s80, v154\n"
    "s_lshr_b32 s80, s79, 2\n"
    "s_lshl_b32 s80, s80, 10\n"
    "s_add_u32 s80, s80, 0\n"
    "v_add3_u32 v145, v145, v156, s80\n"
    "ds_read_b64_tr_b16 v[240:241], v144\n"
    "ds_read_b64_tr_b16 v[242:243], v145\n"
    "ds_read_b64_tr_b16 v[244:245], v144 offset:20480\n"
    "ds_read_b64_tr_b16 v[246:247], v145 offset:20480\n"
    "ds_read_b64_tr_b16 v[248:249], v144 offset:40960\n"
    "ds_read_b64_tr_b16 v[250:251], v145 offset:40960\n"
    "ds_read_b64_tr_b16 v[252:253], v144 offset:61440\n"
    "ds_read_b64_tr_b16 v[254:255], v145 offset:61440\n"
    "s_add_u32 s79, s79, 1\n"
    "s_cmp_lt_u32 s68, 2\n"
    "s_cbranch_scc1 ch_no5_%=\n"
    "s_and_b32 s80, s79, 3\n"
    "s_lshl_b32 s80, s80, 5\n"
    "v_xor_b32 v144, s80, v153\n"
    "s_lshr_b32 s80, s79, 2\n"
    "s_lshl_b32 s80, s80, 10\n"
    "s_add_u32 s80, s80, 0\n"
    "v_add3_u32 v144, v144, v155, s80\n"
    "s_and_b32 s80, s79, 3\n"
    "s_lshl_b32 s80, s80, 5\n"
    "v_xor_b32 v145, s80, v154\n"
    "s_lshr_b32 s80, s79, 2\n"
    "s_lshl_b32 s80, s80, 10\n"
    "s_add_u32 s80, s80, 0\n"
    "v_add3_u32 v145, v145, v156, s80\n"
    "v_lshlrev_b32 v146, 4, v150\n"
    "v_add_u32 v146, s83, v146\n"
    "ds_read_b64_tr_b16 v[152:153], v144\n"
    "ds_read_b64_tr_b16 v[154:155], v145\n"
    "ds_read_b64_tr_b16 v[156:157], v144 offset:20480\n"
    "ds_read_b64_tr_b16 v[158:159], v145 offset:20480\n"
    "s_waitcnt lgkmcnt(0)\n"
    "ds_write_b128 v146, v[152:155]\n"
    "ds_write_b128 v146, v[156:159] offset:1024\n"
    "s_waitcnt lgkmcnt(0)\n"
    "ds_read_b64_tr_b16 v[152:153], v144 offset:40960\n"
    "ds_read_b64_tr_b16 v[154:155], v145 offset:40960\n"
    "ds_read_b64_tr_b16 v[156:157], v144 offset:61440\n"
    "ds_read_b64_tr_b16 v[158:159], v145 offset:61440\n"
    "s_waitcnt lgkmcnt(0)\n"
    "ds_write_b128 v146, v[152:155] offset:2048\n"
    "ds_write_b128 v146, v[156:159] offset:3072\n"
    "s_waitcnt lgkmcnt(0)\n"
    "ch_no5_%=:\n"
    "s_waitcnt lgkmcnt(0)\n"
    "s_barrier\n"
    "s_mul_i32 s79, s68, 36864\n"
    "v_lshlrev_b32 v144, 4, v150\n"
    "v_add_u32 v144, s79, v144\n"
    "ds_write_b128 v144, v[0:3]\n"
    "ds_write_b128 v144, v[72:75] offset:18432\n"
    "ds_write_b128 v144, v[4:7] offset:1024\n"
    "ds_write_b128 v144, v[76:79] offset:19456\n"
    "ds_write_b128 v144, v[8:11] offset:2048\n"
    "ds_write_b128 v144, v[80:83] offset:20480\n"
    "ds_write_b128 v144, v[12:15] offset:3072\n"
    "ds_write_b128 v144, v[84:87] offset:21504\n"
    "ds_write_b128 v144, v[16:19] offset:4096\n"
    "ds_write_b128 v144, v[88:91] offset:22528\n"
    "ds_write_b128 v144, v[20:23] offset:5120\n"
    "ds_write_b128 v144, v[92:95] offset:23552\n"
    "ds_write_b128 v144, v[24:27] offset:6144\n"
    "ds_write_b128 v144, v[96:99] offset:24576\n"
    "ds_write_b128 v144, v[28:31] offset:7168\n"
    "ds_write_b128 v144, v[100:103] offset:25600\n"
    "ds_write_b128 v144, v[32:35] offset:8192\n"
    "ds_write_b128 v144, v[104:107] offset:26624\n"
    "ds_write_b128 v144, v[36:39] offset:9216\n"
    "ds_write_b128 v144, v[108:111] offset:27648\n"
    "ds_write_b128 v144, v[40:43] offset:10240\n"
    "ds_write_b128 v144, v[112:115] offset:28672\n"
    "ds_write_b128 v144, v[44:47] offset:11264\n"
    "ds_write_b128 v144, v[116:119] offset:29696\n"
    "ds_write_b128 v144, v[48:51] offset:12288\n"
    "ds_write_b128 v144, v[120:123] offset:30720\n"
    "ds_write_b128 v144, v[52:55] offset:13312\n"
    "ds_write_b128 v144, v[124:127] offset:31744\n"
    "ds_write_b128 v144, v[56:59] offset:14336\n"
    "ds_write_b128 v144, v[128:131] offset:32768\n"
    "ds_write_b128 v144, v[60:63] offset:15360\n"
    "ds_write_b128 v144, v[132:135] offset:33792\n"
    "ds_write_b128 v144, v[64:67] offset:16384\n"
    "ds_write_b128 v144, v[136:139] offset:34816\n"
    "ds_write_b128 v144, v[68:71] offset:17408\n"
    "ds_write_b128 v144, v[140:143] offset:35840\n"
    "s_waitcnt lgkmcnt(0)\n"
    "v_mbcnt_lo_u32_b32 v0, -1, 0\n"
    "v_mbcnt_hi_u32_b32 v0, -1, v0\n"
    "v_mov_b32 v1, 0x200000\n"
    "buffer_load_dword v2, v1, s[20:23], 0 offen sc1\n"
    "s_waitcnt vmcnt(0)\n"
    "v_readfirstlane_b32 s70, v2\n"
    "s_and_b32 s70, s70, 1\n"
    "s_lshl_b32 s70, s70, 13\n"
    "v_lshlrev_b32 v142, 4, v0\n"
    "s_mul_i32 s79, s68, 36864\n"
    "v_add_u32 v143, s79, v142\n"
    "v_and_b32 v1, 15, v0\n"
    "s_lshl_b32 s79, s68, 9\n"
    "s_add_u32 s79, s79, 147456\n"
    "v_lshl_add_u32 v144, v1, 4, s79\n"
    "v_lshrrev_b32 v2, 5, v0\n"
    "v_lshl_add_u32 v144, v2, 11, v144\n"
    "v_bfe_u32 v2, v0, 4, 1\n"
    "v_lshl_add_u32 v144, v2, 3, v144\n"
    "v_lshrrev_b32 v2, 2, v0\n"
    "s_mov_b32 s79, 147456\n"
    "v_lshl_add_u32 v145, v2, 4, s79\n"
    "v_and_b32 v2, 1, v0\n"
    "v_lshl_add_u32 v145, v2, 3, v145\n"
    "v_and_b32 v2, 7, v0\n"
    "s_mov_b32 s79, 320\n"
    "v_mul_u32_u24 v3, s79, v2\n"
    "v_add_u32 v3, 151552, v3\n"
    "v_lshrrev_b32 v147, 3, v0\n"
    "v_lshl_add_u32 v147, v147, 4, v3\n"
    "s_lshl_b32 s79, s68, 6\n"
    "v_add_u32 v146, s79, v3\n"
    "v_bfe_u32 v3, v0, 3, 1\n"
    "v_lshl_add_u32 v146, v3, 5, v146\n"
    "v_lshrrev_b32 v155, 4, v0\n"
    "v_lshl_add_u32 v146, v155, 3, v146\n"
    "s_lshl_b32 s79, s64, 7\n"
    "s_add_u32 s79, s79, s70\n"
    "s_lshl_b32 s80, s92, 14\n"
    "s_add_u32 s79, s79, s80\n"
    "v_lshl_add_u32 v149, v2, 4, s79\n"
    "v_lshl_add_u32 v149, v3, 12, v149\n"
    "v_bfe_u32 v158, v0, 4, 1\n"
    "v_lshl_add_u32 v148, v158, 15, v149\n"
    "v_lshrrev_b32 v158, 5, v0\n"
    "v_lshl_add_u32 v148, v158, 14, v148\n"
    "v_add_u32 v149, 65536, v149\n"
    "v_cmp_gt_u32 vcc, 16, v0\n"
    "v_mov_b32 v158, 0x7fffffff\n"
    "s_nop 0\n"
    "v_cndmask_b32 v149, v158, v149, vcc\n"
    "s_add_u32 s79, s67, 64\n"
    "s_cmp_lt_u32 s67, 8\n"
    "s_cselect_b32 s79, s79, -1\n"
    "s_cmp_eq_u32 s68, 1\n"
    "s_cselect_b32 s79, s79, -1\n"
    "s_cmp_eq_u32 s68, 0\n"
    "s_cselect_b32 s78, s67, s79\n"
    "s_lshr_b32 s79, s78, 1\n"
    "s_lshl_b32 s79, s79, 14\n"
    "s_and_b32 s80, s78, 1\n"
    "s_lshl_b32 s80, s80, 12\n"
    "s_add_u32 s77, s79, s80\n"
    "s_add_u32 s77, s77, s70\n"
    "s_bfe_u32 s79, s78, 0x10006\n"
    "s_add_u32 s79, s79, 72\n"
    "s_lshl_b32 s79, s79, 14\n"
    "s_and_b32 s80, s78, 63\n"
    "s_lshl_b32 s80, s80, 7\n"
    "s_add_u32 s79, s79, s80\n"
    "s_add_u32 s79, s79, s70\n"
    "v_lshl_add_u32 v150, v2, 4, s79\n"
    "v_lshrrev_b32 v158, 3, v0\n"
    "v_lshl_add_u32 v150, v158, 15, v150\n"
    "v_cmp_gt_u32 vcc, 32, v0\n"
    "v_mov_b32 v158, 0x7fffffff\n"
    "s_nop 0\n"
    "v_cndmask_b32 v150, v158, v150, vcc\n"
    "s_mov_b32 s84, -1\n"
    "s_mov_b32 s85, 0\n"
    "s_mov_b32 s86, 0x00ff00ff\n"
    "s_mov_b32 s87, 0x00ff00ff\n"
    "v_mov_b32 v158, 0xeeee\n"
    "v_mov_b32 v159, 0x4444\n"
    "v_cndmask_b32 v151, v158, v159, s[86:87]\n"
    "v_mov_b32 v132, 0x3f803f80\n"
    "v_mov_b32 v133, 0x3f803f80\n"
    "v_mov_b32 v134, 0x3f803f80\n"
    "v_mov_b32 v135, 0x3f803f80\n"
    "v_cndmask_b32 v136, 0, v132, s[84:85]\n"
    "v_cndmask_b32 v137, 0, v132, s[84:85]\n"
    "s_lshr_b32 s79, s68, 1\n"
    "s_add_u32 s79, s79, 1\n"
    "s_mul_i32 s79, s79, 0x1000000\n"
    "s_and_b32 s80, s68, 1\n"
    "s_lshl_b32 s80, s80, 12\n"
    "s_add_u32 s79, s79, s80\n"
    "v_lshl_add_u32 v139, v0, 7, s79\n"
    "v_cmp_gt_u32 vcc, 32, v0\n"
    "v_mov_b32 v158, 0x7fffffff\n"
    "s_nop 0\n"
    "v_cndmask_b32 v139, v158, v139, vcc\n"
    "s_lshl_b32 s79, s64, 1\n"
    "s_add_u32 s79, s79, s65\n"
    "s_lshl_b32 s79, s79, 7\n"
    "s_add_u32 s79, s79, 0x1a8000\n"
    "v_mov_b32 v152, s79\n"
    "s_cmp_eq_u32 s68, 3\n"
    "s_cselect_b32 s79, 1, 0\n"
    "v_cmp_gt_u32 vcc, s79, v0\n"
    "v_mov_b32 v158, 0x7fffffff\n"
    "s_nop 0\n"
    "v_cndmask_b32 v152, v158, v152, vcc\n"
    "s_lshl_b32 s79, s64, 9\n"
    "s_add_u32 s79, s79, 0x205000\n"
    "v_lshl_add_u32 v153, v2, 6, s79\n"
    "v_lshl_add_u32 v153, v3, 14, v153\n"
    "v_cmp_gt_u32 vcc, 16, v0\n"
    "v_mov_b32 v158, 0x7fffffff\n"
    "s_nop 0\n"
    "v_cndmask_b32 v153, v158, v153, vcc\n"
    "s_lshl_b32 s94, s68, 9\n"
    "s_lshl_b32 s60, s64, 6\n"
    "s_lshl_b32 s90, s67, 4\n"
    "s_cmp_eq_u32 s68, 0\n"
    "s_cselect_b32 s90, s90, 0x7ff00000\n"
    "v_add_u32 v140, s83, v142\n"
    "v_add_u32 v154, s94, v145\n"
    "v_and_b32 v158, 1, v0\n"
    "v_lshlrev_b32 v158, 4, v158\n"
    "v_mov_b32 v159, 0x3f80\n"
    "v_lshlrev_b32 v141, v158, v159\n"
    "s_mov_b32 s69, 0\n"
    "s_mov_b32 s71, 0\n"
    "s_mov_b32 s72, 0\n"
    "s_mov_b32 s82, 1\n"
    "s_mov_b32 s73, 0\n"
    "ch_need1_%=:\n"
    "s_cmp_lt_u32 s82, s71\n"
    "s_cbranch_scc1 ch_needok2_%=\n"
    "s_cmp_lg_u32 s72, 0\n"
    "s_cbranch_scc1 ch_needok2_%=\n"
    "buffer_load_dword v158, v153, s[20:23], 0 offen sc1\n"
    "v_bfe_u32 v159, v142, 6, 1\n"
    "s_waitcnt vmcnt(0)\n"
    "v_lshl_or_b32 v158, v158, 1, v159\n"
    "s_nop 1\n"
    "v_min_u32_dpp v158, v158, v158 row_ror:8 row_mask:0xf bank_mask:0xf\n"
    "s_nop 1\n"
    "v_min_u32_dpp v158, v158, v158 row_ror:4 row_mask:0xf bank_mask:0xf\n"
    "s_nop 1\n"
    "v_min_u32_dpp v158, v158, v158 row_ror:2 row_mask:0xf bank_mask:0xf\n"
    "s_nop 1\n"
    "v_min_u32_dpp v158, v158, v158 row_ror:1 row_mask:0xf bank_mask:0xf\n"
    "s_nop 0\n"
    "v_readfirstlane_b32 s71, v158\n"
    "s_add_u32 s73, s73, 1\n"
    "s_cmp_gt_u32 s73, 0x40000\n"
    "s_cselect_b32 s72, 1, 0\n"
    "s_branch ch_need1_%=\n"
    "ch_needok2_%=:\n"
    "s_add_u32 s79, s69, s60\n"
    "s_lshl_b32 s79, s79, 13\n"
    "s_add_u32 s79, s79, 0x1000000\n"
    "s_add_u32 s80, s79, 4096\n"
    "s_lshl_b32 s81, s68, 11\n"
    "s_add_u32 s81, s81, s79\n"
    "s_add_u32 s81, s81, 0x1000000\n"
    "buffer_load_dwordx4 v[60:63], v142, s[24:27], s79 offen sc1\n"
    "buffer_load_dwordx4 v[64:67], v142, s[24:27], s80 offen sc1\n"
    "buffer_load_dwordx4 v[68:71], v142, s[24:27], s79 offen offset:1024 sc1\n"
    "buffer_load_dwordx4 v[72:75], v142, s[24:27], s80 offen offset:1024 sc1\n"
    "buffer_load_dwordx4 v[76:79], v142, s[24:27], s79 offen offset:2048 sc1\n"
    "buffer_load_dwordx4 v[80:83], v142, s[24:27], s80 offen offset:2048 sc1\n"
    "buffer_load_dwordx4 v[84:87], v142, s[24:27], s79 offen offset:3072 sc1\n"
    "buffer_load_dwordx4 v[88:91], v142, s[24:27], s80 offen offset:3072 sc1\n"
    "buffer_load_dwordx4 v[92:95], v142, s[24:27], s81 offen sc1\n"
    "buffer_load_dwordx4 v[96:99], v142, s[24:27], s81 offen offset:1024 sc1\n"
    "s_add_u32 s79, s69, s60\n"
    "s_lshl_b32 s79, s79, 2\n"
    "s_add_u32 s79, s79, s68\n"
    "s_lshl_b32 s79, s79, 10\n"
    "s_add_u32 s80, s79, 0x800000\n"
    "s_cmp_eq_u32 s65, 0\n"
    "s_cselect_b32 s92, s79, 0x7ff00000\n"
    "s_cselect_b32 s95, 0x7ff00000, s80\n"
    "s_add_u32 s79, s60, 1\n"
    "s_lshl_b32 s54, s79, 13\n"
    "s_add_u32 s36, s42, 0\n"
    "s_addc_u32 s37, s43, 0\n"
    "s_and_b32 s37, s37, 0xffff\n"
    "s_mov_b32 s38, 9216\n"
    "s_mov_b32 s39, 0x20000\n"
    "s_mov_b32 s79, 1152\n"
    "v_mul_u32_u24 v158, s79, v2\n"
    "v_lshrrev_b32 v159, 3, v0\n"
    "v_lshl_add_u32 v158, v159, 4, v158\n"
    "s_nop 1\n"
    "buffer_load_dwordx4 v[0:3], v158, s[36:39], 0 offen\n"
    "buffer_load_dwordx4 v[4:7], v158, s[36:39], 0 offen offset:128\n"
    "buffer_load_dwordx4 v[8:11], v158, s[36:39], 0 offen offset:256\n"
    "buffer_load_dwordx4 v[12:15], v158, s[36:39], 0 offen offset:384\n"
    "buffer_load_dwordx4 v[16:19], v158, s[36:39], 0 offen offset:512\n"
    "buffer_load_dwordx4 v[20:23], v158, s[36:39], 0 offen offset:640\n"
    "buffer_load_dwordx4 v[24:27], v158, s[36:39], 0 offen offset:768\n"
    "buffer_load_dwordx4 v[28:31], v158, s[36:39], 0 offen offset:896\n"
    "buffer_load_dwordx4 v[32:35], v158, s[36:39], 0 offen offset:1024\n"
    "v_mov_b64 v[36:37], 0\n"
    "v_mov_b64 v[38:39], 0\n"
    "v_mov_b64 v[40:41], 0\n"
    "v_mov_b64 v[42:43], 0\n"
    "v_mov_b64 v[44:45], 0\n"
    "v_mov_b64 v[46:47], 0\n"
    "v_mov_b64 v[48:49], 0\n"
    "v_mov_b64 v[50:51], 0\n"
    "v_mov_b64 v[52:53], 0\n"
    "v_mov_b64 v[54:55], 0\n"
    "v_mov_b64 v[56:57], 0\n"
    "v_mov_b64 v[58:59], 0\n"
    "ds_read_b128 v[100:103], v143\n"
    "ds_read_b128 v[104:107], v143 offset:1024\n"
    "ds_read_b128 v[108:111], v143 offset:2048\n"
    "ds_read_b128 v[112:115], v143 offset:3072\n"
    "s_add_u32 s42, s70, 1720320\n"
    "s_waitcnt vmcnt(0)\n"
    "s_memrealtime s[58:59]\n"
    "s_waitcnt lgkmcnt(0)\n"
    "s_mov_b32 s96, s58\n"
    "s_mov_b32 s97, s58\n"
    "s_mov_b32 s98, 250\n"
    "ch_qkn_%=:\n"
    "v_smfmac_f32_16x16x64_bf16 v[36:39], v[0:3], a[0:7], v151\n"
    "v_smfmac_f32_16x16x64_bf16 v[40:43], v[0:3], a[144:151], v151\n"
    "v_smfmac_f32_16x16x64_bf16 v[44:47], v[0:3], a[72:79], v151\n"
    "v_smfmac_f32_16x16x64_bf16 v[48:51], v[0:3], v[100:107], v151\n"
    "ds_read_b128 v[100:103], v143 offset:4096\n"
    "ds_read_b128 v[104:107], v143 offset:5120\n"
    "v_smfmac_f32_16x16x64_bf16 v[36:39], v[4:7], a[8:15], v151\n"
    "v_smfmac_f32_16x16x64_bf16 v[40:43], v[4:7], a[152:159], v151\n"
    "v_smfmac_f32_16x16x64_bf16 v[44:47], v[4:7], a[80:87], v151\n"
    "v_smfmac_f32_16x16x64_bf16 v[48:51], v[4:7], v[108:115], v151\n"
    "ds_read_b128 v[108:111], v143 offset:6144\n"
    "ds_read_b128 v[112:115], v143 offset:7168\n"
    "v_smfmac_f32_16x16x64_bf16 v[36:39], v[8:11], a[16:23], v151\n"
    "v_smfmac_f32_16x16x64_bf16 v[40:43], v[8:11], a[160:167], v151\n"
    "v_smfmac_f32_16x16x64_bf16 v[44:47], v[8:11], a[88:95], v151\n"
    "s_waitcnt lgkmcnt(2)\n"
    "v_smfmac_f32_16x16x64_bf16 v[48:51], v[8:11], v[100:107], v151\n"
    "ds_read_b128 v[100:103], v143 offset:8192\n"
    "ds_read_b128 v[104:107], v143 offset:9216\n"
    "v_smfmac_f32_16x16x64_bf16 v[36:39], v[12:15], a[24:31], v151\n"
    "v_smfmac_f32_16x16x64_bf16 v[40:43], v[12:15], a[168:175], v151\n"
    "v_smfmac_f32_16x16x64_bf16 v[44:47], v[12:15], a[96:103], v151\n"
    "s_waitcnt lgkmcnt(2)\n"
    "v_smfmac_f32_16x16x64_bf16 v[48:51], v[12:15], v[108:115], v151\n"
    "ds_read_b128 v[108:111], v143 offset:10240\n"
    "ds_read_b128 v[112:115], v143 offset:11264\n"
    "v_smfmac_f32_16x16x64_bf16 v[36:39], v[16:19], a[32:39], v151\n"
    "v_smfmac_f32_16x16x64_bf16 v[40:43], v[16:19], a[176:183], v151\n"
    "v_smfmac_f32_16x16x64_bf16 v[44:47], v[16:19], a[104:111], v151\n"
    "s_waitcnt lgkmcnt(2)\n"
    "v_smfmac_f32_16x16x64_bf16 v[48:51], v[16:19], v[100:107], v151\n"
    "ds_read_b128 v[100:103], v143 offset:12288\n"
    "ds_read_b128 v[104:107], v143 offset:13312\n"
    "v_smfmac_f32_16x16x64_bf16 v[36:39], v[20:23], a[40:47], v151\n"
    "v_smfmac_f32_16x16x64_bf16 v[40:43], v[20:23], a[184:191], v151\n"
    "v_smfmac_f32_16x16x64_bf16 v[44:47], v[20:23], a[112:119], v151\n"
    "s_waitcnt lgkmcnt(2)\n"
    "v_smfmac_f32_16x16x64_bf16 v[48:51], v[20:23], v[108:115], v151\n"
    "ds_read_b128 v[108:111], v143 offset:14336\n"
    "ds_read_b128 v[112:115], v143 offset:15360\n"
    "v_smfmac_f32_16x16x64_bf16 v[36:39], v[24:27], a[48:55], v151\n"
    "v_smfmac_f32_16x16x64_bf16 v[40:43], v[24:27], a[192:199], v151\n"
    "v_smfmac_f32_16x16x64_bf16 v[44:47], v[24:27], a[120:127], v151\n"
    "s_waitcnt lgkmcnt(2)\n"
    "v_smfmac_f32_16x16x64_bf16 v[48:51], v[24:27], v[100:107], v151\n"
    "ds_read_b128 v[100:103], v143 offset:16384\n"
    "ds_read_b128 v[104:107], v143 offset:17408\n"
    "v_smfmac_f32_16x16x64_bf16 v[36:39], v[28:31], a[56:63], v151\n"
    "v_smfmac_f32_16x16x64_bf16 v[40:43], v[28:31], a[200:207], v151\n"
    "v_smfmac_f32_16x16x64_bf16 v[44:47], v[28:31], a[128:135], v151\n"
    "s_waitcnt lgkmcnt(2)\n"
    "v_smfmac_f32_16x16x64_bf16 v[48:51], v[28:31], v[108:115], v151\n"
    "ds_read_b128 v[108:111], v143 offset:20480\n"
    "ds_read_b128 v[112:115], v143 offset:21504\n"
    "v_smfmac_f32_16x16x64_bf16 v[36:39], v[32:35], a[64:71], v151\n"
    "v_smfmac_f32_16x16x64_bf16 v[40:43], v[32:35], a[208:215], v151\n"
    "v_smfmac_f32_16x16x64_bf16 v[44:47], v[32:35], a[136:143], v151\n"
    "s_waitcnt lgkmcnt(2)\n"
    "v_smfmac_f32_16x16x64_bf16 v[48:51], v[32:35], v[100:107], v151\n"
    "ds_read_b128 v[100:103], v143 offset:18432\n"
    "ds_read_b128 v[104:107], v143 offset:19456\n"
    "s_branch ch_fin_%=\n"
    "ch_step_%=:\n"
    "buffer_load_dwordx4 v[0:3], v142, s[20:23], s74 offen sc1\n"
    "s_nop 5\n"
    "buffer_load_dwordx4 v[4:7], v142, s[20:23], s74 offen offset:1024 sc1\n"
    "s_nop 5\n"
    "buffer_load_dwordx4 v[8:11], v142, s[20:23], s74 offen offset:2048 sc1\n"
    "s_nop 5\n"
    "buffer_load_dwordx4 v[12:15], v142, s[20:23], s74 offen offset:3072 sc1\n"
    "s_nop 5\n"
    "buffer_load_dwordx4 v[16:19], v142, s[20:23], s75 offen sc1\n"
    "s_nop 5\n"
    "buffer_load_dwordx4 v[20:23], v142, s[20:23], s75 offen offset:1024 sc1\n"
    "s_nop 5\n"
    "buffer_load_dwordx4 v[24:27], v142, s[20:23], s75 offen offset:2048 sc1\n"
    "s_nop 5\n"
    "buffer_load_dwordx4 v[28:31], v142, s[20:23], s75 offen offset:3072 sc1\n"
    "s_nop 5\n"
    "buffer_load_dwordx4 v[32:35], v142, s[20:23], s76 offen sc1\n"
    "v_mov_b32 v158, s69\n"
    "buffer_store_dword v158, v152, s[20:23], s70 offen sc1\n"
    "ch_qkc_%=:\n"
    "s_waitcnt vmcnt(9)\n"
    "v_max_u32 v155, v0, v2\n"
    "v_cmp_eq_u32 vcc, -1, v155\n"
    "s_cbranch_vccnz ch_slowc_0_%=\n"
    "ch_backc_0_%=:\n"
    "v_smfmac_f32_16x16x64_bf16 v[36:39], v[0:3], a[0:7], v151\n"
    "s_waitcnt vmcnt(8)\n"
    "v_max_u32 v155, v4, v6\n"
    "v_smfmac_f32_16x16x64_bf16 v[40:43], v[0:3], a[144:151], v151\n"
    "v_cmp_eq_u32 vcc, -1, v155\n"
    "v_smfmac_f32_16x16x64_bf16 v[44:47], v[0:3], a[72:79], v151\n"
    "v_smfmac_f32_16x16x64_bf16 v[48:51], v[0:3], v[100:107], v151\n"
    "ds_read_b128 v[100:103], v143 offset:4096\n"
    "ds_read_b128 v[104:107], v143 offset:5120\n"
    "s_cbranch_vccnz ch_slowc_1_%=\n"
    "ch_backc_1_%=:\n"
    "v_smfmac_f32_16x16x64_bf16 v[36:39], v[4:7], a[8:15], v151\n"
    "s_waitcnt vmcnt(7)\n"
    "v_max_u32 v155, v8, v10\n"
    "v_smfmac_f32_16x16x64_bf16 v[40:43], v[4:7], a[152:159], v151\n"
    "v_cmp_eq_u32 vcc, -1, v155\n"
    "v_smfmac_f32_16x16x64_bf16 v[44:47], v[4:7], a[80:87], v151\n"
    "v_smfmac_f32_16x16x64_bf16 v[48:51], v[4:7], v[108:115], v151\n"
    "ds_read_b128 v[108:111], v143 offset:6144\n"
    "ds_read_b128 v[112:115], v143 offset:7168\n"
    "s_cbranch_vccnz ch_slowc_2_%=\n"
    "ch_backc_2_%=:\n"
    "v_smfmac_f32_16x16x64_bf16 v[36:39], v[8:11], a[16:23], v151\n"
    "s_waitcnt vmcnt(6)\n"
    "v_max_u32 v155, v12, v14\n"
    "v_smfmac_f32_16x16x64_bf16 v[40:43], v[8:11], a[160:167], v151\n"
    "v_cmp_eq_u32 vcc, -1, v155\n"
    "v_smfmac_f32_16x16x64_bf16 v[44:47], v[8:11], a[88:95], v151\n"
    "s_waitcnt lgkmcnt(2)\n"
    "v_smfmac_f32_16x16x64_bf16 v[48:51], v[8:11], v[100:107], v151\n"
    "ds_read_b128 v[100:103], v143 offset:8192\n"
    "ds_read_b128 v[104:107], v143 offset:9216\n"
    "s_cbranch_vccnz ch_slowc_3_%=\n"
    "ch_backc_3_%=:\n"
    "v_smfmac_f32_16x16x64_bf16 v[36:39], v[12:15], a[24:31], v151\n"
    "s_waitcnt vmcnt(5)\n"
    "v_max_u32 v155, v16, v18\n"
    "v_smfmac_f32_16x16x64_bf16 v[40:43], v[12:15], a[168:175], v151\n"
    "v_cmp_eq_u32 vcc, -1, v155\n"
    "v_smfmac_f32_16x16x64_bf16 v[44:47], v[12:15], a[96:103], v151\n"
    "s_waitcnt lgkmcnt(2)\n"
    "v_smfmac_f32_16x16x64_bf16 v[48:51], v[12:15], v[108:115], v151\n"
    "ds_read_b128 v[108:111], v143 offset:10240\n"
    "ds_read_b128 v[112:115], v143 offset:11264\n"
    "s_cbranch_vccnz ch_slowc_4_%=\n"
    "ch_backc_4_%=:\n"
    "v_smfmac_f32_16x16x64_bf16 v[36:39], v[16:19], a[32:39], v151\n"
    "s_waitcnt vmcnt(4)\n"
    "v_max_u32 v155, v20, v22\n"
    "v_smfmac_f32_16x16x64_bf16 v[40:43], v[16:19], a[176:183], v151\n"
    "v_cmp_eq_u32 vcc, -1, v155\n"
    "v_smfmac_f32_16x16x64_bf16 v[44:47], v[16:19], a[104:111], v151\n"
    "s_waitcnt lgkmcnt(2)\n"
    "v_smfmac_f32_16x16x64_bf16 v[48:51], v[16:19], v[100:107], v151\n"
    "ds_read_b128 v[100:103], v143 offset:12288\n"
    "ds_read_b128 v[104:107], v143 offset:13312\n"
    "s_cbranch_vccnz ch_slowc_5_%=\n"
    "ch_backc_5_%=:\n"
    "v_smfmac_f32_16x16x64_bf16 v[36:39], v[20:23], a[40:47], v151\n"
    "s_waitcnt vmcnt(3)\n"
    "v_max_u32 v155, v24, v26\n"
    "v_smfmac_f32_16x16x64_bf16 v[40:43], v[20:23], a[184:191], v151\n"
    "v_cmp_eq_u32 vcc, -1, v155\n"
    "v_smfmac_f32_16x16x64_bf16 v[44:47], v[20:23], a[112:119], v151\n"
    "s_waitcnt lgkmcnt(2)\n"
    "v_smfmac_f32_16x16x64_bf16 v[48:51], v[20:23], v[108:115], v151\n"
    "ds_read_b128 v[108:111], v143 offset:14336\n"
    "ds_read_b128 v[112:115], v143 offset:15360\n"
    "s_cbranch_vccnz ch_slowc_6_%=\n"
    "ch_backc_6_%=:\n"
    "v_smfmac_f32_16x16x64_bf16 v[36:39], v[24:27], a[48:55], v151\n"
    "s_waitcnt vmcnt(2)\n"
    "v_max_u32 v155, v28, v30\n"
    "v_smfmac_f32_16x16x64_bf16 v[40:43], v[24:27], a[192:199], v151\n"
    "v_cmp_eq_u32 vcc, -1, v155\n"
    "v_smfmac_f32_16x16x64_bf16 v[44:47], v[24:27], a[120:127], v151\n"
    "s_waitcnt lgkmcnt(2)\n"
    "v_smfmac_f32_16x16x64_bf16 v[48:51], v[24:27], v[100:107], v151\n"
    "ds_read_b128 v[100:103], v143 offset:16384\n"
    "ds_read_b128 v[104:107], v143 offset:17408\n"
    "s_cbranch_vccnz ch_slowc_7_%=\n"
    "ch_backc_7_%=:\n"
    "v_smfmac_f32_16x16x64_bf16 v[36:39], v[28:31], a[56:63], v151\n"
    "s_waitcnt vmcnt(1)\n"
    "v_max_u32 v155, v32, v34\n"
    "v_smfmac_f32_16x16x64_bf16 v[40:43], v[28:31], a[200:207], v151\n"
    "v_cmp_eq_u32 vcc, -1, v155\n"
    "v_smfmac_f32_16x16x64_bf16 v[44:47], v[28:31], a[128:135], v151\n"
    "s_waitcnt lgkmcnt(2)\n"
    "v_smfmac_f32_16x16x64_bf16 v[48:51], v[28:31], v[108:115], v151\n"
    "ds_read_b128 v[108:111], v143 offset:20480\n"
    "ds_read_b128 v[112:115], v143 offset:21504\n"
    "s_cbranch_vccnz ch_slowc_8_%=\n"
    "ch_backc_8_%=:\n"
    "v_smfmac_f32_16x16x64_bf16 v[36:39], v[32:35], a[64:71], v151\n"
    "v_smfmac_f32_16x16x64_bf16 v[40:43], v[32:35], a[208:215], v151\n"
    "v_smfmac_f32_16x16x64_bf16 v[44:47], v[32:35], a[136:143], v151\n"
    "s_waitcnt lgkmcnt(2)\n"
    "v_smfmac_f32_16x16x64_bf16 v[48:51], v[32:35], v[100:107], v151\n"
    "ds_read_b128 v[100:103], v143 offset:18432\n"
    "ds_read_b128 v[104:107], v143 offset:19456\n"
    "ch_fin_%=:\n"
    "s_nop 2\n"
    "v_permlane32_swap_b32 v36, v40\n"
    "v_permlane32_swap_b32 v37, v41\n"
    "v_permlane32_swap_b32 v38, v42\n"
    "v_permlane32_swap_b32 v39, v43\n"
    "v_pk_add_f32 v[36:37], v[36:37], v[40:41]\n"
    "v_pk_add_f32 v[38:39], v[38:39], v[42:43]\n"
    "v_permlane32_swap_b32 v44, v48\n"
    "v_permlane32_swap_b32 v45, v49\n"
    "v_permlane32_swap_b32 v46, v50\n"
    "v_permlane32_swap_b32 v47, v51\n"
    "v_cvt_pk_bf16_f32 v40, v36, v37\n"
    "v_cvt_pk_bf16_f32 v41, v38, v39\n"
    "v_pk_add_f32 v[44:45], v[44:45], v[48:49]\n"
    "v_pk_add_f32 v[46:47], v[46:47], v[50:51]\n"
    "ds_write_b64 v144, v[40:41]\n"
    "v_cvt_pk_bf16_f32 v48, v44, v45\n"
    "v_cvt_pk_bf16_f32 v49, v46, v47\n"
    "ds_write_b64 v144, v[48:49] offset:256\n"
    "v_smfmac_f32_16x16x64_bf16 v[52:55], v[0:3], a[216:223], v151\n"
    "s_waitcnt lgkmcnt(2)\n"
    "v_smfmac_f32_16x16x64_bf16 v[56:59], v[0:3], v[100:107], v151\n"
    "v_smfmac_f32_16x16x64_bf16 v[52:55], v[4:7], a[224:231], v151\n"
    "v_smfmac_f32_16x16x64_bf16 v[56:59], v[4:7], v[108:115], v151\n"
    "ds_read_b128 v[100:103], v143 offset:22528\n"
    "ds_read_b128 v[104:107], v143 offset:23552\n"
    "ds_read_b128 v[108:111], v143 offset:32768\n"
    "ds_read_b128 v[112:115], v143 offset:33792\n"
    "s_waitcnt lgkmcnt(4)\n"
    "s_barrier\n"
    "ds_read_b64_tr_b16 v[36:37], v145\n"
    "ds_read_b64_tr_b16 v[38:39], v145 offset:256\n"
    "ds_read_b64_tr_b16 v[40:41], v145 offset:512\n"
    "ds_read_b64_tr_b16 v[42:43], v145 offset:768\n"
    "ds_read_b64_tr_b16 v[44:45], v145 offset:1024\n"
    "ds_read_b64_tr_b16 v[46:47], v145 offset:1280\n"
    "ds_read_b64_tr_b16 v[48:49], v145 offset:1536\n"
    "ds_read_b64_tr_b16 v[50:51], v145 offset:1792\n"
    "v_smfmac_f32_16x16x64_bf16 v[52:55], v[8:11], a[232:239], v151\n"
    "s_waitcnt lgkmcnt(10)\n"
    "v_smfmac_f32_16x16x64_bf16 v[56:59], v[8:11], v[100:107], v151\n"
    "ds_read_b64_tr_b16 v[116:117], v145 offset:2048\n"
    "ds_read_b64_tr_b16 v[118:119], v145 offset:2304\n"
    "ds_read_b64_tr_b16 v[120:121], v145 offset:2560\n"
    "ds_read_b64_tr_b16 v[122:123], v145 offset:2816\n"
    "ds_read_b64_tr_b16 v[124:125], v145 offset:3072\n"
    "ds_read_b64_tr_b16 v[126:127], v145 offset:3328\n"
    "ds_read_b64_tr_b16 v[128:129], v145 offset:3584\n"
    "ds_read_b64_tr_b16 v[130:131], v145 offset:3840\n"
    "s_waitcnt lgkmcnt(14)\n"
    "v_mfma_f32_16x16x32_bf16 v[0:3], v[60:63], v[36:39], 0\n"
    "v_mfma_f32_16x16x32_bf16 v[4:7], v[64:67], v[36:39], 0\n"
    "ds_read_b128 v[60:63], v143 offset:24576\n"
    "ds_read_b128 v[64:67], v143 offset:25600\n"
    "s_waitcnt lgkmcnt(14)\n"
    "v_mfma_f32_16x16x32_bf16 v[0:3], v[68:71], v[40:43], v[0:3]\n"
    "v_mfma_f32_16x16x32_bf16 v[4:7], v[72:75], v[40:43], v[4:7]\n"
    "ds_read_b128 v[68:71], v143 offset:26624\n"
    "ds_read_b128 v[72:75], v143 offset:27648\n"
    "s_waitcnt lgkmcnt(14)\n"
    "v_mfma_f32_16x16x32_bf16 v[0:3], v[76:79], v[44:47], v[0:3]\n"
    "v_mfma_f32_16x16x32_bf16 v[4:7], v[80:83], v[44:47], v[4:7]\n"
    "ds_read_b128 v[76:79], v143 offset:28672\n"
    "ds_read_b128 v[80:83], v143 offset:29696\n"
    "s_waitcnt lgkmcnt(14)\n"
    "v_mfma_f32_16x16x32_bf16 v[0:3], v[84:87], v[48:51], v[0:3]\n"
    "v_mfma_f32_16x16x32_bf16 v[4:7], v[88:91], v[48:51], v[4:7]\n"
    "ds_read_b128 v[84:87], v143 offset:30720\n"
    "ds_read_b128 v[88:91], v143 offset:31744\n"
    "s_waitcnt lgkmcnt(14)\n"
    "v_mfma_f32_16x16x32_bf16 v[8:11], v[116:119], v[36:39], 0\n"
    "ds_read_b128 v[100:103], v143 offset:34816\n"
    "ds_read_b128 v[104:107], v143 offset:35840\n"
    "s_waitcnt lgkmcnt(14)\n"
    "v_mfma_f32_16x16x32_bf16 v[8:11], v[120:123], v[40:43], v[8:11]\n"
    "s_waitcnt lgkmcnt(12)\n"
    "v_mfma_f32_16x16x32_bf16 v[8:11], v[124:127], v[44:47], v[8:11]\n"
    "buffer_load_dword v138, v139, s[24:27], s54 offen sc1\n"
    "s_waitcnt lgkmcnt(10)\n"
    "v_mfma_f32_16x16x32_bf16 v[8:11], v[128:131], v[48:51], v[8:11]\n"
    "buffer_load_dword v138, v142, s[20:23], s42 offen sc1\n"
    "s_cmp_eq_u32 s69, 63\n"
    "v_smfmac_f32_16x16x64_bf16 v[52:55], v[12:15], a[240:247], v151\n"
    "v_max3_f32 v155, v0, v1, v2\n"
    "v_max3_f32 v155, v155, v3, v4\n"
    "s_waitcnt lgkmcnt(8)\n"
    "v_smfmac_f32_16x16x64_bf16 v[56:59], v[12:15], v[60:67], v151\n"
    "v_max3_f32 v155, v155, v5, v6\n"
    "s_cbranch_scc1 ch_last_%=\n"
    "v_smfmac_f32_16x16x64_bf16 v[52:55], v[16:19], a[248:255], v151\n"
    "v_max3_f32 v155, v155, v7, v8\n"
    "v_max3_f32 v155, v155, v9, v10\n"
    "v_max_f32 v155, v155, v11\n"
    "v_mov_b32 v158, v155\n"
    "s_waitcnt lgkmcnt(6)\n"
    "v_smfmac_f32_16x16x64_bf16 v[56:59], v[16:19], v[68:75], v151\n"
    "v_permlane16_swap_b32 v155, v158\n"
    "v_max_f32 v155, v155, v158\n"
    "v_mov_b32 v158, v155\n"
    "v_smfmac_f32_16x16x64_bf16 v[52:55], v[20:23], v[160:167], v151\n"
    "s_nop 0\n"
    "v_permlane32_swap_b32 v155, v158\n"
    "v_max_f32 v155, v155, v158\n"
    "v_mul_f32 v155, 0xbe0293ee, v155\n"
    "v_fmamk_f32 v0, v0, 0x3e0293ee, v155\n"
    "v_fmamk_f32 v1, v1, 0x3e0293ee, v155\n"
    "v_fmamk_f32 v2, v2, 0x3e0293ee, v155\n"
    "v_fmamk_f32 v3, v3, 0x3e0293ee, v155\n"
    "v_fmamk_f32 v4, v4, 0x3e0293ee, v155\n"
    "v_fmamk_f32 v5, v5, 0x3e0293ee, v155\n"
    "v_fmamk_f32 v6, v6, 0x3e0293ee, v155\n"
    "v_fmamk_f32 v7, v7, 0x3e0293ee, v155\n"
    "v_exp_f32 v0, v0\n"
    "s_waitcnt lgkmcnt(4)\n"
    "v_smfmac_f32_16x16x64_bf16 v[56:59], v[20:23], v[76:83], v151\n"
    "v_exp_f32 v1, v1\n"
    "v_smfmac_f32_16x16x64_bf16 v[52:55], v[24:27], v[168:175], v151\n"
    "v_exp_f32 v2, v2\n"
    "s_waitcnt lgkmcnt(2)\n"
    "v_smfmac_f32_16x16x64_bf16 v[56:59], v[24:27], v[84:91], v151\n"
    "v_exp_f32 v3, v3\n"
    "v_smfmac_f32_16x16x64_bf16 v[52:55], v[28:31], v[176:183], v151\n"
    "v_exp_f32 v4, v4\n"
    "v_smfmac_f32_16x16x64_bf16 v[56:59], v[28:31], v[108:115], v151\n"
    "v_exp_f32 v5, v5\n"
    "v_smfmac_f32_16x16x64_bf16 v[52:55], v[32:35], v[184:191], v151\n"
    "v_exp_f32 v6, v6\n"
    "s_waitcnt lgkmcnt(0)\n"
    "v_smfmac_f32_16x16x64_bf16 v[56:59], v[32:35], v[100:107], v151\n"
    "v_exp_f32 v7, v7\n"
    "ds_read_b64_tr_b16 v[108:109], v154 offset:2048\n"
    "ds_read_b64_tr_b16 v[110:111], v154 offset:2304\n"
    "v_lshrrev_b32 v159, 1, v142\n"
    "s_cmp_eq_u32 s65, 0\n"
    "v_fmamk_f32 v8, v8, 0x3e0293ee, v155\n"
    "v_fmamk_f32 v9, v9, 0x3e0293ee, v155\n"
    "v_fmamk_f32 v10, v10, 0x3e0293ee, v155\n"
    "v_fmamk_f32 v11, v11, 0x3e0293ee, v155\n"
    "v_cvt_pk_bf16_f32 v0, v0, v1\n"
    "v_cvt_pk_bf16_f32 v1, v2, v3\n"
    "v_cvt_pk_bf16_f32 v2, v4, v5\n"
    "v_cvt_pk_bf16_f32 v3, v6, v7\n"
    "v_exp_f32 v8, v8\n"
    "v_exp_f32 v9, v9\n"
    "v_mfma_f32_16x16x32_bf16 v[116:119], v[132:135], v[0:3], 0\n"
    "v_exp_f32 v10, v10\n"
    "v_exp_f32 v11, v11\n"
    "v_mfma_f32_16x16x32_bf16 v[120:123], v[92:95], v[0:3], 0\n"
    "v_cvt_pk_bf16_f32 v4, v8, v9\n"
    "v_cvt_pk_bf16_f32 v5, v10, v11\n"
    "v_mfma_f32_16x16x32_bf16 v[124:127], v[96:99], v[0:3], 0\n"
    "v_permlane32_swap_b32 v52, v56\n"
    "v_permlane32_swap_b32 v53, v57\n"
    "v_permlane32_swap_b32 v54, v58\n"
    "v_permlane32_swap_b32 v55, v59\n"
    "v_pk_add_f32 v[52:53], v[52:53], v[56:57]\n"
    "v_pk_add_f32 v[54:55], v[54:55], v[58:59]\n"
    "v_cvt_pk_bf16_f32 v156, v52, v53\n"
    "v_cvt_pk_bf16_f32 v157, v54, v55\n"
    "v_mfma_f32_16x16x16_bf16 v[116:119], v[136:137], v[4:5], v[116:119]\n"
    "v_cndmask_b32 v52, 0, v156, s[84:85]\n"
    "v_cndmask_b32 v53, 0, v157, s[84:85]\n"
    "v_cndmask_b32 v54, v156, 0, s[84:85]\n"
    "v_cndmask_b32 v55, v157, 0, s[84:85]\n"
    "v_mfma_f32_16x16x16_bf16 v[120:123], v[52:53], v[4:5], v[120:123]\n"
    "s_nop 0\n"
    "v_mfma_f32_16x16x16_bf16 v[124:127], v[54:55], v[4:5], v[124:127]\n"
    "s_cbranch_scc1 ch_pubk_%=\n"
    "buffer_store_dwordx2 v[156:157], v159, s[28:31], s95 offen sc1\n"
    "s_branch ch_pubd_%=\n"
    "ch_pubk_%=:\n"
    "s_waitcnt lgkmcnt(0)\n"
    "buffer_store_dwordx4 v[108:111], v142, s[28:31], s92 offen sc1\n"
    "ch_pubd_%=:\n"
    "v_mov_b64 v[36:37], 0\n"
    "v_mov_b64 v[38:39], 0\n"
    "v_mov_b64 v[40:41], 0\n"
    "v_mov_b64 v[42:43], 0\n"
    "v_rcp_f32 v158, v116\n"
    "ds_read_b128 v[68:71], v140\n"
    "ds_read_b128 v[72:75], v140 offset:1024\n"
    "ds_read_b128 v[76:79], v140 offset:2048\n"
    "ds_read_b128 v[80:83], v140 offset:3072\n"
    "v_cndmask_b32 v120, v124, v120, s[86:87]\n"
    "v_cndmask_b32 v121, v125, v121, s[86:87]\n"
    "v_cndmask_b32 v122, v126, v122, s[86:87]\n"
    "v_cndmask_b32 v123, v127, v123, s[86:87]\n"
    "v_pk_mul_f32 v[120:121], v[120:121], v[158:159] op_sel_hi:[1,0]\n"
    "v_pk_mul_f32 v[122:123], v[122:123], v[158:159] op_sel_hi:[1,0]\n"
    "v_cvt_pk_bf16_f32 v124, v120, v121\n"
    "v_cvt_pk_bf16_f32 v125, v122, v123\n"
    "ds_write_b64 v146, v[124:125]\n"
    "v_mov_b64 v[44:45], 0\n"
    "v_mov_b64 v[46:47], 0\n"
    "v_mov_b64 v[48:49], 0\n"
    "v_mov_b64 v[50:51], 0\n"
    "v_mov_b64 v[84:85], 0\n"
    "v_mov_b64 v[86:87], 0\n"
    "s_add_u32 s82, s69, 1\n"
    "s_and_b32 s89, s69, 1\n"
    "s_mul_i32 s89, s89, 589824\n"
    "s_waitcnt lgkmcnt(0)\n"
    "s_barrier\n"
    "ds_read_b128 v[60:63], v147\n"
    "ds_read_b128 v[64:67], v147 offset:128\n"
    "s_waitcnt lgkmcnt(1)\n"
    "v_smfmac_f32_16x16x64_bf16 v[36:39], v[60:63], v[192:199], v151\n"
    "v_smfmac_f32_16x16x64_bf16 v[40:43], v[60:63], v[208:215], v151\n"
    "s_waitcnt lgkmcnt(0)\n"
    "v_smfmac_f32_16x16x64_bf16 v[36:39], v[64:67], v[200:207], v151\n"
    "v_smfmac_f32_16x16x64_bf16 v[40:43], v[64:67], v[216:223], v151\n"
    "v_smfmac_f32_16x16x64_bf16 v[44:47], v[60:63], v[224:231], v151\n"
    "s_cmp_lt_u32 s68, 2\n"
    "v_smfmac_f32_16x16x64_bf16 v[48:51], v[60:63], v[240:247], v151\n"
    "v_smfmac_f32_16x16x64_bf16 v[44:47], v[64:67], v[232:239], v151\n"
    "v_smfmac_f32_16x16x64_bf16 v[48:51], v[64:67], v[248:255], v151\n"
    "s_nop 1\n"
    "s_nop 0\n"
    "v_permlane32_swap_b32 v36, v40\n"
    "v_smfmac_f32_16x16x64_bf16 v[84:87], v[60:63], v[68:75], v151\n"
    "v_permlane32_swap_b32 v37, v41\n"
    "v_permlane32_swap_b32 v38, v42\n"
    "v_smfmac_f32_16x16x64_bf16 v[84:87], v[64:67], v[76:83], v151\n"
    "v_permlane32_swap_b32 v39, v43\n"
    "v_pk_add_f32 v[36:37], v[36:37], v[40:41]\n"
    "v_pk_add_f32 v[38:39], v[38:39], v[42:43]\n"
    "v_permlane32_swap_b32 v44, v48\n"
    "v_permlane32_swap_b32 v45, v49\n"
    "v_permlane32_swap_b32 v46, v50\n"
    "v_permlane32_swap_b32 v47, v51\n"
    "v_cvt_pk_bf16_f32 v128, v36, v37\n"
    "v_cvt_pk_bf16_f32 v129, v38, v39\n"
    "v_pk_add_f32 v[44:45], v[44:45], v[48:49]\n"
    "v_pk_add_f32 v[46:47], v[46:47], v[50:51]\n"
    "v_cvt_pk_bf16_f32 v130, v44, v45\n"
    "v_cvt_pk_bf16_f32 v131, v46, v47\n"
    "v_mov_b64 v[88:89], v[84:85]\n"
    "v_mov_b64 v[90:91], v[86:87]\n"
    "v_permlane16_swap_b32 v128, v130\n"
    "v_permlane16_swap_b32 v129, v131\n"
    "v_min_u32 v128, -2, v128\n"
    "v_min_u32 v130, -2, v130\n"
    "buffer_store_dwordx4 v[128:131], v148, s[20:23], s89 offen sc1\n"
    "s_cbranch_scc1 ch_no5b_%=\n"
    "v_permlane32_swap_b32 v84, v88\n"
    "v_permlane32_swap_b32 v85, v89\n"
    "v_permlane32_swap_b32 v86, v90\n"
    "v_permlane32_swap_b32 v87, v91\n"
    "v_pk_add_f32 v[84:85], v[84:85], v[88:89]\n"
    "v_pk_add_f32 v[86:87], v[86:87], v[90:91]\n"
    "v_cvt_pk_bf16_f32 v124, v84, v85\n"
    "v_cvt_pk_bf16_f32 v125, v86, v87\n"
    "v_mov_b64 v[126:127], v[124:125]\n"
    "s_nop 1\n"
    "v_permlane16_swap_b32 v124, v126\n"
    "v_permlane16_swap_b32 v125, v127\n"
    "v_min_u32 v124, -2, v124\n"
    "v_min_u32 v126, -2, v126\n"
    "buffer_store_dwordx4 v[124:127], v149, s[20:23], s89 offen sc1\n"
    "ch_no5b_%=:\n"
    "s_cmp_ge_i32 s78, 0\n"
    "s_cbranch_scc1 ch_red_%=\n"
    "s_xor_b32 s42, s42, 1024\n"
    "s_cmp_lt_u32 s68, 2\n"
    "s_cbranch_scc1 ch_w1_%=\n"
    "s_add_u32 s79, s82, s60\n"
    "s_lshl_b32 s79, s79, 13\n"
    "s_add_u32 s79, s79, 0x1000000\n"
    "s_add_u32 s80, s79, 4096\n"
    "s_lshl_b32 s81, s68, 11\n"
    "s_add_u32 s81, s81, s79\n"
    "s_add_u32 s81, s81, 0x1000000\n"
    "buffer_load_dwordx4 v[60:63], v142, s[24:27], s79 offen sc1\n"
    "buffer_load_dwordx4 v[64:67], v142, s[24:27], s80 offen sc1\n"
    "buffer_load_dwordx4 v[68:71], v142, s[24:27], s79 offen offset:1024 sc1\n"
    "buffer_load_dwordx4 v[72:75], v142, s[24:27], s80 offen offset:1024 sc1\n"
    "buffer_load_dwordx4 v[76:79], v142, s[24:27], s79 offen offset:2048 sc1\n"
    "buffer_load_dwordx4 v[80:83], v142, s[24:27], s80 offen offset:2048 sc1\n"
    "buffer_load_dwordx4 v[84:87], v142, s[24:27], s79 offen offset:3072 sc1\n"
    "buffer_load_dwordx4 v[88:91], v142, s[24:27], s80 offen offset:3072 sc1\n"
    "buffer_load_dwordx4 v[92:95], v142, s[24:27], s81 offen sc1\n"
    "buffer_load_dwordx4 v[96:99], v142, s[24:27], s81 offen offset:1024 sc1\n"
    "s_and_b32 s79, s82, 3\n"
    "s_lshl_b32 s91, s79, 17\n"
    "s_lshl_b32 s80, s66, 15\n"
    "s_add_u32 s74, s91, s80\n"
    "s_add_u32 s74, s74, s70\n"
    "s_add_u32 s74, s74, 1179648\n"
    "s_add_u32 s75, s74, 4096\n"
    "s_add_u32 s76, s74, 16384\n"
    "v_mov_b64 v[36:37], 0\n"
    "v_mov_b64 v[38:39], 0\n"
    "v_mov_b64 v[40:41], 0\n"
    "v_mov_b64 v[42:43], 0\n"
    "v_mov_b64 v[44:45], 0\n"
    "v_mov_b64 v[46:47], 0\n"
    "v_mov_b64 v[48:49], 0\n"
    "v_mov_b64 v[50:51], 0\n"
    "v_mov_b64 v[52:53], 0\n"
    "v_mov_b64 v[54:55], 0\n"
    "v_mov_b64 v[56:57], 0\n"
    "v_mov_b64 v[58:59], 0\n"
    "ds_read_b128 v[100:103], v143\n"
    "ds_read_b128 v[104:107], v143 offset:1024\n"
    "ds_read_b128 v[108:111], v143 offset:2048\n"
    "ds_read_b128 v[112:115], v143 offset:3072\n"
    "s_add_u32 s69, s69, 1\n"
    "s_add_u32 s79, s69, s60\n"
    "s_lshl_b32 s79, s79, 2\n"
    "s_add_u32 s79, s79, s68\n"
    "s_lshl_b32 s79, s79, 10\n"
    "s_add_u32 s80, s79, 0x800000\n"
    "s_cmp_eq_u32 s65, 0\n"
    "s_cselect_b32 s92, s79, 0x7ff00000\n"
    "s_cselect_b32 s95, 0x7ff00000, s80\n"
    "s_add_u32 s79, s69, 1\n"
    "s_min_u32 s79, s79, 63\n"
    "s_add_u32 s79, s79, s60\n"
    "s_lshl_b32 s54, s79, 13\n"
    "v_mov_b64 v[124:125], -1\n"
    "v_mov_b64 v[126:127], -1\n"
    "s_add_u32 s43, s69, 2\n"
    "s_and_b32 s43, s43, 3\n"
    "s_lshl_b32 s43, s43, 17\n"
    "s_waitcnt vmcnt(12)\n"
    "s_add_u32 s81, s69, 1\n"
    "s_min_u32 s81, s81, 63\n"
    "s_mov_b32 s73, 0\n"
    "ch_need3_%=:\n"
    "s_cmp_lt_u32 s81, s71\n"
    "s_cbranch_scc1 ch_needok4_%=\n"
    "s_cmp_lg_u32 s72, 0\n"
    "s_cbranch_scc1 ch_needok4_%=\n"
    "buffer_load_dword v158, v153, s[20:23], 0 offen sc1\n"
    "v_bfe_u32 v159, v142, 6, 1\n"
    "s_waitcnt vmcnt(0)\n"
    "v_lshl_or_b32 v158, v158, 1, v159\n"
    "s_nop 1\n"
    "v_min_u32_dpp v158, v158, v158 row_ror:8 row_mask:0xf bank_mask:0xf\n"
    "s_nop 1\n"
    "v_min_u32_dpp v158, v158, v158 row_ror:4 row_mask:0xf bank_mask:0xf\n"
    "s_nop 1\n"
    "v_min_u32_dpp v158, v158, v158 row_ror:2 row_mask:0xf bank_mask:0xf\n"
    "s_nop 1\n"
    "v_min_u32_dpp v158, v158, v158 row_ror:1 row_mask:0xf bank_mask:0xf\n"
    "s_nop 0\n"
    "v_readfirstlane_b32 s71, v158\n"
    "s_add_u32 s73, s73, 1\n"
    "s_cmp_gt_u32 s73, 0x40000\n"
    "s_cselect_b32 s72, 1, 0\n"
    "s_branch ch_need3_%=\n"
    "ch_needok4_%=:\n"
    "s_branch ch_tail_%=\n"
    "ch_w1_%=:\n"
    "s_and_b32 s79, s82, 3\n"
    "s_lshl_b32 s91, s79, 17\n"
    "s_lshl_b32 s80, s66, 15\n"
    "s_add_u32 s74, s91, s80\n"
    "s_add_u32 s74, s74, s70\n"
    "s_add_u32 s74, s74, 1179648\n"
    "s_add_u32 s75, s74, 4096\n"
    "s_add_u32 s76, s74, 16384\n"
    "v_mov_b64 v[36:37], 0\n"
    "v_mov_b64 v[38:39], 0\n"
    "v_mov_b64 v[40:41], 0\n"
    "v_mov_b64 v[42:43], 0\n"
    "v_mov_b64 v[44:45], 0\n"
    "v_mov_b64 v[46:47], 0\n"
    "v_mov_b64 v[48:49], 0\n"
    "v_mov_b64 v[50:51], 0\n"
    "v_mov_b64 v[52:53], 0\n"
    "v_mov_b64 v[54:55], 0\n"
    "v_mov_b64 v[56:57], 0\n"
    "v_mov_b64 v[58:59], 0\n"
    "ds_read_b128 v[100:103], v143\n"
    "ds_read_b128 v[104:107], v143 offset:1024\n"
    "ds_read_b128 v[108:111], v143 offset:2048\n"
    "ds_read_b128 v[112:115], v143 offset:3072\n"
    "s_add_u32 s69, s69, 1\n"
    "s_add_u32 s79, s69, s60\n"
    "s_lshl_b32 s79, s79, 2\n"
    "s_add_u32 s79, s79, s68\n"
    "s_lshl_b32 s79, s79, 10\n"
    "s_add_u32 s80, s79, 0x800000\n"
    "s_cmp_eq_u32 s65, 0\n"
    "s_cselect_b32 s92, s79, 0x7ff00000\n"
    "s_cselect_b32 s95, 0x7ff00000, s80\n"
    "s_add_u32 s79, s69, 1\n"
    "s_min_u32 s79, s79, 63\n"
    "s_add_u32 s79, s79, s60\n"
    "s_lshl_b32 s54, s79, 13\n"
    "v_mov_b64 v[124:125], -1\n"
    "v_mov_b64 v[126:127], -1\n"
    "s_add_u32 s43, s69, 2\n"
    "s_and_b32 s43, s43, 3\n"
    "s_lshl_b32 s43, s43, 17\n"
    "s_sleep 3\n"
    "s_waitcnt vmcnt(1)\n"
    "s_add_u32 s79, s69, s60\n"
    "s_lshl_b32 s79, s79, 13\n"
    "s_add_u32 s79, s79, 0x1000000\n"
    "s_add_u32 s80, s79, 4096\n"
    "s_lshl_b32 s81, s68, 11\n"
    "s_add_u32 s81, s81, s79\n"
    "s_add_u32 s81, s81, 0x1000000\n"
    "buffer_load_dwordx4 v[60:63], v142, s[24:27], s79 offen sc1\n"
    "buffer_load_dwordx4 v[64:67], v142, s[24:27], s80 offen sc1\n"
    "buffer_load_dwordx4 v[68:71], v142, s[24:27], s79 offen offset:1024 sc1\n"
    "buffer_load_dwordx4 v[72:75], v142, s[24:27], s80 offen offset:1024 sc1\n"
    "buffer_load_dwordx4 v[76:79], v142, s[24:27], s79 offen offset:2048 sc1\n"
    "buffer_load_dwordx4 v[80:83], v142, s[24:27], s80 offen offset:2048 sc1\n"
    "buffer_load_dwordx4 v[84:87], v142, s[24:27], s79 offen offset:3072 sc1\n"
    "buffer_load_dwordx4 v[88:91], v142, s[24:27], s80 offen offset:3072 sc1\n"
    "buffer_load_dwordx4 v[92:95], v142, s[24:27], s81 offen sc1\n"
    "buffer_load_dwordx4 v[96:99], v142, s[24:27], s81 offen offset:1024 sc1\n"
    "s_add_u32 s81, s69, 1\n"
    "s_min_u32 s81, s81, 63\n"
    "s_mov_b32 s73, 0\n"
    "ch_need5_%=:\n"
    "s_cmp_lt_u32 s81, s71\n"
    "s_cbranch_scc1 ch_needok6_%=\n"
    "s_cmp_lg_u32 s72, 0\n"
    "s_cbranch_scc1 ch_needok6_%=\n"
    "buffer_load_dword v158, v153, s[20:23], 0 offen sc1\n"
    "v_bfe_u32 v159, v142, 6, 1\n"
    "s_waitcnt vmcnt(0)\n"
    "v_lshl_or_b32 v158, v158, 1, v159\n"
    "s_nop 1\n"
    "v_min_u32_dpp v158, v158, v158 row_ror:8 row_mask:0xf bank_mask:0xf\n"
    "s_nop 1\n"
    "v_min_u32_dpp v158, v158, v158 row_ror:4 row_mask:0xf bank_mask:0xf\n"
    "s_nop 1\n"
    "v_min_u32_dpp v158, v158, v158 row_ror:2 row_mask:0xf bank_mask:0xf\n"
    "s_nop 1\n"
    "v_min_u32_dpp v158, v158, v158 row_ror:1 row_mask:0xf bank_mask:0xf\n"
    "s_nop 0\n"
    "v_readfirstlane_b32 s71, v158\n"
    "s_add_u32 s73, s73, 1\n"
    "s_cmp_gt_u32 s73, 0x40000\n"
    "s_cselect_b32 s72, 1, 0\n"
    "s_branch ch_need5_%=\n"
    "ch_needok6_%=:\n"
    "s_branch ch_tail_%=\n"
    "ch_red_%=:\n"
    "s_memrealtime s[58:59]\n"
    "s_add_u32 s88, s77, s89\n"
    "s_waitcnt vmcnt(2)\n"
    "v_subrev_u32 v155, s96, v138\n"
    "v_bfe_i32 v155, v155, 0, 16\n"
    "s_nop 1\n"
    "v_max_i32_dpp v155, v155, v155 row_ror:8 row_mask:0xf bank_mask:0xf\n"
    "s_nop 1\n"
    "v_max_i32_dpp v155, v155, v155 row_ror:4 row_mask:0xf bank_mask:0xf\n"
    "s_nop 1\n"
    "v_max_i32_dpp v155, v155, v155 row_ror:2 row_mask:0xf bank_mask:0xf\n"
    "s_nop 1\n"
    "v_max_i32_dpp v155, v155, v155 row_ror:1 row_mask:0xf bank_mask:0xf\n"
    "v_mov_b32 v159, v155\n"
    "s_nop 1\n"
    "v_permlane16_swap_b32 v155, v159\n"
    "v_max_i32 v155, v155, v159\n"
    "v_mov_b32 v159, v155\n"
    "s_nop 1\n"
    "v_permlane32_swap_b32 v155, v159\n"
    "v_max_i32 v155, v155, v159\n"
    "s_nop 0\n"
    "v_readfirstlane_b32 s79, v155\n"
    "s_waitcnt lgkmcnt(0)\n"
    "s_sub_u32 s80, s58, s96\n"
    "s_add_u32 s98, s98, 2\n"
    "s_min_u32 s98, s98, s80\n"
    "s_cmp_lt_u32 s79, 400\n"
    "s_cselect_b32 s79, s79, s80\n"
    "s_add_u32 s97, s96, s79\n"
    "s_add_u32 s97, s97, s61\n"
    "s_add_u32 s96, s97, s62\n"
    "s_mov_b32 s99, s63\n"
    "s_mov_b32 s73, 0\n"
    "s_mov_b32 s40, 0x3030303\n"
    "s_mov_b32 s41, 0x3030303\n"
    "v_cndmask_b32 v32, 0, v141, s[40:41]\n"
    "s_mov_b32 s40, 0xc0c0c0c\n"
    "s_mov_b32 s41, 0xc0c0c0c\n"
    "v_cndmask_b32 v33, 0, v141, s[40:41]\n"
    "s_mov_b32 s40, 0x30303030\n"
    "s_mov_b32 s41, 0x30303030\n"
    "v_cndmask_b32 v34, 0, v141, s[40:41]\n"
    "s_mov_b32 s40, 0xc0c0c0c0\n"
    "s_mov_b32 s41, 0xc0c0c0c0\n"
    "v_cndmask_b32 v35, 0, v141, s[40:41]\n"
    "v_mov_b64 v[36:37], 0\n"
    "v_mov_b64 v[38:39], 0\n"
    "v_mov_b64 v[40:41], 0\n"
    "v_mov_b64 v[42:43], 0\n"
    "v_mov_b64 v[44:45], 0\n"
    "v_mov_b64 v[46:47], 0\n"
    "v_mov_b64 v[48:49], 0\n"
    "v_mov_b64 v[50:51], 0\n"
    "v_mov_b64 v[52:53], 0\n"
    "v_mov_b64 v[54:55], 0\n"
    "v_mov_b64 v[56:57], 0\n"
    "v_mov_b64 v[58:59], 0\n"
    "ds_read_b128 v[100:103], v143\n"
    "ds_read_b128 v[104:107], v143 offset:1024\n"
    "ds_read_b128 v[108:111], v143 offset:2048\n"
    "ds_read_b128 v[112:115], v143 offset:3072\n"
    "ch_wt7_%=:\n"
    "s_memrealtime s[58:59]\n"
    "s_waitcnt lgkmcnt(0)\n"
    "s_sub_u32 s93, s97, s58\n"
    "s_sub_u32 s93, s93, 1\n"
    "s_cmp_lt_u32 s93, 600\n"
    "s_cbranch_scc1 ch_wt7_%=\n"
    "s_add_u32 s93, s93, 4\n"
    "s_cmp_gt_u32 s93, 3\n"
    "s_cbranch_scc1 ch_wtend8_%=\n"
    "ch_wtf9_%=:\n"
    "s_sub_u32 s93, s93, 1\n"
    "s_cbranch_scc0 ch_wtf9_%=\n"
    "ch_wtend8_%=:\n"
    "buffer_load_dwordx4 v[0:3], v142, s[20:23], s88 offen sc1\n"
    "buffer_load_dwordx4 v[4:7], v142, s[20:23], s88 offen offset:1024 sc1\n"
    "buffer_load_dwordx4 v[8:11], v142, s[20:23], s88 offen offset:2048 sc1\n"
    "buffer_load_dwordx4 v[12:15], v142, s[20:23], s88 offen offset:3072 sc1\n"
    "s_and_b32 s79, s82, 3\n"
    "s_lshl_b32 s91, s79, 17\n"
    "s_lshl_b32 s80, s66, 15\n"
    "s_add_u32 s74, s91, s80\n"
    "s_add_u32 s74, s74, s70\n"
    "s_add_u32 s74, s74, 1179648\n"
    "s_add_u32 s75, s74, 4096\n"
    "s_add_u32 s76, s74, 16384\n"
    "s_add_u32 s69, s69, 1\n"
    "s_add_u32 s79, s69, s60\n"
    "s_lshl_b32 s79, s79, 2\n"
    "s_add_u32 s79, s79, s68\n"
    "s_lshl_b32 s79, s79, 10\n"
    "s_add_u32 s80, s79, 0x800000\n"
    "s_cmp_eq_u32 s65, 0\n"
    "s_cselect_b32 s92, s79, 0x7ff00000\n"
    "s_cselect_b32 s95, 0x7ff00000, s80\n"
    "s_add_u32 s79, s69, 1\n"
    "s_min_u32 s79, s79, 63\n"
    "s_add_u32 s79, s79, s60\n"
    "s_lshl_b32 s54, s79, 13\n"
    "v_mov_b64 v[124:125], -1\n"
    "v_mov_b64 v[126:127], -1\n"
    "s_add_u32 s43, s69, 2\n"
    "s_and_b32 s43, s43, 3\n"
    "s_lshl_b32 s43, s43, 17\n"
    "s_add_u32 s79, s69, s60\n"
    "s_lshl_b32 s79, s79, 13\n"
    "s_add_u32 s79, s79, 0x1000000\n"
    "s_add_u32 s80, s79, 4096\n"
    "s_lshl_b32 s81, s68, 11\n"
    "s_add_u32 s81, s81, s79\n"
    "s_add_u32 s81, s81, 0x1000000\n"
    "buffer_load_dwordx4 v[60:63], v142, s[24:27], s79 offen sc1\n"
    "buffer_load_dwordx4 v[64:67], v142, s[24:27], s80 offen sc1\n"
    "buffer_load_dwordx4 v[68:71], v142, s[24:27], s79 offen offset:1024 sc1\n"
    "buffer_load_dwordx4 v[72:75], v142, s[24:27], s80 offen offset:1024 sc1\n"
    "buffer_load_dwordx4 v[76:79], v142, s[24:27], s79 offen offset:2048 sc1\n"
    "buffer_load_dwordx4 v[80:83], v142, s[24:27], s80 offen offset:2048 sc1\n"
    "buffer_load_dwordx4 v[84:87], v142, s[24:27], s79 offen offset:3072 sc1\n"
    "buffer_load_dwordx4 v[88:91], v142, s[24:27], s80 offen offset:3072 sc1\n"
    "buffer_load_dwordx4 v[92:95], v142, s[24:27], s81 offen sc1\n"
    "buffer_load_dwordx4 v[96:99], v142, s[24:27], s81 offen offset:1024 sc1\n"
    "s_waitcnt vmcnt(13)\n"
    "v_mfma_f32_16x16x32_bf16 v[128:131], v[0:3], v[32:35], 0\n"
    "v_max_u32 v155, v0, v2\n"
    "s_waitcnt vmcnt(12)\n"
    "v_mfma_f32_16x16x32_bf16 v[128:131], v[4:7], v[32:35], v[128:131]\n"
    "v_max3_u32 v155, v155, v4, v6\n"
    "s_waitcnt vmcnt(11)\n"
    "v_mfma_f32_16x16x32_bf16 v[128:131], v[8:11], v[32:35], v[128:131]\n"
    "v_max3_u32 v155, v155, v8, v10\n"
    "s_waitcnt vmcnt(10)\n"
    "v_mfma_f32_16x16x32_bf16 v[128:131], v[12:15], v[32:35], v[128:131]\n"
    "v_max3_u32 v155, v155, v12, v14\n"
    "v_cmp_eq_u32 vcc, -1, v155\n"
    "s_cbranch_vccnz ch_pollslow_%=\n"
    "ch_pollback_%=:\n"
    "s_nop 4\n"
    "v_mov_b64 v[124:125], v[128:129]\n"
    "v_mov_b64 v[126:127], v[130:131]\n"
    "s_nop 0\n"
    "v_permlane32_swap_b32 v128, v124\n"
    "v_permlane32_swap_b32 v129, v125\n"
    "v_permlane32_swap_b32 v130, v126\n"
    "v_permlane32_swap_b32 v131, v127\n"
    "v_pk_add_f32 v[128:129], v[128:129], v[124:125]\n"
    "v_pk_add_f32 v[130:131], v[130:131], v[126:127]\n"
    "v_cvt_pk_bf16_f32 v120, v128, v129\n"
    "v_cvt_pk_bf16_f32 v121, v130, v131\n"
    "v_mov_b64 v[122:123], v[120:121]\n"
    "s_nop 1\n"
    "v_permlane16_swap_b32 v120, v122\n"
    "v_permlane16_swap_b32 v121, v123\n"
    "v_min_u32 v120, -2, v120\n"
    "v_min_u32 v122, -2, v122\n"
    "buffer_store_dwordx4 v[120:123], v150, s[20:23], s91 offen sc1\n"
    "s_memrealtime s[58:59]\n"
    "v_mov_b64 v[124:125], -1\n"
    "v_mov_b64 v[126:127], -1\n"
    "s_xor_b32 s42, s42, 1024\n"
    "s_waitcnt lgkmcnt(0)\n"
    "s_add_u32 s79, s58, s99\n"
    "s_sub_u32 s80, s79, s96\n"
    "s_cmp_gt_i32 s80, 0\n"
    "s_cselect_b32 s96, s79, s96\n"
    "s_add_u32 s79, s96, s98\n"
    "s_pack_ll_b32_b16 s79, s79, s58\n"
    "v_mov_b32 v155, s79\n"
    "v_mov_b32 v158, s90\n"
    "buffer_store_dword v155, v158, s[20:23], s42 offen sc1\n"
    "buffer_store_dwordx4 v[124:127], v142, s[20:23], s88 offen sc1\n"
    "buffer_store_dwordx4 v[124:127], v142, s[20:23], s88 offen offset:1024 sc1\n"
    "buffer_store_dwordx4 v[124:127], v142, s[20:23], s88 offen offset:2048 sc1\n"
    "buffer_store_dwordx4 v[124:127], v142, s[20:23], s88 offen offset:3072 sc1\n"
    "buffer_store_dwordx4 v[124:127], v150, s[20:23], s43 offen sc1\n"
    "s_add_u32 s81, s69, 1\n"
    "s_min_u32 s81, s81, 63\n"
    "s_mov_b32 s73, 0\n"
    "ch_need10_%=:\n"
    "s_cmp_lt_u32 s81, s71\n"
    "s_cbranch_scc1 ch_needok11_%=\n"
    "s_cmp_lg_u32 s72, 0\n"
    "s_cbranch_scc1 ch_needok11_%=\n"
    "buffer_load_dword v158, v153, s[20:23], 0 offen sc1\n"
    "v_bfe_u32 v159, v142, 6, 1\n"
    "s_waitcnt vmcnt(0)\n"
    "v_lshl_or_b32 v158, v158, 1, v159\n"
    "s_nop 1\n"
    "v_min_u32_dpp v158, v158, v158 row_ror:8 row_mask:0xf bank_mask:0xf\n"
    "s_nop 1\n"
    "v_min_u32_dpp v158, v158, v158 row_ror:4 row_mask:0xf bank_mask:0xf\n"
    "s_nop 1\n"
    "v_min_u32_dpp v158, v158, v158 row_ror:2 row_mask:0xf bank_mask:0xf\n"
    "s_nop 1\n"
    "v_min_u32_dpp v158, v158, v158 row_ror:1 row_mask:0xf bank_mask:0xf\n"
    "s_nop 0\n"
    "v_readfirstlane_b32 s71, v158\n"
    "s_add_u32 s73, s73, 1\n"
    "s_cmp_gt_u32 s73, 0x40000\n"
    "s_cselect_b32 s72, 1, 0\n"
    "s_branch ch_need10_%=\n"
    "ch_needok11_%=:\n"
    "ch_wt12_%=:\n"
    "s_memrealtime s[58:59]\n"
    "s_waitcnt lgkmcnt(0)\n"
    "s_sub_u32 s93, s96, s58\n"
    "s_sub_u32 s93, s93, 1\n"
    "s_cmp_lt_u32 s93, 600\n"
    "s_cbranch_scc1 ch_wt12_%=\n"
    "s_add_u32 s93, s93, 4\n"
    "s_cmp_gt_u32 s93, 3\n"
    "s_cbranch_scc1 ch_wtend13_%=\n"
    "ch_wtf14_%=:\n"
    "s_sub_u32 s93, s93, 1\n"
    "s_cbranch_scc0 ch_wtf14_%=\n"
    "ch_wtend13_%=:\n"
    "ch_tail_%=:\n"
    "s_waitcnt lgkmcnt(0)\n"
    "s_barrier\n"
    "s_branch ch_step_%=\n"
    "ch_slowc_0_%=:\n"
    "s_cmp_lg_u32 s72, 0\n"
    "s_cbranch_scc1 ch_backc_0_%=\n"
    "s_mov_b32 s73, 0\n"
    "ch_slowlc_0_%=:\n"
    "buffer_load_dwordx4 v[0:3], v142, s[20:23], s74 offen sc1\n"
    "buffer_load_dwordx4 v[4:7], v142, s[20:23], s74 offen offset:1024 sc1\n"
    "buffer_load_dwordx4 v[8:11], v142, s[20:23], s74 offen offset:2048 sc1\n"
    "buffer_load_dwordx4 v[12:15], v142, s[20:23], s74 offen offset:3072 sc1\n"
    "buffer_load_dwordx4 v[16:19], v142, s[20:23], s75 offen sc1\n"
    "buffer_load_dwordx4 v[20:23], v142, s[20:23], s75 offen offset:1024 sc1\n"
    "buffer_load_dwordx4 v[24:27], v142, s[20:23], s75 offen offset:2048 sc1\n"
    "buffer_load_dwordx4 v[28:31], v142, s[20:23], s75 offen offset:3072 sc1\n"
    "buffer_load_dwordx4 v[32:35], v142, s[20:23], s76 offen sc1\n"
    "v_mov_b32 v158, s69\n"
    "buffer_store_dword v158, v152, s[20:23], s70 offen sc1\n"
    "s_add_u32 s73, s73, 1\n"
    "s_cmp_gt_u32 s73, 0x40000\n"
    "s_cselect_b32 s72, 1, 0\n"
    "s_waitcnt vmcnt(9)\n"
    "s_cbranch_scc1 ch_backc_0_%=\n"
    "v_max_u32 v155, v0, v2\n"
    "v_cmp_eq_u32 vcc, -1, v155\n"
    "s_cbranch_vccnz ch_slowlc_0_%=\n"
    "s_branch ch_backc_0_%=\n"
    "ch_slowc_1_%=:\n"
    "s_cmp_lg_u32 s72, 0\n"
    "s_cbranch_scc1 ch_backc_1_%=\n"
    "s_mov_b32 s73, 0\n"
    "ch_slowlc_1_%=:\n"
    "buffer_load_dwordx4 v[4:7], v142, s[20:23], s74 offen offset:1024 sc1\n"
    "buffer_load_dwordx4 v[8:11], v142, s[20:23], s74 offen offset:2048 sc1\n"
    "buffer_load_dwordx4 v[12:15], v142, s[20:23], s74 offen offset:3072 sc1\n"
    "buffer_load_dwordx4 v[16:19], v142, s[20:23], s75 offen sc1\n"
    "buffer_load_dwordx4 v[20:23], v142, s[20:23], s75 offen offset:1024 sc1\n"
    "buffer_load_dwordx4 v[24:27], v142, s[20:23], s75 offen offset:2048 sc1\n"
    "buffer_load_dwordx4 v[28:31], v142, s[20:23], s75 offen offset:3072 sc1\n"
    "buffer_load_dwordx4 v[32:35], v142, s[20:23], s76 offen sc1\n"
    "v_mov_b32 v158, s69\n"
    "buffer_store_dword v158, v152, s[20:23], s70 offen sc1\n"
    "s_add_u32 s73, s73, 1\n"
    "s_cmp_gt_u32 s73, 0x40000\n"
    "s_cselect_b32 s72, 1, 0\n"
    "s_waitcnt vmcnt(8)\n"
    "s_cbranch_scc1 ch_backc_1_%=\n"
    "v_max_u32 v155, v4, v6\n"
    "v_cmp_eq_u32 vcc, -1, v155\n"
    "s_cbranch_vccnz ch_slowlc_1_%=\n"
    "s_branch ch_backc_1_%=\n"
    "ch_slowc_2_%=:\n"
    "s_cmp_lg_u32 s72, 0\n"
    "s_cbranch_scc1 ch_backc_2_%=\n"
    "s_mov_b32 s73, 0\n"
    "ch_slowlc_2_%=:\n"
    "buffer_load_dwordx4 v[8:11], v142, s[20:23], s74 offen offset:2048 sc1\n"
    "buffer_load_dwordx4 v[12:15], v142, s[20:23], s74 offen offset:3072 sc1\n"
    "buffer_load_dwordx4 v[16:19], v142, s[20:23], s75 offen sc1\n"
    "buffer_load_dwordx4 v[20:23], v142, s[20:23], s75 offen offset:1024 sc1\n"
    "buffer_load_dwordx4 v[24:27], v142, s[20:23], s75 offen offset:2048 sc1\n"
    "buffer_load_dwordx4 v[28:31], v142, s[20:23], s75 offen offset:3072 sc1\n"
    "buffer_load_dwordx4 v[32:35], v142, s[20:23], s76 offen sc1\n"
    "v_mov_b32 v158, s69\n"
    "buffer_store_dword v158, v152, s[20:23], s70 offen sc1\n"
    "s_add_u32 s73, s73, 1\n"
    "s_cmp_gt_u32 s73, 0x40000\n"
    "s_cselect_b32 s72, 1, 0\n"
    "s_waitcnt vmcnt(7)\n"
    "s_cbranch_scc1 ch_backc_2_%=\n"
    "v_max_u32 v155, v8, v10\n"
    "v_cmp_eq_u32 vcc, -1, v155\n"
    "s_cbranch_vccnz ch_slowlc_2_%=\n"
    "s_branch ch_backc_2_%=\n"
    "ch_slowc_3_%=:\n"
    "s_cmp_lg_u32 s72, 0\n"
    "s_cbranch_scc1 ch_backc_3_%=\n"
    "s_mov_b32 s73, 0\n"
    "ch_slowlc_3_%=:\n"
    "buffer_load_dwordx4 v[12:15], v142, s[20:23], s74 offen offset:3072 sc1\n"
    "buffer_load_dwordx4 v[16:19], v142, s[20:23], s75 offen sc1\n"
    "buffer_load_dwordx4 v[20:23], v142, s[20:23], s75 offen offset:1024 sc1\n"
    "buffer_load_dwordx4 v[24:27], v142, s[20:23], s75 offen offset:2048 sc1\n"
    "buffer_load_dwordx4 v[28:31], v142, s[20:23], s75 offen offset:3072 sc1\n"
    "buffer_load_dwordx4 v[32:35], v142, s[20:23], s76 offen sc1\n"
    "v_mov_b32 v158, s69\n"
    "buffer_store_dword v158, v152, s[20:23], s70 offen sc1\n"
    "s_add_u32 s73, s73, 1\n"
    "s_cmp_gt_u32 s73, 0x40000\n"
    "s_cselect_b32 s72, 1, 0\n"
    "s_waitcnt vmcnt(6)\n"
    "s_cbranch_scc1 ch_backc_3_%=\n"
    "v_max_u32 v155, v12, v14\n"
    "v_cmp_eq_u32 vcc, -1, v155\n"
    "s_cbranch_vccnz ch_slowlc_3_%=\n"
    "s_branch ch_backc_3_%=\n"
    "ch_slowc_4_%=:\n"
    "s_cmp_lg_u32 s72, 0\n"
    "s_cbranch_scc1 ch_backc_4_%=\n"
    "s_mov_b32 s73, 0\n"
    "ch_slowlc_4_%=:\n"
    "buffer_load_dwordx4 v[16:19], v142, s[20:23], s75 offen sc1\n"
    "buffer_load_dwordx4 v[20:23], v142, s[20:23], s75 offen offset:1024 sc1\n"
    "buffer_load_dwordx4 v[24:27], v142, s[20:23], s75 offen offset:2048 sc1\n"
    "buffer_load_dwordx4 v[28:31], v142, s[20:23], s75 offen offset:3072 sc1\n"
    "buffer_load_dwordx4 v[32:35], v142, s[20:23], s76 offen sc1\n"
    "v_mov_b32 v158, s69\n"
    "buffer_store_dword v158, v152, s[20:23], s70 offen sc1\n"
    "s_add_u32 s73, s73, 1\n"
    "s_cmp_gt_u32 s73, 0x40000\n"
    "s_cselect_b32 s72, 1, 0\n"
    "s_waitcnt vmcnt(5)\n"
    "s_cbranch_scc1 ch_backc_4_%=\n"
    "v_max_u32 v155, v16, v18\n"
    "v_cmp_eq_u32 vcc, -1, v155\n"
    "s_cbranch_vccnz ch_slowlc_4_%=\n"
    "s_branch ch_backc_4_%=\n"
    "ch_slowc_5_%=:\n"
    "s_cmp_lg_u32 s72, 0\n"
    "s_cbranch_scc1 ch_backc_5_%=\n"
    "s_mov_b32 s73, 0\n"
    "ch_slowlc_5_%=:\n"
    "buffer_load_dwordx4 v[20:23], v142, s[20:23], s75 offen offset:1024 sc1\n"
    "buffer_load_dwordx4 v[24:27], v142, s[20:23], s75 offen offset:2048 sc1\n"
    "buffer_load_dwordx4 v[28:31], v142, s[20:23], s75 offen offset:3072 sc1\n"
    "buffer_load_dwordx4 v[32:35], v142, s[20:23], s76 offen sc1\n"
    "v_mov_b32 v158, s69\n"
    "buffer_store_dword v158, v152, s[20:23], s70 offen sc1\n"
    "s_add_u32 s73, s73, 1\n"
    "s_cmp_gt_u32 s73, 0x40000\n"
    "s_cselect_b32 s72, 1, 0\n"
    "s_waitcnt vmcnt(4)\n"
    "s_cbranch_scc1 ch_backc_5_%=\n"
    "v_max_u32 v155, v20, v22\n"
    "v_cmp_eq_u32 vcc, -1, v155\n"
    "s_cbranch_vccnz ch_slowlc_5_%=\n"
    "s_branch ch_backc_5_%=\n"
    "ch_slowc_6_%=:\n"
    "s_cmp_lg_u32 s72, 0\n"
    "s_cbranch_scc1 ch_backc_6_%=\n"
    "s_mov_b32 s73, 0\n"
    "ch_slowlc_6_%=:\n"
    "buffer_load_dwordx4 v[24:27], v142, s[20:23], s75 offen offset:2048 sc1\n"
    "buffer_load_dwordx4 v[28:31], v142, s[20:23], s75 offen offset:3072 sc1\n"
    "buffer_load_dwordx4 v[32:35], v142, s[20:23], s76 offen sc1\n"
    "v_mov_b32 v158, s69\n"
    "buffer_store_dword v158, v152, s[20:23], s70 offen sc1\n"
    "s_add_u32 s73, s73, 1\n"
    "s_cmp_gt_u32 s73, 0x40000\n"
    "s_cselect_b32 s72, 1, 0\n"
    "s_waitcnt vmcnt(3)\n"
    "s_cbranch_scc1 ch_backc_6_%=\n"
    "v_max_u32 v155, v24, v26\n"
    "v_cmp_eq_u32 vcc, -1, v155\n"
    "s_cbranch_vccnz ch_slowlc_6_%=\n"
    "s_branch ch_backc_6_%=\n"
    "ch_slowc_7_%=:\n"
    "s_cmp_lg_u32 s72, 0\n"
    "s_cbranch_scc1 ch_backc_7_%=\n"
    "s_mov_b32 s73, 0\n"
    "ch_slowlc_7_%=:\n"
    "buffer_load_dwordx4 v[28:31], v142, s[20:23], s75 offen offset:3072 sc1\n"
    "buffer_load_dwordx4 v[32:35], v142, s[20:23], s76 offen sc1\n"
    "v_mov_b32 v158, s69\n"
    "buffer_store_dword v158, v152, s[20:23], s70 offen sc1\n"
    "s_add_u32 s73, s73, 1\n"
    "s_cmp_gt_u32 s73, 0x40000\n"
    "s_cselect_b32 s72, 1, 0\n"
    "s_waitcnt vmcnt(2)\n"
    "s_cbranch_scc1 ch_backc_7_%=\n"
    "v_max_u32 v155, v28, v30\n"
    "v_cmp_eq_u32 vcc, -1, v155\n"
    "s_cbranch_vccnz ch_slowlc_7_%=\n"
    "s_branch ch_backc_7_%=\n"
    "ch_slowc_8_%=:\n"
    "s_cmp_lg_u32 s72, 0\n"
    "s_cbranch_scc1 ch_backc_8_%=\n"
    "s_mov_b32 s73, 0\n"
    "ch_slowlc_8_%=:\n"
    "buffer_load_dwordx4 v[32:35], v142, s[20:23], s76 offen sc1\n"
    "v_mov_b32 v158, s69\n"
    "buffer_store_dword v158, v152, s[20:23], s70 offen sc1\n"
    "s_add_u32 s73, s73, 1\n"
    "s_cmp_gt_u32 s73, 0x40000\n"
    "s_cselect_b32 s72, 1, 0\n"
    "s_waitcnt vmcnt(1)\n"
    "s_cbranch_scc1 ch_backc_8_%=\n"
    "v_max_u32 v155, v32, v34\n"
    "v_cmp_eq_u32 vcc, -1, v155\n"
    "s_cbranch_vccnz ch_slowlc_8_%=\n"
    "s_branch ch_backc_8_%=\n"
    "ch_pollslow_%=:\n"
    "s_add_u32 s99, s63, 28\n"
    "buffer_load_dwordx4 v[0:3], v142, s[20:23], s88 offen sc1\n"
    "buffer_load_dwordx4 v[4:7], v142, s[20:23], s88 offen offset:1024 sc1\n"
    "buffer_load_dwordx4 v[8:11], v142, s[20:23], s88 offen offset:2048 sc1\n"
    "buffer_load_dwordx4 v[12:15], v142, s[20:23], s88 offen offset:3072 sc1\n"
    "ch_poll_%=:\n"
    "s_sleep 2\n"
    "buffer_load_dwordx4 v[16:19], v142, s[20:23], s88 offen sc1\n"
    "buffer_load_dwordx4 v[20:23], v142, s[20:23], s88 offen offset:1024 sc1\n"
    "buffer_load_dwordx4 v[24:27], v142, s[20:23], s88 offen offset:2048 sc1\n"
    "buffer_load_dwordx4 v[28:31], v142, s[20:23], s88 offen offset:3072 sc1\n"
    "s_waitcnt vmcnt(7)\n"
    "v_mfma_f32_16x16x32_bf16 v[128:131], v[0:3], v[32:35], 0\n"
    "v_max_u32 v155, v0, v2\n"
    "s_waitcnt vmcnt(6)\n"
    "v_mfma_f32_16x16x32_bf16 v[128:131], v[4:7], v[32:35], v[128:131]\n"
    "v_max3_u32 v155, v155, v4, v6\n"
    "s_waitcnt vmcnt(5)\n"
    "v_mfma_f32_16x16x32_bf16 v[128:131], v[8:11], v[32:35], v[128:131]\n"
    "v_max3_u32 v155, v155, v8, v10\n"
    "s_waitcnt vmcnt(4)\n"
    "v_mfma_f32_16x16x32_bf16 v[128:131], v[12:15], v[32:35], v[128:131]\n"
    "v_max3_u32 v155, v155, v12, v14\n"
    "v_cmp_eq_u32 vcc, -1, v155\n"
    "s_nop 7\n"
    "s_cbranch_vccz ch_pollback_%=\n"
    "s_sleep 2\n"
    "buffer_load_dwordx4 v[0:3], v142, s[20:23], s88 offen sc1\n"
    "buffer_load_dwordx4 v[4:7], v142, s[20:23], s88 offen offset:1024 sc1\n"
    "buffer_load_dwordx4 v[8:11], v142, s[20:23], s88 offen offset:2048 sc1\n"
    "buffer_load_dwordx4 v[12:15], v142, s[20:23], s88 offen offset:3072 sc1\n"
    "s_waitcnt vmcnt(7)\n"
    "v_mfma_f32_16x16x32_bf16 v[128:131], v[16:19], v[32:35], 0\n"
    "v_max_u32 v155, v16, v18\n"
    "s_waitcnt vmcnt(6)\n"
    "v_mfma_f32_16x16x32_bf16 v[128:131], v[20:23], v[32:35], v[128:131]\n"
    "v_max3_u32 v155, v155, v20, v22\n"
    "s_waitcnt vmcnt(5)\n"
    "v_mfma_f32_16x16x32_bf16 v[128:131], v[24:27], v[32:35], v[128:131]\n"
    "v_max3_u32 v155, v155, v24, v26\n"
    "s_waitcnt vmcnt(4)\n"
    "v_mfma_f32_16x16x32_bf16 v[128:131], v[28:31], v[32:35], v[128:131]\n"
    "v_max3_u32 v155, v155, v28, v30\n"
    "v_cmp_eq_u32 vcc, -1, v155\n"
    "s_nop 7\n"
    "s_cbranch_vccz ch_pollback_%=\n"
    "s_add_u32 s73, s73, 1\n"
    "s_cmp_gt_u32 s73, 0x40000\n"
    "s_cselect_b32 s79, 1, 0\n"
    "s_or_b32 s72, s72, s79\n"
    "s_cbranch_scc1 ch_pollback_%=\n"
    "s_branch ch_poll_%=\n"
    "ch_last_%=:\n"
    "v_smfmac_f32_16x16x64_bf16 v[52:55], v[16:19], a[248:255], v151\n"
    "s_waitcnt lgkmcnt(6)\n"
    "v_smfmac_f32_16x16x64_bf16 v[56:59], v[16:19], v[68:75], v151\n"
    "v_smfmac_f32_16x16x64_bf16 v[52:55], v[20:23], v[160:167], v151\n"
    "s_waitcnt lgkmcnt(4)\n"
    "v_smfmac_f32_16x16x64_bf16 v[56:59], v[20:23], v[76:83], v151\n"
    "v_smfmac_f32_16x16x64_bf16 v[52:55], v[24:27], v[168:175], v151\n"
    "s_waitcnt lgkmcnt(2)\n"
    "v_smfmac_f32_16x16x64_bf16 v[56:59], v[24:27], v[84:91], v151\n"
    "v_smfmac_f32_16x16x64_bf16 v[52:55], v[28:31], v[176:183], v151\n"
    "v_smfmac_f32_16x16x64_bf16 v[56:59], v[28:31], v[108:115], v151\n"
    "v_smfmac_f32_16x16x64_bf16 v[52:55], v[32:35], v[184:191], v151\n"
    "s_waitcnt lgkmcnt(0)\n"
    "v_smfmac_f32_16x16x64_bf16 v[56:59], v[32:35], v[100:107], v151\n"
    "ds_read_b64_tr_b16 v[108:109], v154 offset:2048\n"
    "ds_read_b64_tr_b16 v[110:111], v154 offset:2304\n"
    "v_lshrrev_b32 v159, 1, v142\n"
    "s_nop 4\n"
    "v_permlane32_swap_b32 v52, v56\n"
    "v_permlane32_swap_b32 v53, v57\n"
    "v_permlane32_swap_b32 v54, v58\n"
    "v_permlane32_swap_b32 v55, v59\n"
    "v_pk_add_f32 v[52:53], v[52:53], v[56:57]\n"
    "v_pk_add_f32 v[54:55], v[54:55], v[58:59]\n"
    "v_cvt_pk_bf16_f32 v156, v52, v53\n"
    "v_cvt_pk_bf16_f32 v157, v54, v55\n"
    "buffer_store_dwordx2 v[156:157], v159, s[28:31], s95 offen sc1\n"
    "s_waitcnt lgkmcnt(0)\n"
    "buffer_store_dwordx4 v[108:111], v142, s[28:31], s92 offen sc1\n"
    "s_waitcnt vmcnt(0)\n"
    "s_barrier\n"
    "v_mov_b32 v158, 64\n"
    "buffer_store_dword v158, v152, s[20:23], s70 offen sc1\n"
    "s_waitcnt vmcnt(0)\n"
    :: "s"(__builtin_amdgcn_kernarg_segment_ptr()), "s"(ids)
    : "v0", "v1", "v2", "v3", "v4", "v5", "v6", "v7", "v8", "v9", "v10", "v11", "v12", "v13", "v14", "v15", "v16", "v17", "v18", "v19", "v20", "v21", "v22",
      "v23", "v24", "v25", "v26", "v27", "v28", "v29", "v30", "v31", "v32", "v33", "v34", "v35", "v36", "v37", "v38", "v39", "v40", "v41", "v42", "v43",
      "v44", "v45", "v46", "v47", "v48", "v49", "v50", "v51", "v52", "v53", "v54", "v55", "v56", "v57", "v58", "v59", "v60", "v61", "v62", "v63", "v64",
      "v65", "v66", "v67", "v68", "v69", "v70", "v71", "v72", "v73", "v74", "v75", "v76", "v77", "v78", "v79", "v80", "v81", "v82", "v83", "v84", "v85",
      "v86", "v87", "v88", "v89", "v90", "v91", "v92", "v93", "v94", "v95", "v96", "v97", "v98", "v99", "v100", "v101", "v102", "v103", "v104", "v105",
      "v106", "v107", "v108", "v109", "v110", "v111", "v112", "v113", "v114", "v115", "v116", "v117", "v118", "v119", "v120", "v121", "v122", "v123", "v124",
      "v125", "v126", "v127", "v128", "v129", "v130", "v131", "v132", "v133", "v134", "v135", "v136", "v137", "v138", "v139", "v140", "v141", "v142", "v143",
      "v144", "v145", "v146", "v147", "v148", "v149", "v150", "v151", "v152", "v153", "v154", "v155", "v156", "v157", "v158", "v159", "v160", "v161", "v162",
      "v163", "v164", "v165", "v166", "v167", "v168", "v169", "v170", "v171", "v172", "v173", "v174", "v175", "v176", "v177", "v178", "v179", "v180", "v181",
      "v182", "v183", "v184", "v185", "v186", "v187", "v188", "v189", "v190", "v191", "v192", "v193", "v194", "v195", "v196", "v197", "v198", "v199", "v200",
      "v201", "v202", "v203", "v204", "v205", "v206", "v207", "v208", "v209", "v210", "v211", "v212", "v213", "v214", "v215", "v216", "v217", "v218", "v219",
      "v220", "v221", "v222", "v223", "v224", "v225", "v226", "v227", "v228", "v229", "v230", "v231", "v232", "v233", "v234", "v235", "v236", "v237", "v238",
      "v239", "v240", "v241", "v242", "v243", "v244", "v245", "v246", "v247", "v248", "v249", "v250", "v251", "v252", "v253", "v254", "v255", "a0", "a1",
      "a2", "a3", "a4", "a5", "a6", "a7", "a8", "a9", "a10", "a11", "a12", "a13", "a14", "a15", "a16", "a17", "a18", "a19", "a20", "a21", "a22", "a23",
      "a24", "a25", "a26", "a27", "a28", "a29", "a30", "a31", "a32", "a33", "a34", "a35", "a36", "a37", "a38", "a39", "a40", "a41", "a42", "a43", "a44",
      "a45", "a46", "a47", "a48", "a49", "a50", "a51", "a52", "a53", "a54", "a55", "a56", "a57", "a58", "a59", "a60", "a61", "a62", "a63", "a64", "a65",
      "a66", "a67", "a68", "a69", "a70", "a71", "a72", "a73", "a74", "a75", "a76", "a77", "a78", "a79", "a80", "a81", "a82", "a83", "a84", "a85", "a86",
      "a87", "a88", "a89", "a90", "a91", "a92", "a93", "a94", "a95", "a96", "a97", "a98", "a99", "a100", "a101", "a102", "a103", "a104", "a105", "a106",
      "a107", "a108", "a109", "a110", "a111", "a112", "a113", "a114", "a115", "a116", "a117", "a118", "a119", "a120", "a121", "a122", "a123", "a124", "a125",
      "a126", "a127", "a128", "a129", "a130", "a131", "a132", "a133", "a134", "a135", "a136", "a137", "a138", "a139", "a140", "a141", "a142", "a143", "a144",
      "a145", "a146", "a147", "a148", "a149", "a150", "a151", "a152", "a153", "a154", "a155", "a156", "a157", "a158", "a159", "a160", "a161", "a162", "a163",
      "a164", "a165", "a166", "a167", "a168", "a169", "a170", "a171", "a172", "a173", "a174", "a175", "a176", "a177", "a178", "a179", "a180", "a181", "a182",
      "a183", "a184", "a185", "a186", "a187", "a188", "a189", "a190", "a191", "a192", "a193", "a194", "a195", "a196", "a197", "a198", "a199", "a200", "a201",
      "a202", "a203", "a204", "a205", "a206", "a207", "a208", "a209", "a210", "a211", "a212", "a213", "a214", "a215", "a216", "a217", "a218", "a219", "a220",
      "a221", "a222", "a223", "a224", "a225", "a226", "a227", "a228", "a229", "a230", "a231", "a232", "a233", "a234", "a235", "a236", "a237", "a238", "a239",
      "a240", "a241", "a242", "a243", "a244", "a245", "a246", "a247", "a248", "a249", "a250", "a251", "a252", "a253", "a254", "a255", "s20", "s21", "s22",
      "s23", "s24", "s25", "s26", "s27", "s28", "s29", "s30", "s31", "s36", "s37", "s38", "s39", "s40", "s41", "s42", "s43", "s44", "s45", "s46", "s47",
      "s48", "s49", "s50", "s51", "s52", "s53", "s54", "s55", "s56", "s57", "s58", "s59", "s60", "s61", "s62", "s63", "s64", "s65", "s66", "s67", "s68",
      "s69", "s70", "s71", "s72", "s73", "s74", "s75", "s76", "s77", "s78", "s79", "s80", "s81", "s82", "s83", "s84", "s85", "s86", "s87", "s88", "s89",
      "s90", "s91", "s92", "s93", "s94", "s95", "s96", "s97", "s98", "s99", "vcc", "scc", "memory");
}

DEV void role_proj(u32 ids) {
    asm volatile(
    "s_nop 4\n"
    "s_load_dwordx16 s[40:55], %0, 0x0\n"
    "s_load_dwordx4 s[56:59], %0, 0x40\n"
    "s_load_dwordx2 s[60:61], %0, 0x50\n"
    "s_lshr_b32 s71, %1, 24\n"
    "s_mov_b32 s70, 0\n"
    "s_waitcnt lgkmcnt(0)\n"
    "s_load_dword s42, %0, 0x6c\n"
    "s_waitcnt lgkmcnt(0)\n"
    "s_add_u32 s20, s60, 0\n"
    "s_addc_u32 s21, s61, 0\n"
    "s_and_b32 s21, s21, 0xffff\n"
    "s_mov_b32 s22, 0x300000\n"
    "s_mov_b32 s23, 0x20000\n"
    "s_and_b32 s73, %1, 0xff\n"
    "s_bfe_u32 s72, %1, 0x40008\n"
    "s_bfe_u32 s74, %1, 0x4000c\n"
    "s_cmp_eq_u32 s72, 1\n"
    "s_cselect_b64 s[44:45], s[46:47], s[44:45]\n"
    "s_cmp_eq_u32 s72, 2\n"
    "s_cselect_b64 s[44:45], s[48:49], s[44:45]\n"
    "v_mbcnt_lo_u32_b32 v80, -1, 0\n"
    "v_mbcnt_hi_u32_b32 v80, -1, v80\n"
    "s_lshr_b32 s66, s71, 1\n"
    "v_lshrrev_b32 v85, 3, v80\n"
    "v_and_b32 v86, 6, v85\n"
    "v_and_b32 v81, 7, v80\n"
    "v_xor_b32 v81, v81, v86\n"
    "s_mul_i32 s65, s66, 6\n"
    "v_xor_b32 v81, s65, v81\n"
    "v_lshlrev_b32 v81, 4, v81\n"
    "s_mov_b32 s65, 8192\n"
    "v_mad_u32_u24 v81, v85, s65, v81\n"
    "s_lshl_b32 s67, s73, 8\n"
    "s_add_u32 s36, s44, 0\n"
    "s_addc_u32 s37, s45, 0\n"
    "s_and_b32 s37, s37, 0xffff\n"
    "s_mov_b32 s38, 0x480000\n"
    "s_mov_b32 s39, 0x20000\n"
    "s_lshr_b32 s64, s71, 1\n"
    "s_add_u32 s64, s64, 0\n"
    "s_mul_i32 s64, s64, 65536\n"
    "s_and_b32 s65, s71, 1\n"
    "s_lshl_b32 s65, s65, 7\n"
    "s_add_u32 s64, s64, s65\n"
    "s_add_u32 s64, s64, s67\n"
    "s_lshl_b32 s65, s71, 10\n"
    "s_add_u32 m0, s65, 0\n"
    "s_nop 0\n"
    "buffer_load_dwordx4 v81, s[36:39], s64 offen lds\n"
    "s_add_u32 s64, s64, 131072\n"
    "s_add_u32 m0, m0, 4096\n"
    "s_nop 0\n"
    "buffer_load_dwordx4 v81, s[36:39], s64 offen lds\n"
    "s_add_u32 s64, s64, 131072\n"
    "s_add_u32 m0, m0, 4096\n"
    "s_nop 0\n"
    "buffer_load_dwordx4 v81, s[36:39], s64 offen lds\n"
    "s_add_u32 s64, s64, 131072\n"
    "s_add_u32 m0, m0, 4096\n"
    "s_nop 0\n"
    "buffer_load_dwordx4 v81, s[36:39], s64 offen lds\n"
    "s_add_u32 s64, s64, 131072\n"
    "s_add_u32 m0, m0, 4096\n"
    "s_nop 0\n"
    "buffer_load_dwordx4 v81, s[36:39], s64 offen lds\n"
    "s_add_u32 s64, s64, 131072\n"
    "s_add_u32 m0, m0, 4096\n"
    "s_nop 0\n"
    "buffer_load_dwordx4 v81, s[36:39], s64 offen lds\n"
    "s_add_u32 s64, s64, 131072\n"
    "s_add_u32 m0, m0, 4096\n"
    "s_nop 0\n"
    "buffer_load_dwordx4 v81, s[36:39], s64 offen lds\n"
    "s_add_u32 s64, s64, 131072\n"
    "s_add_u32 m0, m0, 4096\n"
    "s_nop 0\n"
    "buffer_load_dwordx4 v81, s[36:39], s64 offen lds\n"
    "s_add_u32 s64, s64, 131072\n"
    "s_add_u32 m0, m0, 4096\n"
    "s_nop 0\n"
    "buffer_load_dwordx4 v81, s[36:39], s64 offen lds\n"
    "s_add_u32 s64, s64, 131072\n"
    "s_add_u32 m0, m0, 4096\n"
    "s_nop 0\n"
    "buffer_load_dwordx4 v81, s[36:39], s64 offen lds\n"
    "s_add_u32 s64, s64, 131072\n"
    "s_add_u32 m0, m0, 4096\n"
    "s_nop 0\n"
    "buffer_load_dwordx4 v81, s[36:39], s64 offen lds\n"
    "s_add_u32 s64, s64, 131072\n"
    "s_add_u32 m0, m0, 4096\n"
    "s_nop 0\n"
    "buffer_load_dwordx4 v81, s[36:39], s64 offen lds\n"
    "s_add_u32 s64, s64, 131072\n"
    "s_add_u32 m0, m0, 4096\n"
    "s_nop 0\n"
    "buffer_load_dwordx4 v81, s[36:39], s64 offen lds\n"
    "s_add_u32 s64, s64, 131072\n"
    "s_add_u32 m0, m0, 4096\n"
    "s_nop 0\n"
    "buffer_load_dwordx4 v81, s[36:39], s64 offen lds\n"
    "s_add_u32 s64, s64, 131072\n"
    "s_add_u32 m0, m0, 4096\n"
    "s_nop 0\n"
    "buffer_load_dwordx4 v81, s[36:39], s64 offen lds\n"
    "s_add_u32 s64, s64, 131072\n"
    "s_add_u32 m0, m0, 4096\n"
    "s_nop 0\n"
    "buffer_load_dwordx4 v81, s[36:39], s64 offen lds\n"
    "s_add_u32 s64, s64, 131072\n"
    "s_add_u32 m0, m0, 4096\n"
    "s_nop 0\n"
    "buffer_load_dwordx4 v81, s[36:39], s64 offen lds\n"
    "s_add_u32 s64, s64, 131072\n"
    "s_add_u32 m0, m0, 4096\n"
    "s_nop 0\n"
    "buffer_load_dwordx4 v81, s[36:39], s64 offen lds\n"
    "s_add_u32 s64, s64, 131072\n"
    "s_add_u32 m0, m0, 4096\n"
    "s_nop 0\n"
    "buffer_load_dwordx4 v81, s[36:39], s64 offen lds\n"
    "s_add_u32 s64, s64, 131072\n"
    "s_add_u32 m0, m0, 4096\n"
    "s_nop 0\n"
    "buffer_load_dwordx4 v81, s[36:39], s64 offen lds\n"
    "s_add_u32 s64, s64, 131072\n"
    "s_add_u32 m0, m0, 4096\n"
    "s_add_u32 s36, s44, 0\n"
    "s_addc_u32 s37, s45, 0\n"
    "s_and_b32 s37, s37, 0xffff\n"
    "s_mov_b32 s38, 0x480000\n"
    "s_mov_b32 s39, 0x20000\n"
    "s_lshr_b32 s64, s71, 1\n"
    "s_add_u32 s64, s64, 40\n"
    "s_mul_i32 s64, s64, 65536\n"
    "s_and_b32 s65, s71, 1\n"
    "s_lshl_b32 s65, s65, 7\n"
    "s_add_u32 s64, s64, s65\n"
    "s_add_u32 s64, s64, s67\n"
    "s_lshl_b32 s65, s71, 10\n"
    "s_add_u32 m0, s65, 81920\n"
    "s_nop 0\n"
    "buffer_load_dwordx4 v81, s[36:39], s64 offen lds\n"
    "s_add_u32 s64, s64, 131072\n"
    "s_add_u32 m0, m0, 4096\n"
    "s_nop 0\n"
    "buffer_load_dwordx4 v81, s[36:39], s64 offen lds\n"
    "s_add_u32 s64, s64, 131072\n"
    "s_add_u32 m0, m0, 4096\n"
    "s_nop 0\n"
    "buffer_load_dwordx4 v81, s[36:39], s64 offen lds\n"
    "s_add_u32 s64, s64, 131072\n"
    "s_add_u32 m0, m0, 4096\n"
    "s_nop 0\n"
    "buffer_load_dwordx4 v81, s[36:39], s64 offen lds\n"
    "s_add_u32 s64, s64, 131072\n"
    "s_add_u32 m0, m0, 4096\n"
    "s_nop 0\n"
    "buffer_load_dwordx4 v81, s[36:39], s64 offen lds\n"
    "s_add_u32 s64, s64, 131072\n"
    "s_add_u32 m0, m0, 4096\n"
    "s_nop 0\n"
    "buffer_load_dwordx4 v81, s[36:39], s64 offen lds\n"
    "s_add_u32 s64, s64, 131072\n"
    "s_add_u32 m0, m0, 4096\n"
    "s_nop 0\n"
    "buffer_load_dwordx4 v81, s[36:39], s64 offen lds\n"
    "s_add_u32 s64, s64, 131072\n"
    "s_add_u32 m0, m0, 4096\n"
    "s_nop 0\n"
    "buffer_load_dwordx4 v81, s[36:39], s64 offen lds\n"
    "s_add_u32 s64, s64, 131072\n"
    "s_add_u32 m0, m0, 4096\n"
    "s_nop 0\n"
    "buffer_load_dwordx4 v81, s[36:39], s64 offen lds\n"
    "s_add_u32 s64, s64, 131072\n"
    "s_add_u32 m0, m0, 4096\n"
    "s_nop 0\n"
    "buffer_load_dwordx4 v81, s[36:39], s64 offen lds\n"
    "s_add_u32 s64, s64, 131072\n"
    "s_add_u32 m0, m0, 4096\n"
    "s_nop 0\n"
    "buffer_load_dwordx4 v81, s[36:39], s64 offen lds\n"
    "s_add_u32 s64, s64, 131072\n"
    "s_add_u32 m0, m0, 4096\n"
    "s_nop 0\n"
    "buffer_load_dwordx4 v81, s[36:39], s64 offen lds\n"
    "s_add_u32 s64, s64, 131072\n"
    "s_add_u32 m0, m0, 4096\n"
    "s_nop 0\n"
    "buffer_load_dwordx4 v81, s[36:39], s64 offen lds\n"
    "s_add_u32 s64, s64, 131072\n"
    "s_add_u32 m0, m0, 4096\n"
    "s_nop 0\n"
    "buffer_load_dwordx4 v81, s[36:39], s64 offen lds\n"
    "s_add_u32 s64, s64, 131072\n"
    "s_add_u32 m0, m0, 4096\n"
    "s_nop 0\n"
    "buffer_load_dwordx4 v81, s[36:39], s64 offen lds\n"
    "s_add_u32 s64, s64, 131072\n"
    "s_add_u32 m0, m0, 4096\n"
    "s_nop 0\n"
    "buffer_load_dwordx4 v81, s[36:39], s64 offen lds\n"
    "s_add_u32 s64, s64, 131072\n"
    "s_add_u32 m0, m0, 4096\n"
    "s_add_u32 s24, s60, 0x300000\n"
    "s_addc_u32 s25, s61, 0\n"
    "s_and_b32 s25, s25, 0xffff\n"
    "s_mov_b32 s26, 0x240000\n"
    "s_mov_b32 s27, 0x20000\n"
    "s_mul_i32 s64, s72, 0x1000000\n"
    "s_add_u32 s64, s64, 0x540000\n"
    "s_add_u32 s28, s60, s64\n"
    "s_addc_u32 s29, s61, 0\n"
    "s_and_b32 s29, s29, 0xffff\n"
    "s_mov_b32 s30, 0x1000000\n"
    "s_mov_b32 s31, 0x20000\n"
    "v_bfe_u32 v86, v80, 3, 1\n"
    "v_bfe_u32 v87, v80, 4, 1\n"
    "v_and_b32 v90, 3, v80\n"
    "v_lshlrev_b32 v90, 3, v90\n"
    "v_bfe_u32 v85, v80, 2, 2\n"
    "v_lshl_add_u32 v90, v85, 7, v90\n"
    "s_mov_b32 s64, 2048\n"
    "v_xor_b32 v85, v86, v87\n"
    "v_lshlrev_b32 v85, 5, v85\n"
    "v_lshl_or_b32 v88, v87, 6, v85\n"
    "v_xor_b32 v89, 64, v88\n"
    "v_lshrrev_b32 v85, 4, v80\n"
    "v_mad_u32_u24 v90, v85, s64, v90\n"
    "v_add_u32 v91, 512, v90\n"
    "s_lshl_b32 s64, s71, 1\n"
    "s_add_u32 s64, s64, 0\n"
    "s_and_b32 s65, s64, 3\n"
    "s_lshl_b32 s65, s65, 5\n"
    "v_xor_b32 v92, s65, v88\n"
    "s_lshr_b32 s65, s64, 2\n"
    "s_lshl_b32 s65, s65, 10\n"
    "s_add_u32 s65, s65, 0\n"
    "v_add3_u32 v92, v92, v90, s65\n"
    "s_and_b32 s65, s64, 3\n"
    "s_lshl_b32 s65, s65, 5\n"
    "v_xor_b32 v93, s65, v89\n"
    "s_lshr_b32 s65, s64, 2\n"
    "s_lshl_b32 s65, s65, 10\n"
    "s_add_u32 s65, s65, 0\n"
    "v_add3_u32 v93, v93, v91, s65\n"
    "s_lshl_b32 s64, s71, 1\n"
    "s_add_u32 s64, s64, 1\n"
    "s_and_b32 s65, s64, 3\n"
    "s_lshl_b32 s65, s65, 5\n"
    "v_xor_b32 v94, s65, v88\n"
    "s_lshr_b32 s65, s64, 2\n"
    "s_lshl_b32 s65, s65, 10\n"
    "s_add_u32 s65, s65, 0\n"
    "v_add3_u32 v94, v94, v90, s65\n"
    "s_and_b32 s65, s64, 3\n"
    "s_lshl_b32 s65, s65, 5\n"
    "v_xor_b32 v95, s65, v89\n"
    "s_lshr_b32 s65, s64, 2\n"
    "s_lshl_b32 s65, s65, 10\n"
    "s_add_u32 s65, s65, 0\n"
    "v_add3_u32 v95, v95, v91, s65\n"
    "v_lshlrev_b32 v82, 4, v80\n"
    "s_lshl_b32 s64, s72, 5\n"
    "s_add_u32 s64, s64, s73\n"
    "s_lshl_b32 s64, s64, 1\n"
    "s_add_u32 s64, s64, s74\n"
    "s_lshl_b32 s64, s64, 2\n"
    "s_add_u32 s64, s64, s71\n"
    "s_lshl_b32 s64, s64, 6\n"
    "s_add_u32 s64, s64, 0x201000\n"
    "v_mov_b32 v83, s64\n"
    "v_cmp_gt_u32 vcc, 1, v80\n"
    "v_mov_b32 v85, 0x7fffffff\n"
    "s_nop 0\n"
    "v_cndmask_b32 v83, v85, v83, vcc\n"
    "s_waitcnt vmcnt(16)\n"
    "s_waitcnt lgkmcnt(0)\n"
    "s_barrier\n"
    "ds_read_b64_tr_b16 a[0:1], v92\n"
    "ds_read_b64_tr_b16 a[2:3], v93\n"
    "ds_read_b64_tr_b16 a[72:73], v94\n"
    "ds_read_b64_tr_b16 a[74:75], v95\n"
    "ds_read_b64_tr_b16 a[4:5], v92 offset:8192\n"
    "ds_read_b64_tr_b16 a[6:7], v93 offset:8192\n"
    "ds_read_b64_tr_b16 a[76:77], v94 offset:8192\n"
    "ds_read_b64_tr_b16 a[78:79], v95 offset:8192\n"
    "ds_read_b64_tr_b16 a[8:9], v92 offset:16384\n"
    "ds_read_b64_tr_b16 a[10:11], v93 offset:16384\n"
    "ds_read_b64_tr_b16 a[80:81], v94 offset:16384\n"
    "ds_read_b64_tr_b16 a[82:83], v95 offset:16384\n"
    "ds_read_b64_tr_b16 a[12:13], v92 offset:24576\n"
    "ds_read_b64_tr_b16 a[14:15], v93 offset:24576\n"
    "ds_read_b64_tr_b16 a[84:85], v94 offset:24576\n"
    "ds_read_b64_tr_b16 a[86:87], v95 offset:24576\n"
    "ds_read_b64_tr_b16 a[16:17], v92 offset:32768\n"
    "ds_read_b64_tr_b16 a[18:19], v93 offset:32768\n"
    "ds_read_b64_tr_b16 a[88:89], v94 offset:32768\n"
    "ds_read_b64_tr_b16 a[90:91], v95 offset:32768\n"
    "ds_read_b64_tr_b16 a[20:21], v92 offset:40960\n"
    "ds_read_b64_tr_b16 a[22:23], v93 offset:40960\n"
    "ds_read_b64_tr_b16 a[92:93], v94 offset:40960\n"
    "ds_read_b64_tr_b16 a[94:95], v95 offset:40960\n"
    "ds_read_b64_tr_b16 a[24:25], v92 offset:49152\n"
    "ds_read_b64_tr_b16 a[26:27], v93 offset:49152\n"
    "ds_read_b64_tr_b16 a[96:97], v94 offset:49152\n"
    "ds_read_b64_tr_b16 a[98:99], v95 offset:49152\n"
    "ds_read_b64_tr_b16 a[28:29], v92 offset:57344\n"
    "ds_read_b64_tr_b16 a[30:31], v93 offset:57344\n"
    "ds_read_b64_tr_b16 a[100:101], v94 offset:57344\n"
    "ds_read_b64_tr_b16 a[102:103], v95 offset:57344\n"
    "v_add_u32 v92, 0x10000, v92\n"
    "v_add_u32 v93, 0x10000, v93\n"
    "v_add_u32 v94, 0x10000, v94\n"
    "v_add_u32 v95, 0x10000, v95\n"
    "ds_read_b64_tr_b16 a[32:33], v92\n"
    "ds_read_b64_tr_b16 a[34:35], v93\n"
    "ds_read_b64_tr_b16 a[104:105], v94\n"
    "ds_read_b64_tr_b16 a[106:107], v95\n"
    "ds_read_b64_tr_b16 a[36:37], v92 offset:8192\n"
    "ds_read_b64_tr_b16 a[38:39], v93 offset:8192\n"
    "ds_read_b64_tr_b16 a[108:109], v94 offset:8192\n"
    "ds_read_b64_tr_b16 a[110:111], v95 offset:8192\n"
    "s_waitcnt vmcnt(0)\n"
    "s_waitcnt lgkmcnt(0)\n"
    "s_barrier\n"
    "s_mul_i32 s77, s74, 36864\n"
    "s_mul_i32 s64, s71, 9216\n"
    "s_add_u32 s77, s77, s64\n"
    "s_mov_b32 s78, s64\n"
    "s_add_u32 m0, s78, 0\n"
    "s_add_u32 s64, s77, 0\n"
    "buffer_load_dwordx4 v82, s[24:27], s64 offen lds\n"
    "s_add_u32 m0, s78, 1024\n"
    "s_add_u32 s64, s77, 1024\n"
    "buffer_load_dwordx4 v82, s[24:27], s64 offen lds\n"
    "s_add_u32 m0, s78, 2048\n"
    "s_add_u32 s64, s77, 2048\n"
    "buffer_load_dwordx4 v82, s[24:27], s64 offen lds\n"
    "s_add_u32 m0, s78, 3072\n"
    "s_add_u32 s64, s77, 3072\n"
    "buffer_load_dwordx4 v82, s[24:27], s64 offen lds\n"
    "s_add_u32 m0, s78, 4096\n"
    "s_add_u32 s64, s77, 4096\n"
    "buffer_load_dwordx4 v82, s[24:27], s64 offen lds\n"
    "s_add_u32 m0, s78, 5120\n"
    "s_add_u32 s64, s77, 5120\n"
    "buffer_load_dwordx4 v82, s[24:27], s64 offen lds\n"
    "s_add_u32 m0, s78, 6144\n"
    "s_add_u32 s64, s77, 6144\n"
    "buffer_load_dwordx4 v82, s[24:27], s64 offen lds\n"
    "s_add_u32 m0, s78, 7168\n"
    "s_add_u32 s64, s77, 7168\n"
    "buffer_load_dwordx4 v82, s[24:27], s64 offen lds\n"
    "s_add_u32 m0, s78, 8192\n"
    "s_add_u32 s64, s77, 8192\n"
    "buffer_load_dwordx4 v82, s[24:27], s64 offen lds\n"
    "s_add_u32 s77, s77, 73728\n"
    "s_add_u32 s78, s78, 36864\n"
    "s_sub_u32 s64, s78, 147456\n"
    "s_cmp_ge_u32 s78, 147456\n"
    "s_cselect_b32 s78, s64, s78\n"
    "s_add_u32 m0, s78, 0\n"
    "s_add_u32 s64, s77, 0\n"
    "buffer_load_dwordx4 v82, s[24:27], s64 offen lds\n"
    "s_add_u32 m0, s78, 1024\n"
    "s_add_u32 s64, s77, 1024\n"
    "buffer_load_dwordx4 v82, s[24:27], s64 offen lds\n"
    "s_add_u32 m0, s78, 2048\n"
    "s_add_u32 s64, s77, 2048\n"
    "buffer_load_dwordx4 v82, s[24:27], s64 offen lds\n"
    "s_add_u32 m0, s78, 3072\n"
    "s_add_u32 s64, s77, 3072\n"
    "buffer_load_dwordx4 v82, s[24:27], s64 offen lds\n"
    "s_add_u32 m0, s78, 4096\n"
    "s_add_u32 s64, s77, 4096\n"
    "buffer_load_dwordx4 v82, s[24:27], s64 offen lds\n"
    "s_add_u32 m0, s78, 5120\n"
    "s_add_u32 s64, s77, 5120\n"
    "buffer_load_dwordx4 v82, s[24:27], s64 offen lds\n"
    "s_add_u32 m0, s78, 6144\n"
    "s_add_u32 s64, s77, 6144\n"
    "buffer_load_dwordx4 v82, s[24:27], s64 offen lds\n"
    "s_add_u32 m0, s78, 7168\n"
    "s_add_u32 s64, s77, 7168\n"
    "buffer_load_dwordx4 v82, s[24:27], s64 offen lds\n"
    "s_add_u32 m0, s78, 8192\n"
    "s_add_u32 s64, s77, 8192\n"
    "buffer_load_dwordx4 v82, s[24:27], s64 offen lds\n"
    "s_add_u32 s77, s77, 73728\n"
    "s_add_u32 s78, s78, 36864\n"
    "s_sub_u32 s64, s78, 147456\n"
    "s_cmp_ge_u32 s78, 147456\n"
    "s_cselect_b32 s78, s64, s78\n"
    "ds_read_b64_tr_b16 a[40:41], v92 offset:16384\n"
    "ds_read_b64_tr_b16 a[42:43], v93 offset:16384\n"
    "ds_read_b64_tr_b16 a[112:113], v94 offset:16384\n"
    "ds_read_b64_tr_b16 a[114:115], v95 offset:16384\n"
    "ds_read_b64_tr_b16 a[44:45], v92 offset:24576\n"
    "ds_read_b64_tr_b16 a[46:47], v93 offset:24576\n"
    "ds_read_b64_tr_b16 a[116:117], v94 offset:24576\n"
    "ds_read_b64_tr_b16 a[118:119], v95 offset:24576\n"
    "ds_read_b64_tr_b16 a[48:49], v92 offset:32768\n"
    "ds_read_b64_tr_b16 a[50:51], v93 offset:32768\n"
    "ds_read_b64_tr_b16 a[120:121], v94 offset:32768\n"
    "ds_read_b64_tr_b16 a[122:123], v95 offset:32768\n"
    "ds_read_b64_tr_b16 a[52:53], v92 offset:40960\n"
    "ds_read_b64_tr_b16 a[54:55], v93 offset:40960\n"
    "ds_read_b64_tr_b16 a[124:125], v94 offset:40960\n"
    "ds_read_b64_tr_b16 a[126:127], v95 offset:40960\n"
    "ds_read_b64_tr_b16 a[56:57], v92 offset:49152\n"
    "ds_read_b64_tr_b16 a[58:59], v93 offset:49152\n"
    "ds_read_b64_tr_b16 a[128:129], v94 offset:49152\n"
    "ds_read_b64_tr_b16 a[130:131], v95 offset:49152\n"
    "ds_read_b64_tr_b16 a[60:61], v92 offset:57344\n"
    "ds_read_b64_tr_b16 a[62:63], v93 offset:57344\n"
    "ds_read_b64_tr_b16 a[132:133], v94 offset:57344\n"
    "ds_read_b64_tr_b16 a[134:135], v95 offset:57344\n"
    "v_add_u32 v92, 0x10000, v92\n"
    "v_add_u32 v93, 0x10000, v93\n"
    "v_add_u32 v94, 0x10000, v94\n"
    "v_add_u32 v95, 0x10000, v95\n"
    "ds_read_b64_tr_b16 a[64:65], v92\n"
    "ds_read_b64_tr_b16 a[66:67], v93\n"
    "ds_read_b64_tr_b16 a[136:137], v94\n"
    "ds_read_b64_tr_b16 a[138:139], v95\n"
    "ds_read_b64_tr_b16 a[68:69], v92 offset:8192\n"
    "ds_read_b64_tr_b16 a[70:71], v93 offset:8192\n"
    "ds_read_b64_tr_b16 a[140:141], v94 offset:8192\n"
    "ds_read_b64_tr_b16 a[142:143], v95 offset:8192\n"
    "s_mov_b32 s75, 0\n"
    "s_mov_b32 s76, 0\n"
    "s_lshl_b32 s79, s73, 6\n"
    "s_add_u32 s79, s79, s74\n"
    "s_lshl_b32 s79, s79, 13\n"
    "v_mov_b32 v84, v82\n"
    "s_waitcnt vmcnt(9)\n"
    "s_waitcnt lgkmcnt(0)\n"
    "s_barrier\n"
    "s_cmp_eq_u32 s72, 2\n"
    "s_cbranch_scc1 pj_gov_%=\n"
    "pj_goq_%=:\n"
    "s_lshl_b32 s64, s71, 10\n"
    "s_add_u32 s79, s79, s64\n"
    "ds_read_b128 v[0:3], v84\n"
    "ds_read_b128 v[4:7], v84 offset:18432\n"
    "ds_read_b128 v[8:11], v84 offset:1024\n"
    "ds_read_b128 v[12:15], v84 offset:19456\n"
    "ds_read_b128 v[16:19], v84 offset:2048\n"
    "ds_read_b128 v[20:23], v84 offset:20480\n"
    "pj_loopq_%=:\n"
    "s_sub_u32 s67, s79, 16384\n"
    "s_cmp_eq_u32 s75, 0\n"
    "s_cselect_b32 s67, 0x7ff00000, s67\n"
    "s_add_u32 s76, s76, 36864\n"
    "s_cmp_eq_u32 s76, 147456\n"
    "s_cselect_b32 s76, 0, s76\n"
    "v_add_u32 v72, s76, v82\n"
    "s_waitcnt lgkmcnt(5)\n"
    "v_mfma_f32_16x16x32_bf16 v[32:35], a[0:3], v[0:3], 0\n"
    "ds_read_b128 v[24:27], v84 offset:3072\n"
    "v_mfma_f32_16x16x32_bf16 v[36:39], a[72:75], v[0:3], 0\n"
    "s_waitcnt lgkmcnt(5)\n"
    "v_mfma_f32_16x16x32_bf16 v[40:43], a[0:3], v[4:7], 0\n"
    "ds_read_b128 v[28:31], v84 offset:21504\n"
    "v_cvt_pk_bf16_f32 v64, v48, v49\n"
    "v_mfma_f32_16x16x32_bf16 v[44:47], a[72:75], v[4:7], 0\n"
    "s_waitcnt lgkmcnt(5)\n"
    "v_mfma_f32_16x16x32_bf16 v[32:35], a[4:7], v[8:11], v[32:35]\n"
    "ds_read_b128 v[0:3], v84 offset:4096\n"
    "v_cvt_pk_bf16_f32 v65, v50, v51\n"
    "v_mfma_f32_16x16x32_bf16 v[36:39], a[76:79], v[8:11], v[36:39]\n"
    "s_waitcnt lgkmcnt(5)\n"
    "v_mfma_f32_16x16x32_bf16 v[40:43], a[4:7], v[12:15], v[40:43]\n"
    "ds_read_b128 v[4:7], v84 offset:22528\n"
    "v_cvt_pk_bf16_f32 v66, v52, v53\n"
    "v_mfma_f32_16x16x32_bf16 v[44:47], a[76:79], v[12:15], v[44:47]\n"
    "s_waitcnt lgkmcnt(5)\n"
    "v_mfma_f32_16x16x32_bf16 v[32:35], a[8:11], v[16:19], v[32:35]\n"
    "ds_read_b128 v[8:11], v84 offset:5120\n"
    "v_cvt_pk_bf16_f32 v67, v54, v55\n"
    "v_mfma_f32_16x16x32_bf16 v[36:39], a[80:83], v[16:19], v[36:39]\n"
    "s_waitcnt lgkmcnt(5)\n"
    "v_mfma_f32_16x16x32_bf16 v[40:43], a[8:11], v[20:23], v[40:43]\n"
    "ds_read_b128 v[12:15], v84 offset:23552\n"
    "s_add_u32 s67, s67, 0\n"
    "buffer_store_dwordx4 v[64:67], v82, s[28:31], s67 offen sc1\n"
    "v_mfma_f32_16x16x32_bf16 v[44:47], a[80:83], v[20:23], v[44:47]\n"
    "s_waitcnt lgkmcnt(5)\n"
    "v_mfma_f32_16x16x32_bf16 v[32:35], a[12:15], v[24:27], v[32:35]\n"
    "ds_read_b128 v[16:19], v84 offset:6144\n"
    "v_cvt_pk_bf16_f32 v68, v56, v57\n"
    "v_mfma_f32_16x16x32_bf16 v[36:39], a[84:87], v[24:27], v[36:39]\n"
    "s_waitcnt lgkmcnt(5)\n"
    "v_mfma_f32_16x16x32_bf16 v[40:43], a[12:15], v[28:31], v[40:43]\n"
    "ds_read_b128 v[20:23], v84 offset:24576\n"
    "v_cvt_pk_bf16_f32 v69, v58, v59\n"
    "v_mfma_f32_16x16x32_bf16 v[44:47], a[84:87], v[28:31], v[44:47]\n"
    "s_waitcnt lgkmcnt(5)\n"
    "v_mfma_f32_16x16x32_bf16 v[32:35], a[16:19], v[0:3], v[32:35]\n"
    "ds_read_b128 v[24:27], v84 offset:7168\n"
    "v_cvt_pk_bf16_f32 v70, v60, v61\n"
    "v_mfma_f32_16x16x32_bf16 v[36:39], a[88:91], v[0:3], v[36:39]\n"
    "s_waitcnt lgkmcnt(5)\n"
    "v_mfma_f32_16x16x32_bf16 v[40:43], a[16:19], v[4:7], v[40:43]\n"
    "ds_read_b128 v[28:31], v84 offset:25600\n"
    "v_cvt_pk_bf16_f32 v71, v62, v63\n"
    "v_mfma_f32_16x16x32_bf16 v[44:47], a[88:91], v[4:7], v[44:47]\n"
    "s_waitcnt lgkmcnt(5)\n"
    "v_mfma_f32_16x16x32_bf16 v[32:35], a[20:23], v[8:11], v[32:35]\n"
    "ds_read_b128 v[0:3], v84 offset:8192\n"
    "s_add_u32 s67, s67, 4096\n"
    "buffer_store_dwordx4 v[68:71], v82, s[28:31], s67 offen sc1\n"
    "v_mfma_f32_16x16x32_bf16 v[36:39], a[92:95], v[8:11], v[36:39]\n"
    "s_waitcnt lgkmcnt(5)\n"
    "v_mfma_f32_16x16x32_bf16 v[40:43], a[20:23], v[12:15], v[40:43]\n"
    "ds_read_b128 v[4:7], v84 offset:26624\n"
    "v_mfma_f32_16x16x32_bf16 v[44:47], a[92:95], v[12:15], v[44:47]\n"
    "s_waitcnt lgkmcnt(5)\n"
    "v_mfma_f32_16x16x32_bf16 v[32:35], a[24:27], v[16:19], v[32:35]\n"
    "ds_read_b128 v[8:11], v84 offset:9216\n"
    "v_mfma_f32_16x16x32_bf16 v[36:39], a[96:99], v[16:19], v[36:39]\n"
    "s_waitcnt lgkmcnt(5)\n"
    "v_mfma_f32_16x16x32_bf16 v[40:43], a[24:27], v[20:23], v[40:43]\n"
    "ds_read_b128 v[12:15], v84 offset:27648\n"
    "v_mfma_f32_16x16x32_bf16 v[44:47], a[96:99], v[20:23], v[44:47]\n"
    "s_waitcnt lgkmcnt(5)\n"
    "v_mfma_f32_16x16x32_bf16 v[32:35], a[28:31], v[24:27], v[32:35]\n"
    "ds_read_b128 v[16:19], v84 offset:10240\n"
    "v_mfma_f32_16x16x32_bf16 v[36:39], a[100:103], v[24:27], v[36:39]\n"
    "s_add_u32 m0, s78, 0\n"
    "s_add_u32 s64, s77, 0\n"
    "buffer_load_dwordx4 v82, s[24:27], s64 offen lds\n"
    "s_waitcnt lgkmcnt(5)\n"
    "v_mfma_f32_16x16x32_bf16 v[40:43], a[28:31], v[28:31], v[40:43]\n"
    "ds_read_b128 v[20:23], v84 offset:28672\n"
    "v_mfma_f32_16x16x32_bf16 v[44:47], a[100:103], v[28:31], v[44:47]\n"
    "s_waitcnt lgkmcnt(5)\n"
    "v_mfma_f32_16x16x32_bf16 v[32:35], a[32:35], v[0:3], v[32:35]\n"
    "ds_read_b128 v[24:27], v84 offset:11264\n"
    "v_mfma_f32_16x16x32_bf16 v[36:39], a[104:107], v[0:3], v[36:39]\n"
    "s_add_u32 m0, s78, 1024\n"
    "s_add_u32 s64, s77, 1024\n"
    "buffer_load_dwordx4 v82, s[24:27], s64 offen lds\n"
    "s_waitcnt lgkmcnt(5)\n"
    "v_mfma_f32_16x16x32_bf16 v[40:43], a[32:35], v[4:7], v[40:43]\n"
    "ds_read_b128 v[28:31], v84 offset:29696\n"
    "v_mfma_f32_16x16x32_bf16 v[44:47], a[104:107], v[4:7], v[44:47]\n"
    "s_waitcnt lgkmcnt(5)\n"
    "v_mfma_f32_16x16x32_bf16 v[32:35], a[36:39], v[8:11], v[32:35]\n"
    "ds_read_b128 v[0:3], v84 offset:12288\n"
    "v_mfma_f32_16x16x32_bf16 v[36:39], a[108:111], v[8:11], v[36:39]\n"
    "s_add_u32 m0, s78, 2048\n"
    "s_add_u32 s64, s77, 2048\n"
    "buffer_load_dwordx4 v82, s[24:27], s64 offen lds\n"
    "s_waitcnt lgkmcnt(5)\n"
    "v_mfma_f32_16x16x32_bf16 v[40:43], a[36:39], v[12:15], v[40:43]\n"
    "ds_read_b128 v[4:7], v84 offset:30720\n"
    "v_mfma_f32_16x16x32_bf16 v[44:47], a[108:111], v[12:15], v[44:47]\n"
    "s_waitcnt lgkmcnt(5)\n"
    "v_mfma_f32_16x16x32_bf16 v[32:35], a[40:43], v[16:19], v[32:35]\n"
    "ds_read_b128 v[8:11], v84 offset:13312\n"
    "v_mfma_f32_16x16x32_bf16 v[36:39], a[112:115], v[16:19], v[36:39]\n"
    "s_add_u32 m0, s78, 3072\n"
    "s_add_u32 s64, s77, 3072\n"
    "buffer_load_dwordx4 v82, s[24:27], s64 offen lds\n"
    "s_waitcnt lgkmcnt(5)\n"
    "v_mfma_f32_16x16x32_bf16 v[40:43], a[40:43], v[20:23], v[40:43]\n"
    "ds_read_b128 v[12:15], v84 offset:31744\n"
    "v_mfma_f32_16x16x32_bf16 v[44:47], a[112:115], v[20:23], v[44:47]\n"
    "s_waitcnt lgkmcnt(5)\n"
    "v_mfma_f32_16x16x32_bf16 v[32:35], a[44:47], v[24:27], v[32:35]\n"
    "ds_read_b128 v[16:19], v84 offset:14336\n"
    "v_mfma_f32_16x16x32_bf16 v[36:39], a[116:119], v[24:27], v[36:39]\n"
    "s_add_u32 m0, s78, 4096\n"
    "s_add_u32 s64, s77, 4096\n"
    "buffer_load_dwordx4 v82, s[24:27], s64 offen lds\n"
    "s_waitcnt lgkmcnt(5)\n"
    "v_mfma_f32_16x16x32_bf16 v[40:43], a[44:47], v[28:31], v[40:43]\n"
    "ds_read_b128 v[20:23], v84 offset:32768\n"
    "v_mfma_f32_16x16x32_bf16 v[44:47], a[116:119], v[28:31], v[44:47]\n"
    "v_mov_b32 v85, s75\n"
    "s_waitcnt vmcnt(5)\n"
    "buffer_store_dword v85, v83, s[20:23], 0 offen sc1\n"
    "s_barrier\n"
    "s_waitcnt lgkmcnt(5)\n"
    "v_mfma_f32_16x16x32_bf16 v[32:35], a[48:51], v[0:3], v[32:35]\n"
    "ds_read_b128 v[24:27], v84 offset:15360\n"
    "v_mfma_f32_16x16x32_bf16 v[36:39], a[120:123], v[0:3], v[36:39]\n"
    "s_waitcnt lgkmcnt(5)\n"
    "v_mfma_f32_16x16x32_bf16 v[40:43], a[48:51], v[4:7], v[40:43]\n"
    "ds_read_b128 v[28:31], v84 offset:33792\n"
    "v_mfma_f32_16x16x32_bf16 v[44:47], a[120:123], v[4:7], v[44:47]\n"
    "s_waitcnt lgkmcnt(5)\n"
    "v_mfma_f32_16x16x32_bf16 v[32:35], a[52:55], v[8:11], v[32:35]\n"
    "ds_read_b128 v[0:3], v84 offset:16384\n"
    "v_mfma_f32_16x16x32_bf16 v[36:39], a[124:127], v[8:11], v[36:39]\n"
    "s_add_u32 m0, s78, 5120\n"
    "s_add_u32 s64, s77, 5120\n"
    "buffer_load_dwordx4 v82, s[24:27], s64 offen lds\n"
    "s_waitcnt lgkmcnt(5)\n"
    "v_mfma_f32_16x16x32_bf16 v[40:43], a[52:55], v[12:15], v[40:43]\n"
    "ds_read_b128 v[4:7], v84 offset:34816\n"
    "v_mfma_f32_16x16x32_bf16 v[44:47], a[124:127], v[12:15], v[44:47]\n"
    "s_waitcnt lgkmcnt(5)\n"
    "v_mfma_f32_16x16x32_bf16 v[32:35], a[56:59], v[16:19], v[32:35]\n"
    "ds_read_b128 v[8:11], v84 offset:17408\n"
    "v_mfma_f32_16x16x32_bf16 v[36:39], a[128:131], v[16:19], v[36:39]\n"
    "s_add_u32 m0, s78, 6144\n"
    "s_add_u32 s64, s77, 6144\n"
    "buffer_load_dwordx4 v82, s[24:27], s64 offen lds\n"
    "s_waitcnt lgkmcnt(5)\n"
    "v_mfma_f32_16x16x32_bf16 v[40:43], a[56:59], v[20:23], v[40:43]\n"
    "ds_read_b128 v[12:15], v84 offset:35840\n"
    "v_mfma_f32_16x16x32_bf16 v[44:47], a[128:131], v[20:23], v[44:47]\n"
    "s_waitcnt lgkmcnt(5)\n"
    "v_mfma_f32_16x16x32_bf16 v[32:35], a[60:63], v[24:27], v[32:35]\n"
    "ds_read_b128 v[16:19], v72\n"
    "v_mfma_f32_16x16x32_bf16 v[36:39], a[132:135], v[24:27], v[36:39]\n"
    "s_add_u32 m0, s78, 7168\n"
    "s_add_u32 s64, s77, 7168\n"
    "buffer_load_dwordx4 v82, s[24:27], s64 offen lds\n"
    "s_waitcnt lgkmcnt(5)\n"
    "v_mfma_f32_16x16x32_bf16 v[40:43], a[60:63], v[28:31], v[40:43]\n"
    "ds_read_b128 v[20:23], v72 offset:18432\n"
    "v_mfma_f32_16x16x32_bf16 v[44:47], a[132:135], v[28:31], v[44:47]\n"
    "s_waitcnt lgkmcnt(5)\n"
    "v_mfma_f32_16x16x32_bf16 v[32:35], a[64:67], v[0:3], v[32:35]\n"
    "ds_read_b128 v[24:27], v72 offset:1024\n"
    "v_mfma_f32_16x16x32_bf16 v[36:39], a[136:139], v[0:3], v[36:39]\n"
    "s_add_u32 m0, s78, 8192\n"
    "s_add_u32 s64, s77, 8192\n"
    "buffer_load_dwordx4 v82, s[24:27], s64 offen lds\n"
    "s_waitcnt lgkmcnt(5)\n"
    "v_mfma_f32_16x16x32_bf16 v[40:43], a[64:67], v[4:7], v[40:43]\n"
    "ds_read_b128 v[28:31], v72 offset:19456\n"
    "v_mfma_f32_16x16x32_bf16 v[44:47], a[136:139], v[4:7], v[44:47]\n"
    "s_waitcnt lgkmcnt(5)\n"
    "v_mfma_f32_16x16x32_bf16 v[32:35], a[68:71], v[8:11], v[32:35]\n"
    "ds_read_b128 v[0:3], v72 offset:2048\n"
    "v_mfma_f32_16x16x32_bf16 v[36:39], a[140:143], v[8:11], v[36:39]\n"
    "s_waitcnt lgkmcnt(5)\n"
    "v_mfma_f32_16x16x32_bf16 v[40:43], a[68:71], v[12:15], v[40:43]\n"
    "ds_read_b128 v[4:7], v72 offset:20480\n"
    "v_mfma_f32_16x16x32_bf16 v[44:47], a[140:143], v[12:15], v[44:47]\n"
    "s_add_u32 s77, s77, 73728\n"
    "s_add_u32 s78, s78, 36864\n"
    "s_sub_u32 s64, s78, 147456\n"
    "s_cmp_ge_u32 s78, 147456\n"
    "s_cselect_b32 s78, s64, s78\n"
    "v_mov_b32 v84, v72\n"
    "s_add_u32 s79, s79, 16384\n"
    "s_add_u32 s75, s75, 1\n"
    "s_sub_u32 s67, s79, 16384\n"
    "s_cmp_eq_u32 s75, 0\n"
    "s_cselect_b32 s67, 0x7ff00000, s67\n"
    "s_add_u32 s76, s76, 36864\n"
    "s_cmp_eq_u32 s76, 147456\n"
    "s_cselect_b32 s76, 0, s76\n"
    "v_add_u32 v72, s76, v82\n"
    "s_waitcnt lgkmcnt(5)\n"
    "v_mfma_f32_16x16x32_bf16 v[48:51], a[0:3], v[16:19], 0\n"
    "ds_read_b128 v[8:11], v84 offset:3072\n"
    "v_mfma_f32_16x16x32_bf16 v[52:55], a[72:75], v[16:19], 0\n"
    "s_waitcnt lgkmcnt(5)\n"
    "v_mfma_f32_16x16x32_bf16 v[56:59], a[0:3], v[20:23], 0\n"
    "ds_read_b128 v[12:15], v84 offset:21504\n"
    "v_cvt_pk_bf16_f32 v64, v32, v33\n"
    "v_mfma_f32_16x16x32_bf16 v[60:63], a[72:75], v[20:23], 0\n"
    "s_waitcnt lgkmcnt(5)\n"
    "v_mfma_f32_16x16x32_bf16 v[48:51], a[4:7], v[24:27], v[48:51]\n"
    "ds_read_b128 v[16:19], v84 offset:4096\n"
    "v_cvt_pk_bf16_f32 v65, v34, v35\n"
    "v_mfma_f32_16x16x32_bf16 v[52:55], a[76:79], v[24:27], v[52:55]\n"
    "s_waitcnt lgkmcnt(5)\n"
    "v_mfma_f32_16x16x32_bf16 v[56:59], a[4:7], v[28:31], v[56:59]\n"
    "ds_read_b128 v[20:23], v84 offset:22528\n"
    "v_cvt_pk_bf16_f32 v66, v36, v37\n"
    "v_mfma_f32_16x16x32_bf16 v[60:63], a[76:79], v[28:31], v[60:63]\n"
    "s_waitcnt lgkmcnt(5)\n"
    "v_mfma_f32_16x16x32_bf16 v[48:51], a[8:11], v[0:3], v[48:51]\n"
    "ds_read_b128 v[24:27], v84 offset:5120\n"
    "v_cvt_pk_bf16_f32 v67, v38, v39\n"
    "v_mfma_f32_16x16x32_bf16 v[52:55], a[80:83], v[0:3], v[52:55]\n"
    "s_waitcnt lgkmcnt(5)\n"
    "v_mfma_f32_16x16x32_bf16 v[56:59], a[8:11], v[4:7], v[56:59]\n"
    "ds_read_b128 v[28:31], v84 offset:23552\n"
    "s_add_u32 s67, s67, 0\n"
    "buffer_store_dwordx4 v[64:67], v82, s[28:31], s67 offen sc1\n"
    "v_mfma_f32_16x16x32_bf16 v[60:63], a[80:83], v[4:7], v[60:63]\n"
    "s_waitcnt lgkmcnt(5)\n"
    "v_mfma_f32_16x16x32_bf16 v[48:51], a[12:15], v[8:11], v[48:51]\n"
    "ds_read_b128 v[0:3], v84 offset:6144\n"
    "v_cvt_pk_bf16_f32 v68, v40, v41\n"
    "v_mfma_f32_16x16x32_bf16 v[52:55], a[84:87], v[8:11], v[52:55]\n"
    "s_waitcnt lgkmcnt(5)\n"
    "v_mfma_f32_16x16x32_bf16 v[56:59], a[12:15], v[12:15], v[56:59]\n"
    "ds_read_b128 v[4:7], v84 offset:24576\n"
    "v_cvt_pk_bf16_f32 v69, v42, v43\n"
    "v_mfma_f32_16x16x32_bf16 v[60:63], a[84:87], v[12:15], v[60:63]\n"
    "s_waitcnt lgkmcnt(5)\n"
    "v_mfma_f32_16x16x32_bf16 v[48:51], a[16:19], v[16:19], v[48:51]\n"
    "ds_read_b128 v[8:11], v84 offset:7168\n"
    "v_cvt_pk_bf16_f32 v70, v44, v45\n"
    "v_mfma_f32_16x16x32_bf16 v[52:55], a[88:91], v[16:19], v[52:55]\n"
    "s_waitcnt lgkmcnt(5)\n"
    "v_mfma_f32_16x16x32_bf16 v[56:59], a[16:19], v[20:23], v[56:59]\n"
    "ds_read_b128 v[12:15], v84 offset:25600\n"
    "v_cvt_pk_bf16_f32 v71, v46, v47\n"
    "v_mfma_f32_16x16x32_bf16 v[60:63], a[88:91], v[20:23], v[60:63]\n"
    "s_waitcnt lgkmcnt(5)\n"
    "v_mfma_f32_16x16x32_bf16 v[48:51], a[20:23], v[24:27], v[48:51]\n"
    "ds_read_b128 v[16:19], v84 offset:8192\n"
    "s_add_u32 s67, s67, 4096\n"
    "buffer_store_dwordx4 v[68:71], v82, s[28:31], s67 offen sc1\n"
    "v_mfma_f32_16x16x32_bf16 v[52:55], a[92:95], v[24:27], v[52:55]\n"
    "s_waitcnt lgkmcnt(5)\n"
    "v_mfma_f32_16x16x32_bf16 v[56:59], a[20:23], v[28:31], v[56:59]\n"
    "ds_read_b128 v[20:23], v84 offset:26624\n"
    "v_mfma_f32_16x16x32_bf16 v[60:63], a[92:95], v[28:31], v[60:63]\n"
    "s_waitcnt lgkmcnt(5)\n"
    "v_mfma_f32_16x16x32_bf16 v[48:51], a[24:27], v[0:3], v[48:51]\n"
    "ds_read_b128 v[24:27], v84 offset:9216\n"
    "v_mfma_f32_16x16x32_bf16 v[52:55], a[96:99], v[0:3], v[52:55]\n"
    "s_waitcnt lgkmcnt(5)\n"
    "v_mfma_f32_16x16x32_bf16 v[56:59], a[24:27], v[4:7], v[56:59]\n"
    "ds_read_b128 v[28:31], v84 offset:27648\n"
    "v_mfma_f32_16x16x32_bf16 v[60:63], a[96:99], v[4:7], v[60:63]\n"
    "s_waitcnt lgkmcnt(5)\n"
    "v_mfma_f32_16x16x32_bf16 v[48:51], a[28:31], v[8:11], v[48:51]\n"
    "ds_read_b128 v[0:3], v84 offset:10240\n"
    "v_mfma_f32_16x16x32_bf16 v[52:55], a[100:103], v[8:11], v[52:55]\n"
    "s_add_u32 m0, s78, 0\n"
    "s_add_u32 s64, s77, 0\n"
    "buffer_load_dwordx4 v82, s[24:27], s64 offen lds\n"
    "s_waitcnt lgkmcnt(5)\n"
    "v_mfma_f32_16x16x32_bf16 v[56:59], a[28:31], v[12:15], v[56:59]\n"
    "ds_read_b128 v[4:7], v84 offset:28672\n"
    "v_mfma_f32_16x16x32_bf16 v[60:63], a[100:103], v[12:15], v[60:63]\n"
    "s_waitcnt lgkmcnt(5)\n"
    "v_mfma_f32_16x16x32_bf16 v[48:51], a[32:35], v[16:19], v[48:51]\n"
    "ds_read_b128 v[8:11], v84 offset:11264\n"
    "v_mfma_f32_16x16x32_bf16 v[52:55], a[104:107], v[16:19], v[52:55]\n"
    "s_add_u32 m0, s78, 1024\n"
    "s_add_u32 s64, s77, 1024\n"
    "buffer_load_dwordx4 v82, s[24:27], s64 offen lds\n"
    "s_waitcnt lgkmcnt(5)\n"
    "v_mfma_f32_16x16x32_bf16 v[56:59], a[32:35], v[20:23], v[56:59]\n"
    "ds_read_b128 v[12:15], v84 offset:29696\n"
    "v_mfma_f32_16x16x32_bf16 v[60:63], a[104:107], v[20:23], v[60:63]\n"
    "s_waitcnt lgkmcnt(5)\n"
    "v_mfma_f32_16x16x32_bf16 v[48:51], a[36:39], v[24:27], v[48:51]\n"
    "ds_read_b128 v[16:19], v84 offset:12288\n"
    "v_mfma_f32_16x16x32_bf16 v[52:55], a[108:111], v[24:27], v[52:55]\n"
    "s_add_u32 m0, s78, 2048\n"
    "s_add_u32 s64, s77, 2048\n"
    "buffer_load_dwordx4 v82, s[24:27], s64 offen lds\n"
    "s_waitcnt lgkmcnt(5)\n"
    "v_mfma_f32_16x16x32_bf16 v[56:59], a[36:39], v[28:31], v[56:59]\n"
    "ds_read_b128 v[20:23], v84 offset:30720\n"
    "v_mfma_f32_16x16x32_bf16 v[60:63], a[108:111], v[28:31], v[60:63]\n"
    "s_waitcnt lgkmcnt(5)\n"
    "v_mfma_f32_16x16x32_bf16 v[48:51], a[40:43], v[0:3], v[48:51]\n"
    "ds_read_b128 v[24:27], v84 offset:13312\n"
    "v_mfma_f32_16x16x32_bf16 v[52:55], a[112:115], v[0:3], v[52:55]\n"
    "s_add_u32 m0, s78, 3072\n"
    "s_add_u32 s64, s77, 3072\n"
    "buffer_load_dwordx4 v82, s[24:27], s64 offen lds\n"
    "s_waitcnt lgkmcnt(5)\n"
    "v_mfma_f32_16x16x32_bf16 v[56:59], a[40:43], v[4:7], v[56:59]\n"
    "ds_read_b128 v[28:31], v84 offset:31744\n"
    "v_mfma_f32_16x16x32_bf16 v[60:63], a[112:115], v[4:7], v[60:63]\n"
    "s_waitcnt lgkmcnt(5)\n"
    "v_mfma_f32_16x16x32_bf16 v[48:51], a[44:47], v[8:11], v[48:51]\n"
    "ds_read_b128 v[0:3], v84 offset:14336\n"
    "v_mfma_f32_16x16x32_bf16 v[52:55], a[116:119], v[8:11], v[52:55]\n"
    "s_add_u32 m0, s78, 4096\n"
    "s_add_u32 s64, s77, 4096\n"
    "buffer_load_dwordx4 v82, s[24:27], s64 offen lds\n"
    "s_waitcnt lgkmcnt(5)\n"
    "v_mfma_f32_16x16x32_bf16 v[56:59], a[44:47], v[12:15], v[56:59]\n"
    "ds_read_b128 v[4:7], v84 offset:32768\n"
    "v_mfma_f32_16x16x32_bf16 v[60:63], a[116:119], v[12:15], v[60:63]\n"
    "v_mov_b32 v85, s75\n"
    "s_waitcnt vmcnt(5)\n"
    "buffer_store_dword v85, v83, s[20:23], 0 offen sc1\n"
    "s_barrier\n"
    "s_waitcnt lgkmcnt(5)\n"
    "v_mfma_f32_16x16x32_bf16 v[48:51], a[48:51], v[16:19], v[48:51]\n"
    "ds_read_b128 v[8:11], v84 offset:15360\n"
    "v_mfma_f32_16x16x32_bf16 v[52:55], a[120:123], v[16:19], v[52:55]\n"
    "s_waitcnt lgkmcnt(5)\n"
    "v_mfma_f32_16x16x32_bf16 v[56:59], a[48:51], v[20:23], v[56:59]\n"
    "ds_read_b128 v[12:15], v84 offset:33792\n"
    "v_mfma_f32_16x16x32_bf16 v[60:63], a[120:123], v[20:23], v[60:63]\n"
    "s_waitcnt lgkmcnt(5)\n"
    "v_mfma_f32_16x16x32_bf16 v[48:51], a[52:55], v[24:27], v[48:51]\n"
    "ds_read_b128 v[16:19], v84 offset:16384\n"
    "v_mfma_f32_16x16x32_bf16 v[52:55], a[124:127], v[24:27], v[52:55]\n"
    "s_add_u32 m0, s78, 5120\n"
    "s_add_u32 s64, s77, 5120\n"
    "buffer_load_dwordx4 v82, s[24:27], s64 offen lds\n"
    "s_waitcnt lgkmcnt(5)\n"
    "v_mfma_f32_16x16x32_bf16 v[56:59], a[52:55], v[28:31], v[56:59]\n"
    "ds_read_b128 v[20:23], v84 offset:34816\n"
    "v_mfma_f32_16x16x32_bf16 v[60:63], a[124:127], v[28:31], v[60:63]\n"
    "s_waitcnt lgkmcnt(5)\n"
    "v_mfma_f32_16x16x32_bf16 v[48:51], a[56:59], v[0:3], v[48:51]\n"
    "ds_read_b128 v[24:27], v84 offset:17408\n"
    "v_mfma_f32_16x16x32_bf16 v[52:55], a[128:131], v[0:3], v[52:55]\n"
    "s_add_u32 m0, s78, 6144\n"
    "s_add_u32 s64, s77, 6144\n"
    "buffer_load_dwordx4 v82, s[24:27], s64 offen lds\n"
    "s_waitcnt lgkmcnt(5)\n"
    "v_mfma_f32_16x16x32_bf16 v[56:59], a[56:59], v[4:7], v[56:59]\n"
    "ds_read_b128 v[28:31], v84 offset:35840\n"
    "v_mfma_f32_16x16x32_bf16 v[60:63], a[128:131], v[4:7], v[60:63]\n"
    "s_waitcnt lgkmcnt(5)\n"
    "v_mfma_f32_16x16x32_bf16 v[48:51], a[60:63], v[8:11], v[48:51]\n"
    "ds_read_b128 v[0:3], v72\n"
    "v_mfma_f32_16x16x32_bf16 v[52:55], a[132:135], v[8:11], v[52:55]\n"
    "s_add_u32 m0, s78, 7168\n"
    "s_add_u32 s64, s77, 7168\n"
    "buffer_load_dwordx4 v82, s[24:27], s64 offen lds\n"
    "s_waitcnt lgkmcnt(5)\n"
    "v_mfma_f32_16x16x32_bf16 v[56:59], a[60:63], v[12:15], v[56:59]\n"
    "ds_read_b128 v[4:7], v72 offset:18432\n"
    "v_mfma_f32_16x16x32_bf16 v[60:63], a[132:135], v[12:15], v[60:63]\n"
    "s_waitcnt lgkmcnt(5)\n"
    "v_mfma_f32_16x16x32_bf16 v[48:51], a[64:67], v[16:19], v[48:51]\n"
    "ds_read_b128 v[8:11], v72 offset:1024\n"
    "v_mfma_f32_16x16x32_bf16 v[52:55], a[136:139], v[16:19], v[52:55]\n"
    "s_add_u32 m0, s78, 8192\n"
    "s_add_u32 s64, s77, 8192\n"
    "buffer_load_dwordx4 v82, s[24:27], s64 offen lds\n"
    "s_waitcnt lgkmcnt(5)\n"
    "v_mfma_f32_16x16x32_bf16 v[56:59], a[64:67], v[20:23], v[56:59]\n"
    "ds_read_b128 v[12:15], v72 offset:19456\n"
    "v_mfma_f32_16x16x32_bf16 v[60:63], a[136:139], v[20:23], v[60:63]\n"
    "s_waitcnt lgkmcnt(5)\n"
    "v_mfma_f32_16x16x32_bf16 v[48:51], a[68:71], v[24:27], v[48:51]\n"
    "ds_read_b128 v[16:19], v72 offset:2048\n"
    "v_mfma_f32_16x16x32_bf16 v[52:55], a[140:143], v[24:27], v[52:55]\n"
    "s_waitcnt lgkmcnt(5)\n"
    "v_mfma_f32_16x16x32_bf16 v[56:59], a[68:71], v[28:31], v[56:59]\n"
    "ds_read_b128 v[20:23], v72 offset:20480\n"
    "v_mfma_f32_16x16x32_bf16 v[60:63], a[140:143], v[28:31], v[60:63]\n"
    "s_add_u32 s77, s77, 73728\n"
    "s_add_u32 s78, s78, 36864\n"
    "s_sub_u32 s64, s78, 147456\n"
    "s_cmp_ge_u32 s78, 147456\n"
    "s_cselect_b32 s78, s64, s78\n"
    "v_mov_b32 v84, v72\n"
    "s_add_u32 s79, s79, 16384\n"
    "s_add_u32 s75, s75, 1\n"
    "s_cmp_lt_u32 s75, 32\n"
    "s_cbranch_scc1 pj_loopq_%=\n"
    "s_sub_u32 s67, s79, 16384\n"
    "v_cvt_pk_bf16_f32 v64, v48, v49\n"
    "v_cvt_pk_bf16_f32 v65, v50, v51\n"
    "v_cvt_pk_bf16_f32 v66, v52, v53\n"
    "v_cvt_pk_bf16_f32 v67, v54, v55\n"
    "buffer_store_dwordx4 v[64:67], v82, s[28:31], s67 offen sc1\n"
    "s_nop 1\n"
    "s_add_u32 s67, s67, 4096\n"
    "v_cvt_pk_bf16_f32 v68, v56, v57\n"
    "v_cvt_pk_bf16_f32 v69, v58, v59\n"
    "v_cvt_pk_bf16_f32 v70, v60, v61\n"
    "v_cvt_pk_bf16_f32 v71, v62, v63\n"
    "buffer_store_dwordx4 v[68:71], v82, s[28:31], s67 offen sc1\n"
    "s_nop 1\n"
    "s_add_u32 s67, s67, 4096\n"
    "s_branch pj_end_%=\n"
    "pj_gov_%=:\n"
    "s_lshl_b32 s64, s71, 11\n"
    "s_add_u32 s79, s79, s64\n"
    "ds_read_b128 v[0:3], v84\n"
    "ds_read_b128 v[4:7], v84 offset:18432\n"
    "ds_read_b128 v[8:11], v84 offset:1024\n"
    "ds_read_b128 v[12:15], v84 offset:19456\n"
    "ds_read_b128 v[16:19], v84 offset:2048\n"
    "ds_read_b128 v[20:23], v84 offset:20480\n"
    "pj_loopv_%=:\n"
    "s_sub_u32 s67, s79, 16384\n"
    "s_cmp_eq_u32 s75, 0\n"
    "s_cselect_b32 s67, 0x7ff00000, s67\n"
    "s_add_u32 s76, s76, 36864\n"
    "s_cmp_eq_u32 s76, 147456\n"
    "s_cselect_b32 s76, 0, s76\n"
    "v_add_u32 v72, s76, v82\n"
    "s_waitcnt lgkmcnt(5)\n"
    "v_mfma_f32_16x16x32_bf16 v[32:35], v[0:3], a[0:3], 0\n"
    "ds_read_b128 v[24:27], v84 offset:3072\n"
    "v_mfma_f32_16x16x32_bf16 v[36:39], v[0:3], a[72:75], 0\n"
    "s_waitcnt lgkmcnt(5)\n"
    "v_mfma_f32_16x16x32_bf16 v[40:43], v[4:7], a[0:3], 0\n"
    "ds_read_b128 v[28:31], v84 offset:21504\n"
    "v_cvt_pk_bf16_f32 v64, v48, v49\n"
    "v_mfma_f32_16x16x32_bf16 v[44:47], v[4:7], a[72:75], 0\n"
    "s_waitcnt lgkmcnt(5)\n"
    "v_mfma_f32_16x16x32_bf16 v[32:35], v[8:11], a[4:7], v[32:35]\n"
    "ds_read_b128 v[0:3], v84 offset:4096\n"
    "v_cvt_pk_bf16_f32 v65, v50, v51\n"
    "v_mfma_f32_16x16x32_bf16 v[36:39], v[8:11], a[76:79], v[36:39]\n"
    "s_waitcnt lgkmcnt(5)\n"
    "v_mfma_f32_16x16x32_bf16 v[40:43], v[12:15], a[4:7], v[40:43]\n"
    "ds_read_b128 v[4:7], v84 offset:22528\n"
    "v_cvt_pk_bf16_f32 v66, v56, v57\n"
    "v_mfma_f32_16x16x32_bf16 v[44:47], v[12:15], a[76:79], v[44:47]\n"
    "s_waitcnt lgkmcnt(5)\n"
    "v_mfma_f32_16x16x32_bf16 v[32:35], v[16:19], a[8:11], v[32:35]\n"
    "ds_read_b128 v[8:11], v84 offset:5120\n"
    "v_cvt_pk_bf16_f32 v67, v58, v59\n"
    "v_mfma_f32_16x16x32_bf16 v[36:39], v[16:19], a[80:83], v[36:39]\n"
    "s_waitcnt lgkmcnt(5)\n"
    "v_mfma_f32_16x16x32_bf16 v[40:43], v[20:23], a[8:11], v[40:43]\n"
    "ds_read_b128 v[12:15], v84 offset:23552\n"
    "buffer_store_dwordx4 v[64:67], v82, s[28:31], s67 offen sc1\n"
    "v_mfma_f32_16x16x32_bf16 v[44:47], v[20:23], a[80:83], v[44:47]\n"
    "s_waitcnt lgkmcnt(5)\n"
    "v_mfma_f32_16x16x32_bf16 v[32:35], v[24:27], a[12:15], v[32:35]\n"
    "ds_read_b128 v[16:19], v84 offset:6144\n"
    "v_cvt_pk_bf16_f32 v68, v52, v53\n"
    "v_mfma_f32_16x16x32_bf16 v[36:39], v[24:27], a[84:87], v[36:39]\n"
    "s_waitcnt lgkmcnt(5)\n"
    "v_mfma_f32_16x16x32_bf16 v[40:43], v[28:31], a[12:15], v[40:43]\n"
    "ds_read_b128 v[20:23], v84 offset:24576\n"
    "v_cvt_pk_bf16_f32 v69, v54, v55\n"
    "v_mfma_f32_16x16x32_bf16 v[44:47], v[28:31], a[84:87], v[44:47]\n"
    "s_waitcnt lgkmcnt(5)\n"
    "v_mfma_f32_16x16x32_bf16 v[32:35], v[0:3], a[16:19], v[32:35]\n"
    "ds_read_b128 v[24:27], v84 offset:7168\n"
    "v_cvt_pk_bf16_f32 v70, v60, v61\n"
    "v_mfma_f32_16x16x32_bf16 v[36:39], v[0:3], a[88:91], v[36:39]\n"
    "s_waitcnt lgkmcnt(5)\n"
    "v_mfma_f32_16x16x32_bf16 v[40:43], v[4:7], a[16:19], v[40:43]\n"
    "ds_read_b128 v[28:31], v84 offset:25600\n"
    "v_cvt_pk_bf16_f32 v71, v62, v63\n"
    "v_mfma_f32_16x16x32_bf16 v[44:47], v[4:7], a[88:91], v[44:47]\n"
    "s_waitcnt lgkmcnt(5)\n"
    "v_mfma_f32_16x16x32_bf16 v[32:35], v[8:11], a[20:23], v[32:35]\n"
    "ds_read_b128 v[0:3], v84 offset:8192\n"
    "buffer_store_dwordx4 v[68:71], v82, s[28:31], s67 offen offset:1024 sc1\n"
    "v_mfma_f32_16x16x32_bf16 v[36:39], v[8:11], a[92:95], v[36:39]\n"
    "s_waitcnt lgkmcnt(5)\n"
    "v_mfma_f32_16x16x32_bf16 v[40:43], v[12:15], a[20:23], v[40:43]\n"
    "ds_read_b128 v[4:7], v84 offset:26624\n"
    "v_mfma_f32_16x16x32_bf16 v[44:47], v[12:15], a[92:95], v[44:47]\n"
    "s_waitcnt lgkmcnt(5)\n"
    "v_mfma_f32_16x16x32_bf16 v[32:35], v[16:19], a[24:27], v[32:35]\n"
    "ds_read_b128 v[8:11], v84 offset:9216\n"
    "v_mfma_f32_16x16x32_bf16 v[36:39], v[16:19], a[96:99], v[36:39]\n"
    "s_waitcnt lgkmcnt(5)\n"
    "v_mfma_f32_16x16x32_bf16 v[40:43], v[20:23], a[24:27], v[40:43]\n"
    "ds_read_b128 v[12:15], v84 offset:27648\n"
    "v_mfma_f32_16x16x32_bf16 v[44:47], v[20:23], a[96:99], v[44:47]\n"
    "s_waitcnt lgkmcnt(5)\n"
    "v_mfma_f32_16x16x32_bf16 v[32:35], v[24:27], a[28:31], v[32:35]\n"
    "ds_read_b128 v[16:19], v84 offset:10240\n"
    "v_mfma_f32_16x16x32_bf16 v[36:39], v[24:27], a[100:103], v[36:39]\n"
    "s_add_u32 m0, s78, 0\n"
    "s_add_u32 s64, s77, 0\n"
    "buffer_load_dwordx4 v82, s[24:27], s64 offen lds\n"
    "s_waitcnt lgkmcnt(5)\n"
    "v_mfma_f32_16x16x32_bf16 v[40:43], v[28:31], a[28:31], v[40:43]\n"
    "ds_read_b128 v[20:23], v84 offset:28672\n"
    "v_mfma_f32_16x16x32_bf16 v[44:47], v[28:31], a[100:103], v[44:47]\n"
    "s_waitcnt lgkmcnt(5)\n"
    "v_mfma_f32_16x16x32_bf16 v[32:35], v[0:3], a[32:35], v[32:35]\n"
    "ds_read_b128 v[24:27], v84 offset:11264\n"
    "v_mfma_f32_16x16x32_bf16 v[36:39], v[0:3], a[104:107], v[36:39]\n"
    "s_add_u32 m0, s78, 1024\n"
    "s_add_u32 s64, s77, 1024\n"
    "buffer_load_dwordx4 v82, s[24:27], s64 offen lds\n"
    "s_waitcnt lgkmcnt(5)\n"
    "v_mfma_f32_16x16x32_bf16 v[40:43], v[4:7], a[32:35], v[40:43]\n"
    "ds_read_b128 v[28:31], v84 offset:29696\n"
    "v_mfma_f32_16x16x32_bf16 v[44:47], v[4:7], a[104:107], v[44:47]\n"
    "s_waitcnt lgkmcnt(5)\n"
    "v_mfma_f32_16x16x32_bf16 v[32:35], v[8:11], a[36:39], v[32:35]\n"
    "ds_read_b128 v[0:3], v84 offset:12288\n"
    "v_mfma_f32_16x16x32_bf16 v[36:39], v[8:11], a[108:111], v[36:39]\n"
    "s_add_u32 m0, s78, 2048\n"
    "s_add_u32 s64, s77, 2048\n"
    "buffer_load_dwordx4 v82, s[24:27], s64 offen lds\n"
    "s_waitcnt lgkmcnt(5)\n"
    "v_mfma_f32_16x16x32_bf16 v[40:43], v[12:15], a[36:39], v[40:43]\n"
    "ds_read_b128 v[4:7], v84 offset:30720\n"
    "v_mfma_f32_16x16x32_bf16 v[44:47], v[12:15], a[108:111], v[44:47]\n"
    "s_waitcnt lgkmcnt(5)\n"
    "v_mfma_f32_16x16x32_bf16 v[32:35], v[16:19], a[40:43], v[32:35]\n"
    "ds_read_b128 v[8:11], v84 offset:13312\n"
    "v_mfma_f32_16x16x32_bf16 v[36:39], v[16:19], a[112:115], v[36:39]\n"
    "s_add_u32 m0, s78, 3072\n"
    "s_add_u32 s64, s77, 3072\n"
    "buffer_load_dwordx4 v82, s[24:27], s64 offen lds\n"
    "s_waitcnt lgkmcnt(5)\n"
    "v_mfma_f32_16x16x32_bf16 v[40:43], v[20:23], a[40:43], v[40:43]\n"
    "ds_read_b128 v[12:15], v84 offset:31744\n"
    "v_mfma_f32_16x16x32_bf16 v[44:47], v[20:23], a[112:115], v[44:47]\n"
    "s_waitcnt lgkmcnt(5)\n"
    "v_mfma_f32_16x16x32_bf16 v[32:35], v[24:27], a[44:47], v[32:35]\n"
    "ds_read_b128 v[16:19], v84 offset:14336\n"
    "v_mfma_f32_16x16x32_bf16 v[36:39], v[24:27], a[116:119], v[36:39]\n"
    "s_add_u32 m0, s78, 4096\n"
    "s_add_u32 s64, s77, 4096\n"
    "buffer_load_dwordx4 v82, s[24:27], s64 offen lds\n"
    "s_waitcnt lgkmcnt(5)\n"
    "v_mfma_f32_16x16x32_bf16 v[40:43], v[28:31], a[44:47], v[40:43]\n"
    "ds_read_b128 v[20:23], v84 offset:32768\n"
    "v_mfma_f32_16x16x32_bf16 v[44:47], v[28:31], a[116:119], v[44:47]\n"
    "v_mov_b32 v85, s75\n"
    "s_waitcnt vmcnt(5)\n"
    "buffer_store_dword v85, v83, s[20:23], 0 offen sc1\n"
    "s_barrier\n"
    "s_waitcnt lgkmcnt(5)\n"
    "v_mfma_f32_16x16x32_bf16 v[32:35], v[0:3], a[48:51], v[32:35]\n"
    "ds_read_b128 v[24:27], v84 offset:15360\n"
    "v_mfma_f32_16x16x32_bf16 v[36:39], v[0:3], a[120:123], v[36:39]\n"
    "s_waitcnt lgkmcnt(5)\n"
    "v_mfma_f32_16x16x32_bf16 v[40:43], v[4:7], a[48:51], v[40:43]\n"
    "ds_read_b128 v[28:31], v84 offset:33792\n"
    "v_mfma_f32_16x16x32_bf16 v[44:47], v[4:7], a[120:123], v[44:47]\n"
    "s_waitcnt lgkmcnt(5)\n"
    "v_mfma_f32_16x16x32_bf16 v[32:35], v[8:11], a[52:55], v[32:35]\n"
    "ds_read_b128 v[0:3], v84 offset:16384\n"
    "v_mfma_f32_16x16x32_bf16 v[36:39], v[8:11], a[124:127], v[36:39]\n"
    "s_add_u32 m0, s78, 5120\n"
    "s_add_u32 s64, s77, 5120\n"
    "buffer_load_dwordx4 v82, s[24:27], s64 offen lds\n"
    "s_waitcnt lgkmcnt(5)\n"
    "v_mfma_f32_16x16x32_bf16 v[40:43], v[12:15], a[52:55], v[40:43]\n"
    "ds_read_b128 v[4:7], v84 offset:34816\n"
    "v_mfma_f32_16x16x32_bf16 v[44:47], v[12:15], a[124:127], v[44:47]\n"
    "s_waitcnt lgkmcnt(5)\n"
    "v_mfma_f32_16x16x32_bf16 v[32:35], v[16:19], a[56:59], v[32:35]\n"
    "ds_read_b128 v[8:11], v84 offset:17408\n"
    "v_mfma_f32_16x16x32_bf16 v[36:39], v[16:19], a[128:131], v[36:39]\n"
    "s_add_u32 m0, s78, 6144\n"
    "s_add_u32 s64, s77, 6144\n"
    "buffer_load_dwordx4 v82, s[24:27], s64 offen lds\n"
    "s_waitcnt lgkmcnt(5)\n"
    "v_mfma_f32_16x16x32_bf16 v[40:43], v[20:23], a[56:59], v[40:43]\n"
    "ds_read_b128 v[12:15], v84 offset:35840\n"
    "v_mfma_f32_16x16x32_bf16 v[44:47], v[20:23], a[128:131], v[44:47]\n"
    "s_waitcnt lgkmcnt(5)\n"
    "v_mfma_f32_16x16x32_bf16 v[32:35], v[24:27], a[60:63], v[32:35]\n"
    "ds_read_b128 v[16:19], v72\n"
    "v_mfma_f32_16x16x32_bf16 v[36:39], v[24:27], a[132:135], v[36:39]\n"
    "s_add_u32 m0, s78, 7168\n"
    "s_add_u32 s64, s77, 7168\n"
    "buffer_load_dwordx4 v82, s[24:27], s64 offen lds\n"
    "s_waitcnt lgkmcnt(5)\n"
    "v_mfma_f32_16x16x32_bf16 v[40:43], v[28:31], a[60:63], v[40:43]\n"
    "ds_read_b128 v[20:23], v72 offset:18432\n"
    "v_mfma_f32_16x16x32_bf16 v[44:47], v[28:31], a[132:135], v[44:47]\n"
    "s_waitcnt lgkmcnt(5)\n"
    "v_mfma_f32_16x16x32_bf16 v[32:35], v[0:3], a[64:67], v[32:35]\n"
    "ds_read_b128 v[24:27], v72 offset:1024\n"
    "v_mfma_f32_16x16x32_bf16 v[36:39], v[0:3], a[136:139], v[36:39]\n"
    "s_add_u32 m0, s78, 8192\n"
    "s_add_u32 s64, s77, 8192\n"
    "buffer_load_dwordx4 v82, s[24:27], s64 offen lds\n"
    "s_waitcnt lgkmcnt(5)\n"
    "v_mfma_f32_16x16x32_bf16 v[40:43], v[4:7], a[64:67], v[40:43]\n"
    "ds_read_b128 v[28:31], v72 offset:19456\n"
    "v_mfma_f32_16x16x32_bf16 v[44:47], v[4:7], a[136:139], v[44:47]\n"
    "s_waitcnt lgkmcnt(5)\n"
    "v_mfma_f32_16x16x32_bf16 v[32:35], v[8:11], a[68:71], v[32:35]\n"
    "ds_read_b128 v[0:3], v72 offset:2048\n"
    "v_mfma_f32_16x16x32_bf16 v[36:39], v[8:11], a[140:143], v[36:39]\n"
    "s_waitcnt lgkmcnt(5)\n"
    "v_mfma_f32_16x16x32_bf16 v[40:43], v[12:15], a[68:71], v[40:43]\n"
    "ds_read_b128 v[4:7], v72 offset:20480\n"
    "v_mfma_f32_16x16x32_bf16 v[44:47], v[12:15], a[140:143], v[44:47]\n"
    "s_add_u32 s77, s77, 73728\n"
    "s_add_u32 s78, s78, 36864\n"
    "s_sub_u32 s64, s78, 147456\n"
    "s_cmp_ge_u32 s78, 147456\n"
    "s_cselect_b32 s78, s64, s78\n"
    "v_mov_b32 v84, v72\n"
    "s_add_u32 s79, s79, 16384\n"
    "s_add_u32 s75, s75, 1\n"
    "s_sub_u32 s67, s79, 16384\n"
    "s_cmp_eq_u32 s75, 0\n"
    "s_cselect_b32 s67, 0x7ff00000, s67\n"
    "s_add_u32 s76, s76, 36864\n"
    "s_cmp_eq_u32 s76, 147456\n"
    "s_cselect_b32 s76, 0, s76\n"
    "v_add_u32 v72, s76, v82\n"
    "s_waitcnt lgkmcnt(5)\n"
    "v_mfma_f32_16x16x32_bf16 v[48:51], v[16:19], a[0:3], 0\n"
    "ds_read_b128 v[8:11], v84 offset:3072\n"
    "v_mfma_f32_16x16x32_bf16 v[52:55], v[16:19], a[72:75], 0\n"
    "s_waitcnt lgkmcnt(5)\n"
    "v_mfma_f32_16x16x32_bf16 v[56:59], v[20:23], a[0:3], 0\n"
    "ds_read_b128 v[12:15], v84 offset:21504\n"
    "v_cvt_pk_bf16_f32 v64, v32, v33\n"
    "v_mfma_f32_16x16x32_bf16 v[60:63], v[20:23], a[72:75], 0\n"
    "s_waitcnt lgkmcnt(5)\n"
    "v_mfma_f32_16x16x32_bf16 v[48:51], v[24:27], a[4:7], v[48:51]\n"
    "ds_read_b128 v[16:19], v84 offset:4096\n"
    "v_cvt_pk_bf16_f32 v65, v34, v35\n"
    "v_mfma_f32_16x16x32_bf16 v[52:55], v[24:27], a[76:79], v[52:55]\n"
    "s_waitcnt lgkmcnt(5)\n"
    "v_mfma_f32_16x16x32_bf16 v[56:59], v[28:31], a[4:7], v[56:59]\n"
    "ds_read_b128 v[20:23], v84 offset:22528\n"
    "v_cvt_pk_bf16_f32 v66, v40, v41\n"
    "v_mfma_f32_16x16x32_bf16 v[60:63], v[28:31], a[76:79], v[60:63]\n"
    "s_waitcnt lgkmcnt(5)\n"
    "v_mfma_f32_16x16x32_bf16 v[48:51], v[0:3], a[8:11], v[48:51]\n"
    "ds_read_b128 v[24:27], v84 offset:5120\n"
    "v_cvt_pk_bf16_f32 v67, v42, v43\n"
    "v_mfma_f32_16x16x32_bf16 v[52:55], v[0:3], a[80:83], v[52:55]\n"
    "s_waitcnt lgkmcnt(5)\n"
    "v_mfma_f32_16x16x32_bf16 v[56:59], v[4:7], a[8:11], v[56:59]\n"
    "ds_read_b128 v[28:31], v84 offset:23552\n"
    "buffer_store_dwordx4 v[64:67], v82, s[28:31], s67 offen sc1\n"
    "v_mfma_f32_16x16x32_bf16 v[60:63], v[4:7], a[80:83], v[60:63]\n"
    "s_waitcnt lgkmcnt(5)\n"
    "v_mfma_f32_16x16x32_bf16 v[48:51], v[8:11], a[12:15], v[48:51]\n"
    "ds_read_b128 v[0:3], v84 offset:6144\n"
    "v_cvt_pk_bf16_f32 v68, v36, v37\n"
    "v_mfma_f32_16x16x32_bf16 v[52:55], v[8:11], a[84:87], v[52:55]\n"
    "s_waitcnt lgkmcnt(5)\n"
    "v_mfma_f32_16x16x32_bf16 v[56:59], v[12:15], a[12:15], v[56:59]\n"
    "ds_read_b128 v[4:7], v84 offset:24576\n"
    "v_cvt_pk_bf16_f32 v69, v38, v39\n"
    "v_mfma_f32_16x16x32_bf16 v[60:63], v[12:15], a[84:87], v[60:63]\n"
    "s_waitcnt lgkmcnt(5)\n"
    "v_mfma_f32_16x16x32_bf16 v[48:51], v[16:19], a[16:19], v[48:51]\n"
    "ds_read_b128 v[8:11], v84 offset:7168\n"
    "v_cvt_pk_bf16_f32 v70, v44, v45\n"
    "v_mfma_f32_16x16x32_bf16 v[52:55], v[16:19], a[88:91], v[52:55]\n"
    "s_waitcnt lgkmcnt(5)\n"
    "v_mfma_f32_16x16x32_bf16 v[56:59], v[20:23], a[16:19], v[56:59]\n"
    "ds_read_b128 v[12:15], v84 offset:25600\n"
    "v_cvt_pk_bf16_f32 v71, v46, v47\n"
    "v_mfma_f32_16x16x32_bf16 v[60:63], v[20:23], a[88:91], v[60:63]\n"
    "s_waitcnt lgkmcnt(5)\n"
    "v_mfma_f32_16x16x32_bf16 v[48:51], v[24:27], a[20:23], v[48:51]\n"
    "ds_read_b128 v[16:19], v84 offset:8192\n"
    "buffer_store_dwordx4 v[68:71], v82, s[28:31], s67 offen offset:1024 sc1\n"
    "v_mfma_f32_16x16x32_bf16 v[52:55], v[24:27], a[92:95], v[52:55]\n"
    "s_waitcnt lgkmcnt(5)\n"
    "v_mfma_f32_16x16x32_bf16 v[56:59], v[28:31], a[20:23], v[56:59]\n"
    "ds_read_b128 v[20:23], v84 offset:26624\n"
    "v_mfma_f32_16x16x32_bf16 v[60:63], v[28:31], a[92:95], v[60:63]\n"
    "s_waitcnt lgkmcnt(5)\n"
    "v_mfma_f32_16x16x32_bf16 v[48:51], v[0:3], a[24:27], v[48:51]\n"
    "ds_read_b128 v[24:27], v84 offset:9216\n"
    "v_mfma_f32_16x16x32_bf16 v[52:55], v[0:3], a[96:99], v[52:55]\n"
    "s_waitcnt lgkmcnt(5)\n"
    "v_mfma_f32_16x16x32_bf16 v[56:59], v[4:7], a[24:27], v[56:59]\n"
    "ds_read_b128 v[28:31], v84 offset:27648\n"
    "v_mfma_f32_16x16x32_bf16 v[60:63], v[4:7], a[96:99], v[60:63]\n"
    "s_waitcnt lgkmcnt(5)\n"
    "v_mfma_f32_16x16x32_bf16 v[48:51], v[8:11], a[28:31], v[48:51]\n"
    "ds_read_b128 v[0:3], v84 offset:10240\n"
    "v_mfma_f32_16x16x32_bf16 v[52:55], v[8:11], a[100:103], v[52:55]\n"
    "s_add_u32 m0, s78, 0\n"
    "s_add_u32 s64, s77, 0\n"
    "buffer_load_dwordx4 v82, s[24:27], s64 offen lds\n"
    "s_waitcnt lgkmcnt(5)\n"
    "v_mfma_f32_16x16x32_bf16 v[56:59], v[12:15], a[28:31], v[56:59]\n"
    "ds_read_b128 v[4:7], v84 offset:28672\n"
    "v_mfma_f32_16x16x32_bf16 v[60:63], v[12:15], a[100:103], v[60:63]\n"
    "s_waitcnt lgkmcnt(5)\n"
    "v_mfma_f32_16x16x32_bf16 v[48:51], v[16:19], a[32:35], v[48:51]\n"
    "ds_read_b128 v[8:11], v84 offset:11264\n"
    "v_mfma_f32_16x16x32_bf16 v[52:55], v[16:19], a[104:107], v[52:55]\n"
    "s_add_u32 m0, s78, 1024\n"
    "s_add_u32 s64, s77, 1024\n"
    "buffer_load_dwordx4 v82, s[24:27], s64 offen lds\n"
    "s_waitcnt lgkmcnt(5)\n"
    "v_mfma_f32_16x16x32_bf16 v[56:59], v[20:23], a[32:35], v[56:59]\n"
    "ds_read_b128 v[12:15], v84 offset:29696\n"
    "v_mfma_f32_16x16x32_bf16 v[60:63], v[20:23], a[104:107], v[60:63]\n"
    "s_waitcnt lgkmcnt(5)\n"
    "v_mfma_f32_16x16x32_bf16 v[48:51], v[24:27], a[36:39], v[48:51]\n"
    "ds_read_b128 v[16:19], v84 offset:12288\n"
    "v_mfma_f32_16x16x32_bf16 v[52:55], v[24:27], a[108:111], v[52:55]\n"
    "s_add_u32 m0, s78, 2048\n"
    "s_add_u32 s64, s77, 2048\n"
    "buffer_load_dwordx4 v82, s[24:27], s64 offen lds\n"
    "s_waitcnt lgkmcnt(5)\n"
    "v_mfma_f32_16x16x32_bf16 v[56:59], v[28:31], a[36:39], v[56:59]\n"
    "ds_read_b128 v[20:23], v84 offset:30720\n"
    "v_mfma_f32_16x16x32_bf16 v[60:63], v[28:31], a[108:111], v[60:63]\n"
    "s_waitcnt lgkmcnt(5)\n"
    "v_mfma_f32_16x16x32_bf16 v[48:51], v[0:3], a[40:43], v[48:51]\n"
    "ds_read_b128 v[24:27], v84 offset:13312\n"
    "v_mfma_f32_16x16x32_bf16 v[52:55], v[0:3], a[112:115], v[52:55]\n"
    "s_add_u32 m0, s78, 3072\n"
    "s_add_u32 s64, s77, 3072\n"
    "buffer_load_dwordx4 v82, s[24:27], s64 offen lds\n"
    "s_waitcnt lgkmcnt(5)\n"
    "v_mfma_f32_16x16x32_bf16 v[56:59], v[4:7], a[40:43], v[56:59]\n"
    "ds_read_b128 v[28:31], v84 offset:31744\n"
    "v_mfma_f32_16x16x32_bf16 v[60:63], v[4:7], a[112:115], v[60:63]\n"
    "s_waitcnt lgkmcnt(5)\n"
    "v_mfma_f32_16x16x32_bf16 v[48:51], v[8:11], a[44:47], v[48:51]\n"
    "ds_read_b128 v[0:3], v84 offset:14336\n"
    "v_mfma_f32_16x16x32_bf16 v[52:55], v[8:11], a[116:119], v[52:55]\n"
    "s_add_u32 m0, s78, 4096\n"
    "s_add_u32 s64, s77, 4096\n"
    "buffer_load_dwordx4 v82, s[24:27], s64 offen lds\n"
    "s_waitcnt lgkmcnt(5)\n"
    "v_mfma_f32_16x16x32_bf16 v[56:59], v[12:15], a[44:47], v[56:59]\n"
    "ds_read_b128 v[4:7], v84 offset:32768\n"
    "v_mfma_f32_16x16x32_bf16 v[60:63], v[12:15], a[116:119], v[60:63]\n"
    "v_mov_b32 v85, s75\n"
    "s_waitcnt vmcnt(5)\n"
    "buffer_store_dword v85, v83, s[20:23], 0 offen sc1\n"
    "s_barrier\n"
    "s_waitcnt lgkmcnt(5)\n"
    "v_mfma_f32_16x16x32_bf16 v[48:51], v[16:19], a[48:51], v[48:51]\n"
    "ds_read_b128 v[8:11], v84 offset:15360\n"
    "v_mfma_f32_16x16x32_bf16 v[52:55], v[16:19], a[120:123], v[52:55]\n"
    "s_waitcnt lgkmcnt(5)\n"
    "v_mfma_f32_16x16x32_bf16 v[56:59], v[20:23], a[48:51], v[56:59]\n"
    "ds_read_b128 v[12:15], v84 offset:33792\n"
    "v_mfma_f32_16x16x32_bf16 v[60:63], v[20:23], a[120:123], v[60:63]\n"
    "s_waitcnt lgkmcnt(5)\n"
    "v_mfma_f32_16x16x32_bf16 v[48:51], v[24:27], a[52:55], v[48:51]\n"
    "ds_read_b128 v[16:19], v84 offset:16384\n"
    "v_mfma_f32_16x16x32_bf16 v[52:55], v[24:27], a[124:127], v[52:55]\n"
    "s_add_u32 m0, s78, 5120\n"
    "s_add_u32 s64, s77, 5120\n"
    "buffer_load_dwordx4 v82, s[24:27], s64 offen lds\n"
    "s_waitcnt lgkmcnt(5)\n"
    "v_mfma_f32_16x16x32_bf16 v[56:59], v[28:31], a[52:55], v[56:59]\n"
    "ds_read_b128 v[20:23], v84 offset:34816\n"
    "v_mfma_f32_16x16x32_bf16 v[60:63], v[28:31], a[124:127], v[60:63]\n"
    "s_waitcnt lgkmcnt(5)\n"
    "v_mfma_f32_16x16x32_bf16 v[48:51], v[0:3], a[56:59], v[48:51]\n"
    "ds_read_b128 v[24:27], v84 offset:17408\n"
    "v_mfma_f32_16x16x32_bf16 v[52:55], v[0:3], a[128:131], v[52:55]\n"
    "s_add_u32 m0, s78, 6144\n"
    "s_add_u32 s64, s77, 6144\n"
    "buffer_load_dwordx4 v82, s[24:27], s64 offen lds\n"
    "s_waitcnt lgkmcnt(5)\n"
    "v_mfma_f32_16x16x32_bf16 v[56:59], v[4:7], a[56:59], v[56:59]\n"
    "ds_read_b128 v[28:31], v84 offset:35840\n"
    "v_mfma_f32_16x16x32_bf16 v[60:63], v[4:7], a[128:131], v[60:63]\n"
    "s_waitcnt lgkmcnt(5)\n"
    "v_mfma_f32_16x16x32_bf16 v[48:51], v[8:11], a[60:63], v[48:51]\n"
    "ds_read_b128 v[0:3], v72\n"
    "v_mfma_f32_16x16x32_bf16 v[52:55], v[8:11], a[132:135], v[52:55]\n"
    "s_add_u32 m0, s78, 7168\n"
    "s_add_u32 s64, s77, 7168\n"
    "buffer_load_dwordx4 v82, s[24:27], s64 offen lds\n"
    "s_waitcnt lgkmcnt(5)\n"
    "v_mfma_f32_16x16x32_bf16 v[56:59], v[12:15], a[60:63], v[56:59]\n"
    "ds_read_b128 v[4:7], v72 offset:18432\n"
    "v_mfma_f32_16x16x32_bf16 v[60:63], v[12:15], a[132:135], v[60:63]\n"
    "s_waitcnt lgkmcnt(5)\n"
    "v_mfma_f32_16x16x32_bf16 v[48:51], v[16:19], a[64:67], v[48:51]\n"
    "ds_read_b128 v[8:11], v72 offset:1024\n"
    "v_mfma_f32_16x16x32_bf16 v[52:55], v[16:19], a[136:139], v[52:55]\n"
    "s_add_u32 m0, s78, 8192\n"
    "s_add_u32 s64, s77, 8192\n"
    "buffer_load_dwordx4 v82, s[24:27], s64 offen lds\n"
    "s_waitcnt lgkmcnt(5)\n"
    "v_mfma_f32_16x16x32_bf16 v[56:59], v[20:23], a[64:67], v[56:59]\n"
    "ds_read_b128 v[12:15], v72 offset:19456\n"
    "v_mfma_f32_16x16x32_bf16 v[60:63], v[20:23], a[136:139], v[60:63]\n"
    "s_waitcnt lgkmcnt(5)\n"
    "v_mfma_f32_16x16x32_bf16 v[48:51], v[24:27], a[68:71], v[48:51]\n"
    "ds_read_b128 v[16:19], v72 offset:2048\n"
    "v_mfma_f32_16x16x32_bf16 v[52:55], v[24:27], a[140:143], v[52:55]\n"
    "s_waitcnt lgkmcnt(5)\n"
    "v_mfma_f32_16x16x32_bf16 v[56:59], v[28:31], a[68:71], v[56:59]\n"
    "ds_read_b128 v[20:23], v72 offset:20480\n"
    "v_mfma_f32_16x16x32_bf16 v[60:63], v[28:31], a[140:143], v[60:63]\n"
    "s_add_u32 s77, s77, 73728\n"
    "s_add_u32 s78, s78, 36864\n"
    "s_sub_u32 s64, s78, 147456\n"
    "s_cmp_ge_u32 s78, 147456\n"
    "s_cselect_b32 s78, s64, s78\n"
    "v_mov_b32 v84, v72\n"
    "s_add_u32 s79, s79, 16384\n"
    "s_add_u32 s75, s75, 1\n"
    "s_cmp_lt_u32 s75, 32\n"
    "s_cbranch_scc1 pj_loopv_%=\n"
    "s_sub_u32 s67, s79, 16384\n"
    "v_cvt_pk_bf16_f32 v64, v48, v49\n"
    "v_cvt_pk_bf16_f32 v65, v50, v51\n"
    "v_cvt_pk_bf16_f32 v66, v56, v57\n"
    "v_cvt_pk_bf16_f32 v67, v58, v59\n"
    "buffer_store_dwordx4 v[64:67], v82, s[28:31], s67 offen sc1\n"
    "s_nop 1\n"
    "s_add_u32 s67, s67, 1024\n"
    "v_cvt_pk_bf16_f32 v68, v52, v53\n"
    "v_cvt_pk_bf16_f32 v69, v54, v55\n"
    "v_cvt_pk_bf16_f32 v70, v60, v61\n"
    "v_cvt_pk_bf16_f32 v71, v62, v63\n"
    "buffer_store_dwordx4 v[68:71], v82, s[28:31], s67 offen sc1\n"
    "s_nop 1\n"
    "s_add_u32 s67, s67, 1024\n"
    "s_branch pj_end_%=\n"
    "pj_end_%=:\n"
    "v_mov_b32 v85, 32\n"
    "s_waitcnt vmcnt(0)\n"
    "buffer_store_dword v85, v83, s[20:23], 0 offen sc1\n"
    "s_waitcnt lgkmcnt(0)\n"
    "s_barrier\n"
    :: "s"(__builtin_amdgcn_kernarg_segment_ptr()), "s"(ids)
    : "v0", "v1", "v2", "v3", "v4", "v5", "v6", "v7", "v8", "v9", "v10", "v11", "v12", "v13", "v14", "v15", "v16", "v17", "v18", "v19", "v20", "v21", "v22",
      "v23", "v24", "v25", "v26", "v27", "v28", "v29", "v30", "v31", "v32", "v33", "v34", "v35", "v36", "v37", "v38", "v39", "v40", "v41", "v42", "v43",
      "v44", "v45", "v46", "v47", "v48", "v49", "v50", "v51", "v52", "v53", "v54", "v55", "v56", "v57", "v58", "v59", "v60", "v61", "v62", "v63", "v64",
      "v65", "v66", "v67", "v68", "v69", "v70", "v71", "v72", "v73", "v74", "v75", "v76", "v77", "v78", "v79", "v80", "v81", "v82", "v83", "v84", "v85",
      "v86", "v87", "v88", "v89", "v90", "v91", "v92", "v93", "v94", "v95", "a0", "a1", "a2", "a3", "a4", "a5", "a6", "a7", "a8", "a9", "a10", "a11", "a12",
      "a13", "a14", "a15", "a16", "a17", "a18", "a19", "a20", "a21", "a22", "a23", "a24", "a25", "a26", "a27", "a28", "a29", "a30", "a31", "a32", "a33",
      "a34", "a35", "a36", "a37", "a38", "a39", "a40", "a41", "a42", "a43", "a44", "a45", "a46", "a47", "a48", "a49", "a50", "a51", "a52", "a53", "a54",
      "a55", "a56", "a57", "a58", "a59", "a60", "a61", "a62", "a63", "a64", "a65", "a66", "a67", "a68", "a69", "a70", "a71", "a72", "a73", "a74", "a75",
      "a76", "a77", "a78", "a79", "a80", "a81", "a82", "a83", "a84", "a85", "a86", "a87", "a88", "a89", "a90", "a91", "a92", "a93", "a94", "a95", "a96",
      "a97", "a98", "a99", "a100", "a101", "a102", "a103", "a104", "a105", "a106", "a107", "a108", "a109", "a110", "a111", "a112", "a113", "a114", "a115",
      "a116", "a117", "a118", "a119", "a120", "a121", "a122", "a123", "a124", "a125", "a126", "a127", "a128", "a129", "a130", "a131", "a132", "a133", "a134",
      "a135", "a136", "a137", "a138", "a139", "a140", "a141", "a142", "a143", "s20", "s21", "s22", "s23", "s24", "s25", "s26", "s27", "s28", "s29", "s30",
      "s31", "s36", "s37", "s38", "s39", "s40", "s41", "s42", "s43", "s44", "s45", "s46", "s47", "s48", "s49", "s50", "s51", "s52", "s53", "s54", "s55",
      "s56", "s57", "s58", "s59", "s60", "s61", "s62", "s63", "s64", "s65", "s66", "s67", "s68", "s69", "s70", "s71", "s72", "s73", "s74", "s75", "s76",
      "s77", "s78", "s79", "s80", "s81", "s82", "s83", "s84", "s85", "s86", "s87", "s88", "s89", "s90", "s91", "s92", "s93", "s94", "s95", "s96", "s97",
      "s98", "s99", "vcc", "scc", "memory");
}

DEV void role_ab(u32 ids) {
    asm volatile(
    "s_nop 4\n"
    "s_load_dwordx16 s[40:55], %0, 0x0\n"
    "s_load_dwordx4 s[56:59], %0, 0x40\n"
    "s_load_dwordx2 s[60:61], %0, 0x50\n"
    "s_lshr_b32 s71, %1, 24\n"
    "s_mov_b32 s70, 0\n"
    "s_waitcnt lgkmcnt(0)\n"
    "s_load_dword s42, %0, 0x6c\n"
    "s_waitcnt lgkmcnt(0)\n"
    "s_add_u32 s20, s60, 0\n"
    "s_addc_u32 s21, s61, 0\n"
    "s_and_b32 s21, s21, 0xffff\n"
    "s_mov_b32 s22, 0x300000\n"
    "s_mov_b32 s23, 0x20000\n"
    "s_and_b32 s72, %1, 0xff\n"
    "s_and_b32 s73, s71, 1\n"
    "s_lshr_b32 s74, s71, 1\n"
    "v_mbcnt_lo_u32_b32 v200, -1, 0\n"
    "v_mbcnt_hi_u32_b32 v200, -1, v200\n"
    "v_lshrrev_b32 v208, 3, v200\n"
    "v_and_b32 v209, 6, v208\n"
    "v_and_b32 v201, 7, v200\n"
    "v_xor_b32 v201, v201, v209\n"
    "s_mul_i32 s65, s73, 6\n"
    "v_xor_b32 v201, s65, v201\n"
    "v_lshlrev_b32 v201, 4, v201\n"
    "s_mov_b32 s65, 1152\n"
    "v_mad_u32_u24 v201, v208, s65, v201\n"
    "s_mul_i32 s67, s72, 147456\n"
    "s_add_u32 s36, s50, 0\n"
    "s_addc_u32 s37, s51, 0\n"
    "s_and_b32 s37, s37, 0xffff\n"
    "s_mov_b32 s38, 0x480000\n"
    "s_mov_b32 s39, 0x20000\n"
    "s_add_u32 s64, s71, 0\n"
    "s_mul_i32 s64, s64, 9216\n"
    "s_add_u32 s64, s64, s67\n"
    "s_mul_i32 s65, s71, 9216\n"
    "s_add_u32 m0, s65, 0\n"
    "s_nop 0\n"
    "buffer_load_dwordx4 v201, s[36:39], s64 offen lds\n"
    "s_add_u32 s64, s64, 128\n"
    "s_add_u32 m0, m0, 1024\n"
    "s_nop 0\n"
    "buffer_load_dwordx4 v201, s[36:39], s64 offen lds\n"
    "s_add_u32 s64, s64, 128\n"
    "s_add_u32 m0, m0, 1024\n"
    "s_nop 0\n"
    "buffer_load_dwordx4 v201, s[36:39], s64 offen lds\n"
    "s_add_u32 s64, s64, 128\n"
    "s_add_u32 m0, m0, 1024\n"
    "s_nop 0\n"
    "buffer_load_dwordx4 v201, s[36:39], s64 offen lds\n"
    "s_add_u32 s64, s64, 128\n"
    "s_add_u32 m0, m0, 1024\n"
    "s_nop 0\n"
    "buffer_load_dwordx4 v201, s[36:39], s64 offen lds\n"
    "s_add_u32 s64, s64, 128\n"
    "s_add_u32 m0, m0, 1024\n"
    "s_nop 0\n"
    "buffer_load_dwordx4 v201, s[36:39], s64 offen lds\n"
    "s_add_u32 s64, s64, 128\n"
    "s_add_u32 m0, m0, 1024\n"
    "s_nop 0\n"
    "buffer_load_dwordx4 v201, s[36:39], s64 offen lds\n"
    "s_add_u32 s64, s64, 128\n"
    "s_add_u32 m0, m0, 1024\n"
    "s_nop 0\n"
    "buffer_load_dwordx4 v201, s[36:39], s64 offen lds\n"
    "s_add_u32 s64, s64, 128\n"
    "s_add_u32 m0, m0, 1024\n"
    "s_nop 0\n"
    "buffer_load_dwordx4 v201, s[36:39], s64 offen lds\n"
    "s_add_u32 s64, s64, 35840\n"
    "s_add_u32 m0, m0, 28672\n"
    "s_nop 0\n"
    "buffer_load_dwordx4 v201, s[36:39], s64 offen lds\n"
    "s_add_u32 s64, s64, 128\n"
    "s_add_u32 m0, m0, 1024\n"
    "s_nop 0\n"
    "buffer_load_dwordx4 v201, s[36:39], s64 offen lds\n"
    "s_add_u32 s64, s64, 128\n"
    "s_add_u32 m0, m0, 1024\n"
    "s_nop 0\n"
    "buffer_load_dwordx4 v201, s[36:39], s64 offen lds\n"
    "s_add_u32 s64, s64, 128\n"
    "s_add_u32 m0, m0, 1024\n"
    "s_nop 0\n"
    "buffer_load_dwordx4 v201, s[36:39], s64 offen lds\n"
    "s_add_u32 s64, s64, 128\n"
    "s_add_u32 m0, m0, 1024\n"
    "s_nop 0\n"
    "buffer_load_dwordx4 v201, s[36:39], s64 offen lds\n"
    "s_add_u32 s64, s64, 128\n"
    "s_add_u32 m0, m0, 1024\n"
    "s_nop 0\n"
    "buffer_load_dwordx4 v201, s[36:39], s64 offen lds\n"
    "s_add_u32 s64, s64, 128\n"
    "s_add_u32 m0, m0, 1024\n"
    "s_nop 0\n"
    "buffer_load_dwordx4 v201, s[36:39], s64 offen lds\n"
    "s_add_u32 s64, s64, 128\n"
    "s_add_u32 m0, m0, 1024\n"
    "s_nop 0\n"
    "buffer_load_dwordx4 v201, s[36:39], s64 offen lds\n"
    "s_add_u32 s64, s64, 128\n"
    "s_add_u32 m0, m0, 1024\n"
    "s_nop 0\n"
    "buffer_load_dwordx4 v201, s[36:39], s64 offen lds\n"
    "s_add_u32 s64, s64, 35840\n"
    "s_add_u32 m0, m0, 28672\n"
    "s_add_u32 s36, s50, 0\n"
    "s_addc_u32 s37, s51, 0\n"
    "s_and_b32 s37, s37, 0xffff\n"
    "s_mov_b32 s38, 0x480000\n"
    "s_mov_b32 s39, 0x20000\n"
    "s_add_u32 s64, s71, 8\n"
    "s_mul_i32 s64, s64, 9216\n"
    "s_add_u32 s64, s64, s67\n"
    "s_mul_i32 s65, s71, 9216\n"
    "s_add_u32 m0, s65, 73728\n"
    "s_nop 0\n"
    "buffer_load_dwordx4 v201, s[36:39], s64 offen lds\n"
    "s_add_u32 s64, s64, 128\n"
    "s_add_u32 m0, m0, 1024\n"
    "s_nop 0\n"
    "buffer_load_dwordx4 v201, s[36:39], s64 offen lds\n"
    "s_add_u32 s64, s64, 128\n"
    "s_add_u32 m0, m0, 1024\n"
    "s_nop 0\n"
    "buffer_load_dwordx4 v201, s[36:39], s64 offen lds\n"
    "s_add_u32 s64, s64, 128\n"
    "s_add_u32 m0, m0, 1024\n"
    "s_nop 0\n"
    "buffer_load_dwordx4 v201, s[36:39], s64 offen lds\n"
    "s_add_u32 s64, s64, 128\n"
    "s_add_u32 m0, m0, 1024\n"
    "s_nop 0\n"
    "buffer_load_dwordx4 v201, s[36:39], s64 offen lds\n"
    "s_add_u32 s64, s64, 128\n"
    "s_add_u32 m0, m0, 1024\n"
    "s_nop 0\n"
    "buffer_load_dwordx4 v201, s[36:39], s64 offen lds\n"
    "s_add_u32 s64, s64, 128\n"
    "s_add_u32 m0, m0, 1024\n"
    "s_nop 0\n"
    "buffer_load_dwordx4 v201, s[36:39], s64 offen lds\n"
    "s_add_u32 s64, s64, 128\n"
    "s_add_u32 m0, m0, 1024\n"
    "s_nop 0\n"
    "buffer_load_dwordx4 v201, s[36:39], s64 offen lds\n"
    "s_add_u32 s64, s64, 128\n"
    "s_add_u32 m0, m0, 1024\n"
    "s_nop 0\n"
    "buffer_load_dwordx4 v201, s[36:39], s64 offen lds\n"
    "s_add_u32 s64, s64, 35840\n"
    "s_add_u32 m0, m0, 28672\n"
    "s_nop 0\n"
    "buffer_load_dwordx4 v201, s[36:39], s64 offen lds\n"
    "s_add_u32 s64, s64, 128\n"
    "s_add_u32 m0, m0, 1024\n"
    "s_nop 0\n"
    "buffer_load_dwordx4 v201, s[36:39], s64 offen lds\n"
    "s_add_u32 s64, s64, 128\n"
    "s_add_u32 m0, m0, 1024\n"
    "s_nop 0\n"
    "buffer_load_dwordx4 v201, s[36:39], s64 offen lds\n"
    "s_add_u32 s64, s64, 128\n"
    "s_add_u32 m0, m0, 1024\n"
    "s_nop 0\n"
    "buffer_load_dwordx4 v201, s[36:39], s64 offen lds\n"
    "s_add_u32 s64, s64, 128\n"
    "s_add_u32 m0, m0, 1024\n"
    "s_nop 0\n"
    "buffer_load_dwordx4 v201, s[36:39], s64 offen lds\n"
    "s_add_u32 s64, s64, 128\n"
    "s_add_u32 m0, m0, 1024\n"
    "s_nop 0\n"
    "buffer_load_dwordx4 v201, s[36:39], s64 offen lds\n"
    "s_add_u32 s64, s64, 128\n"
    "s_add_u32 m0, m0, 1024\n"
    "s_nop 0\n"
    "buffer_load_dwordx4 v201, s[36:39], s64 offen lds\n"
    "s_add_u32 s64, s64, 128\n"
    "s_add_u32 m0, m0, 1024\n"
    "s_nop 0\n"
    "buffer_load_dwordx4 v201, s[36:39], s64 offen lds\n"
    "s_add_u32 s64, s64, 128\n"
    "s_add_u32 m0, m0, 1024\n"
    "s_nop 0\n"
    "buffer_load_dwordx4 v201, s[36:39], s64 offen lds\n"
    "s_add_u32 s64, s64, 35840\n"
    "s_add_u32 m0, m0, 28672\n"
    "s_add_u32 s24, s60, 0x540000\n"
    "s_addc_u32 s25, s61, 0\n"
    "s_and_b32 s25, s25, 0xffff\n"
    "s_mov_b32 s26, 0x3000000\n"
    "s_mov_b32 s27, 0x20000\n"
    "s_add_u32 s28, s60, 0x3540000\n"
    "s_addc_u32 s29, s61, 0\n"
    "s_and_b32 s29, s29, 0xffff\n"
    "s_mov_b32 s30, 0x1000000\n"
    "s_mov_b32 s31, 0x20000\n"
    "s_add_u32 s84, s60, 0x4540000\n"
    "s_addc_u32 s85, s61, 0\n"
    "s_and_b32 s85, s85, 0xffff\n"
    "s_mov_b32 s86, 0x4800000\n"
    "s_mov_b32 s87, 0x20000\n"
    "v_bfe_u32 v209, v200, 3, 1\n"
    "v_bfe_u32 v210, v200, 4, 1\n"
    "v_and_b32 v214, 3, v200\n"
    "v_lshlrev_b32 v214, 3, v214\n"
    "v_bfe_u32 v208, v200, 2, 2\n"
    "v_lshl_add_u32 v214, v208, 7, v214\n"
    "s_mov_b32 s64, 9216\n"
    "v_lshrrev_b32 v208, 5, v200\n"
    "v_xor_b32 v209, v209, v208\n"
    "v_lshlrev_b32 v209, 5, v209\n"
    "v_xor_b32 v213, v210, v208\n"
    "v_lshl_or_b32 v212, v213, 6, v209\n"
    "v_mov_b32 v213, v212\n"
    "v_mad_u32_u24 v214, v208, s64, v214\n"
    "v_lshl_add_u32 v214, v210, 9, v214\n"
    "v_add_u32 v215, 18432, v214\n"
    "s_waitcnt vmcnt(18)\n"
    "s_waitcnt lgkmcnt(0)\n"
    "s_barrier\n"
    "s_mul_i32 s66, s74, 18\n"
    "s_and_b32 s65, s66, 3\n"
    "s_lshl_b32 s65, s65, 5\n"
    "v_xor_b32 v216, s65, v212\n"
    "s_lshr_b32 s65, s66, 2\n"
    "s_lshl_b32 s65, s65, 10\n"
    "s_add_u32 s65, s65, 0\n"
    "v_add3_u32 v216, v216, v214, s65\n"
    "s_and_b32 s65, s66, 3\n"
    "s_lshl_b32 s65, s65, 5\n"
    "v_xor_b32 v217, s65, v213\n"
    "s_lshr_b32 s65, s66, 2\n"
    "s_lshl_b32 s65, s65, 10\n"
    "s_add_u32 s65, s65, 0\n"
    "v_add3_u32 v217, v217, v215, s65\n"
    "ds_read_b64_tr_b16 a[0:1], v216\n"
    "ds_read_b64_tr_b16 a[2:3], v217\n"
    "ds_read_b64_tr_b16 a[4:5], v216 offset:36864\n"
    "ds_read_b64_tr_b16 a[6:7], v217 offset:36864\n"
    "s_add_u32 s66, s66, 1\n"
    "s_and_b32 s65, s66, 3\n"
    "s_lshl_b32 s65, s65, 5\n"
    "v_xor_b32 v216, s65, v212\n"
    "s_lshr_b32 s65, s66, 2\n"
    "s_lshl_b32 s65, s65, 10\n"
    "s_add_u32 s65, s65, 0\n"
    "v_add3_u32 v216, v216, v214, s65\n"
    "s_and_b32 s65, s66, 3\n"
    "s_lshl_b32 s65, s65, 5\n"
    "v_xor_b32 v217, s65, v213\n"
    "s_lshr_b32 s65, s66, 2\n"
    "s_lshl_b32 s65, s65, 10\n"
    "s_add_u32 s65, s65, 0\n"
    "v_add3_u32 v217, v217, v215, s65\n"
    "ds_read_b64_tr_b16 a[16:17], v216\n"
    "ds_read_b64_tr_b16 a[18:19], v217\n"
    "ds_read_b64_tr_b16 a[20:21], v216 offset:36864\n"
    "ds_read_b64_tr_b16 a[22:23], v217 offset:36864\n"
    "s_add_u32 s66, s66, 1\n"
    "s_and_b32 s65, s66, 3\n"
    "s_lshl_b32 s65, s65, 5\n"
    "v_xor_b32 v216, s65, v212\n"
    "s_lshr_b32 s65, s66, 2\n"
    "s_lshl_b32 s65, s65, 10\n"
    "s_add_u32 s65, s65, 0\n"
    "v_add3_u32 v216, v216, v214, s65\n"
    "s_and_b32 s65, s66, 3\n"
    "s_lshl_b32 s65, s65, 5\n"
    "v_xor_b32 v217, s65, v213\n"
    "s_lshr_b32 s65, s66, 2\n"
    "s_lshl_b32 s65, s65, 10\n"
    "s_add_u32 s65, s65, 0\n"
    "v_add3_u32 v217, v217, v215, s65\n"
    "ds_read_b64_tr_b16 a[32:33], v216\n"
    "ds_read_b64_tr_b16 a[34:35], v217\n"
    "ds_read_b64_tr_b16 a[36:37], v216 offset:36864\n"
    "ds_read_b64_tr_b16 a[38:39], v217 offset:36864\n"
    "s_add_u32 s66, s66, 1\n"
    "s_and_b32 s65, s66, 3\n"
    "s_lshl_b32 s65, s65, 5\n"
    "v_xor_b32 v216, s65, v212\n"
    "s_lshr_b32 s65, s66, 2\n"
    "s_lshl_b32 s65, s65, 10\n"
    "s_add_u32 s65, s65, 0\n"
    "v_add3_u32 v216, v216, v214, s65\n"
    "s_and_b32 s65, s66, 3\n"
    "s_lshl_b32 s65, s65, 5\n"
    "v_xor_b32 v217, s65, v213\n"
    "s_lshr_b32 s65, s66, 2\n"
    "s_lshl_b32 s65, s65, 10\n"
    "s_add_u32 s65, s65, 0\n"
    "v_add3_u32 v217, v217, v215, s65\n"
    "ds_read_b64_tr_b16 a[48:49], v216\n"
    "ds_read_b64_tr_b16 a[50:51], v217\n"
    "ds_read_b64_tr_b16 a[52:53], v216 offset:36864\n"
    "ds_read_b64_tr_b16 a[54:55], v217 offset:36864\n"
    "s_add_u32 s66, s66, 1\n"
    "s_and_b32 s65, s66, 3\n"
    "s_lshl_b32 s65, s65, 5\n"
    "v_xor_b32 v216, s65, v212\n"
    "s_lshr_b32 s65, s66, 2\n"
    "s_lshl_b32 s65, s65, 10\n"
    "s_add_u32 s65, s65, 0\n"
    "v_add3_u32 v216, v216, v214, s65\n"
    "s_and_b32 s65, s66, 3\n"
    "s_lshl_b32 s65, s65, 5\n"
    "v_xor_b32 v217, s65, v213\n"
    "s_lshr_b32 s65, s66, 2\n"
    "s_lshl_b32 s65, s65, 10\n"
    "s_add_u32 s65, s65, 0\n"
    "v_add3_u32 v217, v217, v215, s65\n"
    "ds_read_b64_tr_b16 a[64:65], v216\n"
    "ds_read_b64_tr_b16 a[66:67], v217\n"
    "ds_read_b64_tr_b16 a[68:69], v216 offset:36864\n"
    "ds_read_b64_tr_b16 a[70:71], v217 offset:36864\n"
    "s_add_u32 s66, s66, 1\n"
    "s_and_b32 s65, s66, 3\n"
    "s_lshl_b32 s65, s65, 5\n"
    "v_xor_b32 v216, s65, v212\n"
    "s_lshr_b32 s65, s66, 2\n"
    "s_lshl_b32 s65, s65, 10\n"
    "s_add_u32 s65, s65, 0\n"
    "v_add3_u32 v216, v216, v214, s65\n"
    "s_and_b32 s65, s66, 3\n"
    "s_lshl_b32 s65, s65, 5\n"
    "v_xor_b32 v217, s65, v213\n"
    "s_lshr_b32 s65, s66, 2\n"
    "s_lshl_b32 s65, s65, 10\n"
    "s_add_u32 s65, s65, 0\n"
    "v_add3_u32 v217, v217, v215, s65\n"
    "ds_read_b64_tr_b16 a[80:81], v216\n"
    "ds_read_b64_tr_b16 a[82:83], v217\n"
    "ds_read_b64_tr_b16 a[84:85], v216 offset:36864\n"
    "ds_read_b64_tr_b16 a[86:87], v217 offset:36864\n"
    "s_add_u32 s66, s66, 1\n"
    "s_and_b32 s65, s66, 3\n"
    "s_lshl_b32 s65, s65, 5\n"
    "v_xor_b32 v216, s65, v212\n"
    "s_lshr_b32 s65, s66, 2\n"
    "s_lshl_b32 s65, s65, 10\n"
    "s_add_u32 s65, s65, 0\n"
    "v_add3_u32 v216, v216, v214, s65\n"
    "s_and_b32 s65, s66, 3\n"
    "s_lshl_b32 s65, s65, 5\n"
    "v_xor_b32 v217, s65, v213\n"
    "s_lshr_b32 s65, s66, 2\n"
    "s_lshl_b32 s65, s65, 10\n"
    "s_add_u32 s65, s65, 0\n"
    "v_add3_u32 v217, v217, v215, s65\n"
    "ds_read_b64_tr_b16 a[96:97], v216\n"
    "ds_read_b64_tr_b16 a[98:99], v217\n"
    "ds_read_b64_tr_b16 a[100:101], v216 offset:36864\n"
    "ds_read_b64_tr_b16 a[102:103], v217 offset:36864\n"
    "s_add_u32 s66, s66, 1\n"
    "s_and_b32 s65, s66, 3\n"
    "s_lshl_b32 s65, s65, 5\n"
    "v_xor_b32 v216, s65, v212\n"
    "s_lshr_b32 s65, s66, 2\n"
    "s_lshl_b32 s65, s65, 10\n"
    "s_add_u32 s65, s65, 0\n"
    "v_add3_u32 v216, v216, v214, s65\n"
    "s_and_b32 s65, s66, 3\n"
    "s_lshl_b32 s65, s65, 5\n"
    "v_xor_b32 v217, s65, v213\n"
    "s_lshr_b32 s65, s66, 2\n"
    "s_lshl_b32 s65, s65, 10\n"
    "s_add_u32 s65, s65, 0\n"
    "v_add3_u32 v217, v217, v215, s65\n"
    "ds_read_b64_tr_b16 a[112:113], v216\n"
    "ds_read_b64_tr_b16 a[114:115], v217\n"
    "ds_read_b64_tr_b16 a[116:117], v216 offset:36864\n"
    "ds_read_b64_tr_b16 a[118:119], v217 offset:36864\n"
    "s_add_u32 s66, s66, 1\n"
    "s_and_b32 s65, s66, 3\n"
    "s_lshl_b32 s65, s65, 5\n"
    "v_xor_b32 v216, s65, v212\n"
    "s_lshr_b32 s65, s66, 2\n"
    "s_lshl_b32 s65, s65, 10\n"
    "s_add_u32 s65, s65, 0\n"
    "v_add3_u32 v216, v216, v214, s65\n"
    "s_and_b32 s65, s66, 3\n"
    "s_lshl_b32 s65, s65, 5\n"
    "v_xor_b32 v217, s65, v213\n"
    "s_lshr_b32 s65, s66, 2\n"
    "s_lshl_b32 s65, s65, 10\n"
    "s_add_u32 s65, s65, 0\n"
    "v_add3_u32 v217, v217, v215, s65\n"
    "ds_read_b64_tr_b16 a[128:129], v216\n"
    "ds_read_b64_tr_b16 a[130:131], v217\n"
    "ds_read_b64_tr_b16 a[132:133], v216 offset:36864\n"
    "ds_read_b64_tr_b16 a[134:135], v217 offset:36864\n"
    "s_add_u32 s66, s66, 1\n"
    "s_and_b32 s65, s66, 3\n"
    "s_lshl_b32 s65, s65, 5\n"
    "v_xor_b32 v216, s65, v212\n"
    "s_lshr_b32 s65, s66, 2\n"
    "s_lshl_b32 s65, s65, 10\n"
    "s_add_u32 s65, s65, 0\n"
    "v_add3_u32 v216, v216, v214, s65\n"
    "s_and_b32 s65, s66, 3\n"
    "s_lshl_b32 s65, s65, 5\n"
    "v_xor_b32 v217, s65, v213\n"
    "s_lshr_b32 s65, s66, 2\n"
    "s_lshl_b32 s65, s65, 10\n"
    "s_add_u32 s65, s65, 0\n"
    "v_add3_u32 v217, v217, v215, s65\n"
    "ds_read_b64_tr_b16 a[144:145], v216\n"
    "ds_read_b64_tr_b16 a[146:147], v217\n"
    "ds_read_b64_tr_b16 a[148:149], v216 offset:36864\n"
    "ds_read_b64_tr_b16 a[150:151], v217 offset:36864\n"
    "s_add_u32 s66, s66, 1\n"
    "s_and_b32 s65, s66, 3\n"
    "s_lshl_b32 s65, s65, 5\n"
    "v_xor_b32 v216, s65, v212\n"
    "s_lshr_b32 s65, s66, 2\n"
    "s_lshl_b32 s65, s65, 10\n"
    "s_add_u32 s65, s65, 0\n"
    "v_add3_u32 v216, v216, v214, s65\n"
    "s_and_b32 s65, s66, 3\n"
    "s_lshl_b32 s65, s65, 5\n"
    "v_xor_b32 v217, s65, v213\n"
    "s_lshr_b32 s65, s66, 2\n"
    "s_lshl_b32 s65, s65, 10\n"
    "s_add_u32 s65, s65, 0\n"
    "v_add3_u32 v217, v217, v215, s65\n"
    "ds_read_b64_tr_b16 a[160:161], v216\n"
    "ds_read_b64_tr_b16 a[162:163], v217\n"
    "ds_read_b64_tr_b16 a[164:165], v216 offset:36864\n"
    "ds_read_b64_tr_b16 a[166:167], v217 offset:36864\n"
    "s_add_u32 s66, s66, 1\n"
    "s_and_b32 s65, s66, 3\n"
    "s_lshl_b32 s65, s65, 5\n"
    "v_xor_b32 v216, s65, v212\n"
    "s_lshr_b32 s65, s66, 2\n"
    "s_lshl_b32 s65, s65, 10\n"
    "s_add_u32 s65, s65, 0\n"
    "v_add3_u32 v216, v216, v214, s65\n"
    "s_and_b32 s65, s66, 3\n"
    "s_lshl_b32 s65, s65, 5\n"
    "v_xor_b32 v217, s65, v213\n"
    "s_lshr_b32 s65, s66, 2\n"
    "s_lshl_b32 s65, s65, 10\n"
    "s_add_u32 s65, s65, 0\n"
    "v_add3_u32 v217, v217, v215, s65\n"
    "ds_read_b64_tr_b16 a[176:177], v216\n"
    "ds_read_b64_tr_b16 a[178:179], v217\n"
    "ds_read_b64_tr_b16 a[180:181], v216 offset:36864\n"
    "ds_read_b64_tr_b16 a[182:183], v217 offset:36864\n"
    "s_add_u32 s66, s66, 1\n"
    "s_and_b32 s65, s66, 3\n"
    "s_lshl_b32 s65, s65, 5\n"
    "v_xor_b32 v216, s65, v212\n"
    "s_lshr_b32 s65, s66, 2\n"
    "s_lshl_b32 s65, s65, 10\n"
    "s_add_u32 s65, s65, 0\n"
    "v_add3_u32 v216, v216, v214, s65\n"
    "s_and_b32 s65, s66, 3\n"
    "s_lshl_b32 s65, s65, 5\n"
    "v_xor_b32 v217, s65, v213\n"
    "s_lshr_b32 s65, s66, 2\n"
    "s_lshl_b32 s65, s65, 10\n"
    "s_add_u32 s65, s65, 0\n"
    "v_add3_u32 v217, v217, v215, s65\n"
    "ds_read_b64_tr_b16 a[192:193], v216\n"
    "ds_read_b64_tr_b16 a[194:195], v217\n"
    "ds_read_b64_tr_b16 a[196:197], v216 offset:36864\n"
    "ds_read_b64_tr_b16 a[198:199], v217 offset:36864\n"
    "s_add_u32 s66, s66, 1\n"
    "s_and_b32 s65, s66, 3\n"
    "s_lshl_b32 s65, s65, 5\n"
    "v_xor_b32 v216, s65, v212\n"
    "s_lshr_b32 s65, s66, 2\n"
    "s_lshl_b32 s65, s65, 10\n"
    "s_add_u32 s65, s65, 0\n"
    "v_add3_u32 v216, v216, v214, s65\n"
    "s_and_b32 s65, s66, 3\n"
    "s_lshl_b32 s65, s65, 5\n"
    "v_xor_b32 v217, s65, v213\n"
    "s_lshr_b32 s65, s66, 2\n"
    "s_lshl_b32 s65, s65, 10\n"
    "s_add_u32 s65, s65, 0\n"
    "v_add3_u32 v217, v217, v215, s65\n"
    "ds_read_b64_tr_b16 a[208:209], v216\n"
    "ds_read_b64_tr_b16 a[210:211], v217\n"
    "ds_read_b64_tr_b16 a[212:213], v216 offset:36864\n"
    "ds_read_b64_tr_b16 a[214:215], v217 offset:36864\n"
    "s_add_u32 s66, s66, 1\n"
    "s_and_b32 s65, s66, 3\n"
    "s_lshl_b32 s65, s65, 5\n"
    "v_xor_b32 v216, s65, v212\n"
    "s_lshr_b32 s65, s66, 2\n"
    "s_lshl_b32 s65, s65, 10\n"
    "s_add_u32 s65, s65, 0\n"
    "v_add3_u32 v216, v216, v214, s65\n"
    "s_and_b32 s65, s66, 3\n"
    "s_lshl_b32 s65, s65, 5\n"
    "v_xor_b32 v217, s65, v213\n"
    "s_lshr_b32 s65, s66, 2\n"
    "s_lshl_b32 s65, s65, 10\n"
    "s_add_u32 s65, s65, 0\n"
    "v_add3_u32 v217, v217, v215, s65\n"
    "ds_read_b64_tr_b16 a[224:225], v216\n"
    "ds_read_b64_tr_b16 a[226:227], v217\n"
    "ds_read_b64_tr_b16 a[228:229], v216 offset:36864\n"
    "ds_read_b64_tr_b16 a[230:231], v217 offset:36864\n"
    "s_add_u32 s66, s66, 1\n"
    "s_and_b32 s65, s66, 3\n"
    "s_lshl_b32 s65, s65, 5\n"
    "v_xor_b32 v216, s65, v212\n"
    "s_lshr_b32 s65, s66, 2\n"
    "s_lshl_b32 s65, s65, 10\n"
    "s_add_u32 s65, s65, 0\n"
    "v_add3_u32 v216, v216, v214, s65\n"
    "s_and_b32 s65, s66, 3\n"
    "s_lshl_b32 s65, s65, 5\n"
    "v_xor_b32 v217, s65, v213\n"
    "s_lshr_b32 s65, s66, 2\n"
    "s_lshl_b32 s65, s65, 10\n"
    "s_add_u32 s65, s65, 0\n"
    "v_add3_u32 v217, v217, v215, s65\n"
    "ds_read_b64_tr_b16 a[240:241], v216\n"
    "ds_read_b64_tr_b16 a[242:243], v217\n"
    "ds_read_b64_tr_b16 a[244:245], v216 offset:36864\n"
    "ds_read_b64_tr_b16 a[246:247], v217 offset:36864\n"
    "s_add_u32 s66, s66, 1\n"
    "s_and_b32 s65, s66, 3\n"
    "s_lshl_b32 s65, s65, 5\n"
    "v_xor_b32 v216, s65, v212\n"
    "s_lshr_b32 s65, s66, 2\n"
    "s_lshl_b32 s65, s65, 10\n"
    "s_add_u32 s65, s65, 0\n"
    "v_add3_u32 v216, v216, v214, s65\n"
    "s_and_b32 s65, s66, 3\n"
    "s_lshl_b32 s65, s65, 5\n"
    "v_xor_b32 v217, s65, v213\n"
    "s_lshr_b32 s65, s66, 2\n"
    "s_lshl_b32 s65, s65, 10\n"
    "s_add_u32 s65, s65, 0\n"
    "v_add3_u32 v217, v217, v215, s65\n"
    "ds_read_b64_tr_b16 v[224:225], v216\n"
    "ds_read_b64_tr_b16 v[226:227], v217\n"
    "ds_read_b64_tr_b16 v[228:229], v216 offset:36864\n"
    "ds_read_b64_tr_b16 v[230:231], v217 offset:36864\n"
    "s_add_u32 s66, s66, 1\n"
    "s_and_b32 s65, s66, 3\n"
    "s_lshl_b32 s65, s65, 5\n"
    "v_xor_b32 v216, s65, v212\n"
    "s_lshr_b32 s65, s66, 2\n"
    "s_lshl_b32 s65, s65, 10\n"
    "s_add_u32 s65, s65, 0\n"
    "v_add3_u32 v216, v216, v214, s65\n"
    "s_and_b32 s65, s66, 3\n"
    "s_lshl_b32 s65, s65, 5\n"
    "v_xor_b32 v217, s65, v213\n"
    "s_lshr_b32 s65, s66, 2\n"
    "s_lshl_b32 s65, s65, 10\n"
    "s_add_u32 s65, s65, 0\n"
    "v_add3_u32 v217, v217, v215, s65\n"
    "ds_read_b64_tr_b16 v[240:241], v216\n"
    "ds_read_b64_tr_b16 v[242:243], v217\n"
    "ds_read_b64_tr_b16 v[244:245], v216 offset:36864\n"
    "ds_read_b64_tr_b16 v[246:247], v217 offset:36864\n"
    "s_add_u32 s66, s66, 1\n"
    "s_waitcnt vmcnt(0)\n"
    "s_waitcnt lgkmcnt(0)\n"
    "s_barrier\n"
    "s_mul_i32 s66, s74, 18\n"
    "s_and_b32 s65, s66, 3\n"
    "s_lshl_b32 s65, s65, 5\n"
    "v_xor_b32 v216, s65, v212\n"
    "s_lshr_b32 s65, s66, 2\n"
    "s_lshl_b32 s65, s65, 10\n"
    "s_add_u32 s65, s65, 73728\n"
    "v_add3_u32 v216, v216, v214, s65\n"
    "s_and_b32 s65, s66, 3\n"
    "s_lshl_b32 s65, s65, 5\n"
    "v_xor_b32 v217, s65, v213\n"
    "s_lshr_b32 s65, s66, 2\n"
    "s_lshl_b32 s65, s65, 10\n"
    "s_add_u32 s65, s65, 73728\n"
    "v_add3_u32 v217, v217, v215, s65\n"
    "ds_read_b64_tr_b16 a[8:9], v216\n"
    "ds_read_b64_tr_b16 a[10:11], v217\n"
    "ds_read_b64_tr_b16 a[12:13], v216 offset:36864\n"
    "ds_read_b64_tr_b16 a[14:15], v217 offset:36864\n"
    "s_add_u32 s66, s66, 1\n"
    "s_and_b32 s65, s66, 3\n"
    "s_lshl_b32 s65, s65, 5\n"
    "v_xor_b32 v216, s65, v212\n"
    "s_lshr_b32 s65, s66, 2\n"
    "s_lshl_b32 s65, s65, 10\n"
    "s_add_u32 s65, s65, 73728\n"
    "v_add3_u32 v216, v216, v214, s65\n"
    "s_and_b32 s65, s66, 3\n"
    "s_lshl_b32 s65, s65, 5\n"
    "v_xor_b32 v217, s65, v213\n"
    "s_lshr_b32 s65, s66, 2\n"
    "s_lshl_b32 s65, s65, 10\n"
    "s_add_u32 s65, s65, 73728\n"
    "v_add3_u32 v217, v217, v215, s65\n"
    "ds_read_b64_tr_b16 a[24:25], v216\n"
    "ds_read_b64_tr_b16 a[26:27], v217\n"
    "ds_read_b64_tr_b16 a[28:29], v216 offset:36864\n"
    "ds_read_b64_tr_b16 a[30:31], v217 offset:36864\n"
    "s_add_u32 s66, s66, 1\n"
    "s_and_b32 s65, s66, 3\n"
    "s_lshl_b32 s65, s65, 5\n"
    "v_xor_b32 v216, s65, v212\n"
    "s_lshr_b32 s65, s66, 2\n"
    "s_lshl_b32 s65, s65, 10\n"
    "s_add_u32 s65, s65, 73728\n"
    "v_add3_u32 v216, v216, v214, s65\n"
    "s_and_b32 s65, s66, 3\n"
    "s_lshl_b32 s65, s65, 5\n"
    "v_xor_b32 v217, s65, v213\n"
    "s_lshr_b32 s65, s66, 2\n"
    "s_lshl_b32 s65, s65, 10\n"
    "s_add_u32 s65, s65, 73728\n"
    "v_add3_u32 v217, v217, v215, s65\n"
    "ds_read_b64_tr_b16 a[40:41], v216\n"
    "ds_read_b64_tr_b16 a[42:43], v217\n"
    "ds_read_b64_tr_b16 a[44:45], v216 offset:36864\n"
    "ds_read_b64_tr_b16 a[46:47], v217 offset:36864\n"
    "s_add_u32 s66, s66, 1\n"
    "s_and_b32 s65, s66, 3\n"
    "s_lshl_b32 s65, s65, 5\n"
    "v_xor_b32 v216, s65, v212\n"
    "s_lshr_b32 s65, s66, 2\n"
    "s_lshl_b32 s65, s65, 10\n"
    "s_add_u32 s65, s65, 73728\n"
    "v_add3_u32 v216, v216, v214, s65\n"
    "s_and_b32 s65, s66, 3\n"
    "s_lshl_b32 s65, s65, 5\n"
    "v_xor_b32 v217, s65, v213\n"
    "s_lshr_b32 s65, s66, 2\n"
    "s_lshl_b32 s65, s65, 10\n"
    "s_add_u32 s65, s65, 73728\n"
    "v_add3_u32 v217, v217, v215, s65\n"
    "ds_read_b64_tr_b16 a[56:57], v216\n"
    "ds_read_b64_tr_b16 a[58:59], v217\n"
    "ds_read_b64_tr_b16 a[60:61], v216 offset:36864\n"
    "ds_read_b64_tr_b16 a[62:63], v217 offset:36864\n"
    "s_add_u32 s66, s66, 1\n"
    "s_and_b32 s65, s66, 3\n"
    "s_lshl_b32 s65, s65, 5\n"
    "v_xor_b32 v216, s65, v212\n"
    "s_lshr_b32 s65, s66, 2\n"
    "s_lshl_b32 s65, s65, 10\n"
    "s_add_u32 s65, s65, 73728\n"
    "v_add3_u32 v216, v216, v214, s65\n"
    "s_and_b32 s65, s66, 3\n"
    "s_lshl_b32 s65, s65, 5\n"
    "v_xor_b32 v217, s65, v213\n"
    "s_lshr_b32 s65, s66, 2\n"
    "s_lshl_b32 s65, s65, 10\n"
    "s_add_u32 s65, s65, 73728\n"
    "v_add3_u32 v217, v217, v215, s65\n"
    "ds_read_b64_tr_b16 a[72:73], v216\n"
    "ds_read_b64_tr_b16 a[74:75], v217\n"
    "ds_read_b64_tr_b16 a[76:77], v216 offset:36864\n"
    "ds_read_b64_tr_b16 a[78:79], v217 offset:36864\n"
    "s_add_u32 s66, s66, 1\n"
    "s_and_b32 s65, s66, 3\n"
    "s_lshl_b32 s65, s65, 5\n"
    "v_xor_b32 v216, s65, v212\n"
    "s_lshr_b32 s65, s66, 2\n"
    "s_lshl_b32 s65, s65, 10\n"
    "s_add_u32 s65, s65, 73728\n"
    "v_add3_u32 v216, v216, v214, s65\n"
    "s_and_b32 s65, s66, 3\n"
    "s_lshl_b32 s65, s65, 5\n"
    "v_xor_b32 v217, s65, v213\n"
    "s_lshr_b32 s65, s66, 2\n"
    "s_lshl_b32 s65, s65, 10\n"
    "s_add_u32 s65, s65, 73728\n"
    "v_add3_u32 v217, v217, v215, s65\n"
    "ds_read_b64_tr_b16 a[88:89], v216\n"
    "ds_read_b64_tr_b16 a[90:91], v217\n"
    "ds_read_b64_tr_b16 a[92:93], v216 offset:36864\n"
    "ds_read_b64_tr_b16 a[94:95], v217 offset:36864\n"
    "s_add_u32 s66, s66, 1\n"
    "s_and_b32 s65, s66, 3\n"
    "s_lshl_b32 s65, s65, 5\n"
    "v_xor_b32 v216, s65, v212\n"
    "s_lshr_b32 s65, s66, 2\n"
    "s_lshl_b32 s65, s65, 10\n"
    "s_add_u32 s65, s65, 73728\n"
    "v_add3_u32 v216, v216, v214, s65\n"
    "s_and_b32 s65, s66, 3\n"
    "s_lshl_b32 s65, s65, 5\n"
    "v_xor_b32 v217, s65, v213\n"
    "s_lshr_b32 s65, s66, 2\n"
    "s_lshl_b32 s65, s65, 10\n"
    "s_add_u32 s65, s65, 73728\n"
    "v_add3_u32 v217, v217, v215, s65\n"
    "ds_read_b64_tr_b16 a[104:105], v216\n"
    "ds_read_b64_tr_b16 a[106:107], v217\n"
    "ds_read_b64_tr_b16 a[108:109], v216 offset:36864\n"
    "ds_read_b64_tr_b16 a[110:111], v217 offset:36864\n"
    "s_add_u32 s66, s66, 1\n"
    "s_and_b32 s65, s66, 3\n"
    "s_lshl_b32 s65, s65, 5\n"
    "v_xor_b32 v216, s65, v212\n"
    "s_lshr_b32 s65, s66, 2\n"
    "s_lshl_b32 s65, s65, 10\n"
    "s_add_u32 s65, s65, 73728\n"
    "v_add3_u32 v216, v216, v214, s65\n"
    "s_and_b32 s65, s66, 3\n"
    "s_lshl_b32 s65, s65, 5\n"
    "v_xor_b32 v217, s65, v213\n"
    "s_lshr_b32 s65, s66, 2\n"
    "s_lshl_b32 s65, s65, 10\n"
    "s_add_u32 s65, s65, 73728\n"
    "v_add3_u32 v217, v217, v215, s65\n"
    "ds_read_b64_tr_b16 a[120:121], v216\n"
    "ds_read_b64_tr_b16 a[122:123], v217\n"
    "ds_read_b64_tr_b16 a[124:125], v216 offset:36864\n"
    "ds_read_b64_tr_b16 a[126:127], v217 offset:36864\n"
    "s_add_u32 s66, s66, 1\n"
    "s_and_b32 s65, s66, 3\n"
    "s_lshl_b32 s65, s65, 5\n"
    "v_xor_b32 v216, s65, v212\n"
    "s_lshr_b32 s65, s66, 2\n"
    "s_lshl_b32 s65, s65, 10\n"
    "s_add_u32 s65, s65, 73728\n"
    "v_add3_u32 v216, v216, v214, s65\n"
    "s_and_b32 s65, s66, 3\n"
    "s_lshl_b32 s65, s65, 5\n"
    "v_xor_b32 v217, s65, v213\n"
    "s_lshr_b32 s65, s66, 2\n"
    "s_lshl_b32 s65, s65, 10\n"
    "s_add_u32 s65, s65, 73728\n"
    "v_add3_u32 v217, v217, v215, s65\n"
    "ds_read_b64_tr_b16 a[136:137], v216\n"
    "ds_read_b64_tr_b16 a[138:139], v217\n"
    "ds_read_b64_tr_b16 a[140:141], v216 offset:36864\n"
    "ds_read_b64_tr_b16 a[142:143], v217 offset:36864\n"
    "s_add_u32 s66, s66, 1\n"
    "s_and_b32 s65, s66, 3\n"
    "s_lshl_b32 s65, s65, 5\n"
    "v_xor_b32 v216, s65, v212\n"
    "s_lshr_b32 s65, s66, 2\n"
    "s_lshl_b32 s65, s65, 10\n"
    "s_add_u32 s65, s65, 73728\n"
    "v_add3_u32 v216, v216, v214, s65\n"
    "s_and_b32 s65, s66, 3\n"
    "s_lshl_b32 s65, s65, 5\n"
    "v_xor_b32 v217, s65, v213\n"
    "s_lshr_b32 s65, s66, 2\n"
    "s_lshl_b32 s65, s65, 10\n"
    "s_add_u32 s65, s65, 73728\n"
    "v_add3_u32 v217, v217, v215, s65\n"
    "ds_read_b64_tr_b16 a[152:153], v216\n"
    "ds_read_b64_tr_b16 a[154:155], v217\n"
    "ds_read_b64_tr_b16 a[156:157], v216 offset:36864\n"
    "ds_read_b64_tr_b16 a[158:159], v217 offset:36864\n"
    "s_add_u32 s66, s66, 1\n"
    "s_and_b32 s65, s66, 3\n"
    "s_lshl_b32 s65, s65, 5\n"
    "v_xor_b32 v216, s65, v212\n"
    "s_lshr_b32 s65, s66, 2\n"
    "s_lshl_b32 s65, s65, 10\n"
    "s_add_u32 s65, s65, 73728\n"
    "v_add3_u32 v216, v216, v214, s65\n"
    "s_and_b32 s65, s66, 3\n"
    "s_lshl_b32 s65, s65, 5\n"
    "v_xor_b32 v217, s65, v213\n"
    "s_lshr_b32 s65, s66, 2\n"
    "s_lshl_b32 s65, s65, 10\n"
    "s_add_u32 s65, s65, 73728\n"
    "v_add3_u32 v217, v217, v215, s65\n"
    "ds_read_b64_tr_b16 a[168:169], v216\n"
    "ds_read_b64_tr_b16 a[170:171], v217\n"
    "ds_read_b64_tr_b16 a[172:173], v216 offset:36864\n"
    "ds_read_b64_tr_b16 a[174:175], v217 offset:36864\n"
    "s_add_u32 s66, s66, 1\n"
    "s_and_b32 s65, s66, 3\n"
    "s_lshl_b32 s65, s65, 5\n"
    "v_xor_b32 v216, s65, v212\n"
    "s_lshr_b32 s65, s66, 2\n"
    "s_lshl_b32 s65, s65, 10\n"
    "s_add_u32 s65, s65, 73728\n"
    "v_add3_u32 v216, v216, v214, s65\n"
    "s_and_b32 s65, s66, 3\n"
    "s_lshl_b32 s65, s65, 5\n"
    "v_xor_b32 v217, s65, v213\n"
    "s_lshr_b32 s65, s66, 2\n"
    "s_lshl_b32 s65, s65, 10\n"
    "s_add_u32 s65, s65, 73728\n"
    "v_add3_u32 v217, v217, v215, s65\n"
    "ds_read_b64_tr_b16 a[184:185], v216\n"
    "ds_read_b64_tr_b16 a[186:187], v217\n"
    "ds_read_b64_tr_b16 a[188:189], v216 offset:36864\n"
    "ds_read_b64_tr_b16 a[190:191], v217 offset:36864\n"
    "s_add_u32 s66, s66, 1\n"
    "s_and_b32 s65, s66, 3\n"
    "s_lshl_b32 s65, s65, 5\n"
    "v_xor_b32 v216, s65, v212\n"
    "s_lshr_b32 s65, s66, 2\n"
    "s_lshl_b32 s65, s65, 10\n"
    "s_add_u32 s65, s65, 73728\n"
    "v_add3_u32 v216, v216, v214, s65\n"
    "s_and_b32 s65, s66, 3\n"
    "s_lshl_b32 s65, s65, 5\n"
    "v_xor_b32 v217, s65, v213\n"
    "s_lshr_b32 s65, s66, 2\n"
    "s_lshl_b32 s65, s65, 10\n"
    "s_add_u32 s65, s65, 73728\n"
    "v_add3_u32 v217, v217, v215, s65\n"
    "ds_read_b64_tr_b16 a[200:201], v216\n"
    "ds_read_b64_tr_b16 a[202:203], v217\n"
    "ds_read_b64_tr_b16 a[204:205], v216 offset:36864\n"
    "ds_read_b64_tr_b16 a[206:207], v217 offset:36864\n"
    "s_add_u32 s66, s66, 1\n"
    "s_and_b32 s65, s66, 3\n"
    "s_lshl_b32 s65, s65, 5\n"
    "v_xor_b32 v216, s65, v212\n"
    "s_lshr_b32 s65, s66, 2\n"
    "s_lshl_b32 s65, s65, 10\n"
    "s_add_u32 s65, s65, 73728\n"
    "v_add3_u32 v216, v216, v214, s65\n"
    "s_and_b32 s65, s66, 3\n"
    "s_lshl_b32 s65, s65, 5\n"
    "v_xor_b32 v217, s65, v213\n"
    "s_lshr_b32 s65, s66, 2\n"
    "s_lshl_b32 s65, s65, 10\n"
    "s_add_u32 s65, s65, 73728\n"
    "v_add3_u32 v217, v217, v215, s65\n"
    "ds_read_b64_tr_b16 a[216:217], v216\n"
    "ds_read_b64_tr_b16 a[218:219], v217\n"
    "ds_read_b64_tr_b16 a[220:221], v216 offset:36864\n"
    "ds_read_b64_tr_b16 a[222:223], v217 offset:36864\n"
    "s_add_u32 s66, s66, 1\n"
    "s_and_b32 s65, s66, 3\n"
    "s_lshl_b32 s65, s65, 5\n"
    "v_xor_b32 v216, s65, v212\n"
    "s_lshr_b32 s65, s66, 2\n"
    "s_lshl_b32 s65, s65, 10\n"
    "s_add_u32 s65, s65, 73728\n"
    "v_add3_u32 v216, v216, v214, s65\n"
    "s_and_b32 s65, s66, 3\n"
    "s_lshl_b32 s65, s65, 5\n"
    "v_xor_b32 v217, s65, v213\n"
    "s_lshr_b32 s65, s66, 2\n"
    "s_lshl_b32 s65, s65, 10\n"
    "s_add_u32 s65, s65, 73728\n"
    "v_add3_u32 v217, v217, v215, s65\n"
    "ds_read_b64_tr_b16 a[232:233], v216\n"
    "ds_read_b64_tr_b16 a[234:235], v217\n"
    "ds_read_b64_tr_b16 a[236:237], v216 offset:36864\n"
    "ds_read_b64_tr_b16 a[238:239], v217 offset:36864\n"
    "s_add_u32 s66, s66, 1\n"
    "s_and_b32 s65, s66, 3\n"
    "s_lshl_b32 s65, s65, 5\n"
    "v_xor_b32 v216, s65, v212\n"
    "s_lshr_b32 s65, s66, 2\n"
    "s_lshl_b32 s65, s65, 10\n"
    "s_add_u32 s65, s65, 73728\n"
    "v_add3_u32 v216, v216, v214, s65\n"
    "s_and_b32 s65, s66, 3\n"
    "s_lshl_b32 s65, s65, 5\n"
    "v_xor_b32 v217, s65, v213\n"
    "s_lshr_b32 s65, s66, 2\n"
    "s_lshl_b32 s65, s65, 10\n"
    "s_add_u32 s65, s65, 73728\n"
    "v_add3_u32 v217, v217, v215, s65\n"
    "ds_read_b64_tr_b16 a[248:249], v216\n"
    "ds_read_b64_tr_b16 a[250:251], v217\n"
    "ds_read_b64_tr_b16 a[252:253], v216 offset:36864\n"
    "ds_read_b64_tr_b16 a[254:255], v217 offset:36864\n"
    "s_add_u32 s66, s66, 1\n"
    "s_and_b32 s65, s66, 3\n"
    "s_lshl_b32 s65, s65, 5\n"
    "v_xor_b32 v216, s65, v212\n"
    "s_lshr_b32 s65, s66, 2\n"
    "s_lshl_b32 s65, s65, 10\n"
    "s_add_u32 s65, s65, 73728\n"
    "v_add3_u32 v216, v216, v214, s65\n"
    "s_and_b32 s65, s66, 3\n"
    "s_lshl_b32 s65, s65, 5\n"
    "v_xor_b32 v217, s65, v213\n"
    "s_lshr_b32 s65, s66, 2\n"
    "s_lshl_b32 s65, s65, 10\n"
    "s_add_u32 s65, s65, 73728\n"
    "v_add3_u32 v217, v217, v215, s65\n"
    "ds_read_b64_tr_b16 v[232:233], v216\n"
    "ds_read_b64_tr_b16 v[234:235], v217\n"
    "ds_read_b64_tr_b16 v[236:237], v216 offset:36864\n"
    "ds_read_b64_tr_b16 v[238:239], v217 offset:36864\n"
    "s_add_u32 s66, s66, 1\n"
    "s_and_b32 s65, s66, 3\n"
    "s_lshl_b32 s65, s65, 5\n"
    "v_xor_b32 v216, s65, v212\n"
    "s_lshr_b32 s65, s66, 2\n"
    "s_lshl_b32 s65, s65, 10\n"
    "s_add_u32 s65, s65, 73728\n"
    "v_add3_u32 v216, v216, v214, s65\n"
    "s_and_b32 s65, s66, 3\n"
    "s_lshl_b32 s65, s65, 5\n"
    "v_xor_b32 v217, s65, v213\n"
    "s_lshr_b32 s65, s66, 2\n"
    "s_lshl_b32 s65, s65, 10\n"
    "s_add_u32 s65, s65, 73728\n"
    "v_add3_u32 v217, v217, v215, s65\n"
    "ds_read_b64_tr_b16 v[248:249], v216\n"
    "ds_read_b64_tr_b16 v[250:251], v217\n"
    "ds_read_b64_tr_b16 v[252:253], v216 offset:36864\n"
    "ds_read_b64_tr_b16 v[254:255], v217 offset:36864\n"
    "s_add_u32 s66, s66, 1\n"
    "v_lshlrev_b32 v202, 4, v200\n"
    "s_mov_b32 s80, -1\n"
    "s_mov_b32 s81, 0\n"
    "s_mov_b32 s82, 3\n"
    "s_mov_b32 s83, 0\n"
    "s_mov_b32 s88, 0xffffff\n"
    "s_mov_b32 s89, 0\n"
    "s_lshl_b32 s64, s73, 1\n"
    "s_add_u32 s64, s64, s74\n"
    "s_mul_i32 s98, s64, 9\n"
    "s_lshl_b32 s64, s98, 5\n"
    "s_add_u32 s64, s64, s72\n"
    "s_lshl_b32 s64, s64, 6\n"
    "s_add_u32 s64, s64, 0x20d000\n"
    "v_lshl_add_u32 v203, v200, 11, s64\n"
    "v_cmp_gt_u32 vcc, 9, v200\n"
    "v_mov_b32 v208, 0x7fffffff\n"
    "s_nop 0\n"
    "v_cndmask_b32 v203, v208, v203, vcc\n"
    "v_mov_b32 v208, 0x200000\n"
    "buffer_load_dword v209, v208, s[20:23], 0 offen sc1\n"
    "s_waitcnt vmcnt(0)\n"
    "v_readfirstlane_b32 s65, v209\n"
    "s_and_b32 s65, s65, 1\n"
    "s_lshl_b32 s65, s65, 13\n"
    "s_lshl_b32 s64, s72, 8\n"
    "s_add_u32 s64, s64, s65\n"
    "s_add_u32 s64, s64, 0x1a8000\n"
    "v_lshl_add_u32 v204, v200, 7, s64\n"
    "v_cmp_gt_u32 vcc, 2, v200\n"
    "v_mov_b32 v208, 0x7fffffff\n"
    "s_nop 0\n"
    "v_cndmask_b32 v204, v208, v204, vcc\n"
    "v_lshrrev_b32 v209, 4, v200\n"
    "v_lshlrev_b32 v209, 2, v209\n"
    "v_lshlrev_b32 v207, 3, v200\n"
    "s_lshl_b32 s64, s73, 12\n"
    "v_add_u32 v206, s64, v202\n"
    "v_and_b32 v208, 15, v200\n"
    "v_sub_u32 v208, v208, v209\n"
    "v_cmp_le_i32 s[90:91], 0, v208\n"
    "v_cmp_le_i32 s[92:93], 1, v208\n"
    "v_cmp_le_i32 s[94:95], 2, v208\n"
    "v_cmp_le_i32 s[96:97], 3, v208\n"
    "v_mov_b32 v156, 0x3f803f80\n"
    "v_mov_b32 v157, 0x3f803f80\n"
    "v_mov_b32 v158, 0x3f803f80\n"
    "v_mov_b32 v159, 0x3f803f80\n"
    "v_cndmask_b32 v160, 0, v156, s[80:81]\n"
    "v_cndmask_b32 v161, 0, v156, s[80:81]\n"
    "v_mov_b32 v162, 0xd028965f\n"
    "s_lshl_b32 s65, s72, 9\n"
    "s_add_u32 s65, s65, 0x201000\n"
    "v_and_b32 v208, 7, v200\n"
    "v_lshl_add_u32 v208, v208, 6, s65\n"
    "v_lshrrev_b32 v209, 3, v200\n"
    "v_lshl_add_u32 v208, v209, 14, v208\n"
    "v_cmp_gt_u32 vcc, 24, v200\n"
    "v_mov_b32 v209, 0x7fffffff\n"
    "s_nop 0\n"
    "v_cndmask_b32 v208, v209, v208, vcc\n"
    "s_mov_b32 s69, 0\n"
    "ab_ww1_%=:\n"
    "buffer_load_dword v209, v208, s[20:23], 0 offen sc1\n"
    "s_add_u32 s69, s69, 1\n"
    "s_cmp_gt_u32 s69, 0x40000\n"
    "s_cselect_b32 s64, 1, 0\n"
    "s_or_b32 s70, s70, s64\n"
    "s_waitcnt vmcnt(0)\n"
    "s_cbranch_scc1 ab_wwok2_%=\n"
    "v_cmp_gt_u32 vcc, 32, v209\n"
    "s_and_b64 vcc, vcc, s[88:89]\n"
    "s_cbranch_scc0 ab_wwok2_%=\n"
    "s_sleep 1\n"
    "s_branch ab_ww1_%=\n"
    "ab_wwok2_%=:\n"
    "buffer_inv sc1\n"
    "s_waitcnt vmcnt(0)\n"
    "s_waitcnt lgkmcnt(0)\n"
    "s_barrier\n"
    "s_mul_i32 s64, s71, 6\n"
    "s_add_u32 s64, s64, 0\n"
    "s_lshr_b32 s65, s64, 3\n"
    "s_mul_i32 s65, s65, 0x1000000\n"
    "s_and_b32 s64, s64, 7\n"
    "s_lshl_b32 s64, s64, 10\n"
    "s_add_u32 s64, s64, s65\n"
    "s_lshl_b32 s65, s72, 19\n"
    "s_add_u32 s44, s64, s65\n"
    "s_mul_i32 s64, s71, 6\n"
    "s_add_u32 s64, s64, 1\n"
    "s_lshr_b32 s65, s64, 3\n"
    "s_mul_i32 s65, s65, 0x1000000\n"
    "s_and_b32 s64, s64, 7\n"
    "s_lshl_b32 s64, s64, 10\n"
    "s_add_u32 s64, s64, s65\n"
    "s_lshl_b32 s65, s72, 19\n"
    "s_add_u32 s45, s64, s65\n"
    "s_mul_i32 s64, s71, 6\n"
    "s_add_u32 s64, s64, 2\n"
    "s_lshr_b32 s65, s64, 3\n"
    "s_mul_i32 s65, s65, 0x1000000\n"
    "s_and_b32 s64, s64, 7\n"
    "s_lshl_b32 s64, s64, 10\n"
    "s_add_u32 s64, s64, s65\n"
    "s_lshl_b32 s65, s72, 19\n"
    "s_add_u32 s46, s64, s65\n"
    "s_mul_i32 s64, s71, 6\n"
    "s_add_u32 s64, s64, 3\n"
    "s_lshr_b32 s65, s64, 3\n"
    "s_mul_i32 s65, s65, 0x1000000\n"
    "s_and_b32 s64, s64, 7\n"
    "s_lshl_b32 s64, s64, 10\n"
    "s_add_u32 s64, s64, s65\n"
    "s_lshl_b32 s65, s72, 19\n"
    "s_add_u32 s47, s64, s65\n"
    "s_mul_i32 s64, s71, 6\n"
    "s_add_u32 s64, s64, 4\n"
    "s_lshr_b32 s65, s64, 3\n"
    "s_mul_i32 s65, s65, 0x1000000\n"
    "s_and_b32 s64, s64, 7\n"
    "s_lshl_b32 s64, s64, 10\n"
    "s_add_u32 s64, s64, s65\n"
    "s_lshl_b32 s65, s72, 19\n"
    "s_add_u32 s48, s64, s65\n"
    "s_mul_i32 s64, s71, 6\n"
    "s_add_u32 s64, s64, 5\n"
    "s_lshr_b32 s65, s64, 3\n"
    "s_mul_i32 s65, s65, 0x1000000\n"
    "s_and_b32 s64, s64, 7\n"
    "s_lshl_b32 s64, s64, 10\n"
    "s_add_u32 s64, s64, s65\n"
    "s_lshl_b32 s65, s72, 19\n"
    "s_add_u32 s49, s64, s65\n"
    "s_mul_i32 s50, s71, 6144\n"
    "s_add_u32 s50, s50, 16384\n"
    "s_mov_b32 s43, 0\n"
    "s_mov_b32 s51, 16384\n"
    "s_add_u32 m0, s50, 0\n"
    "s_add_u32 s64, s44, s43\n"
    "buffer_load_dwordx4 v202, s[24:27], s64 offen lds\n"
    "s_add_u32 m0, s50, 1024\n"
    "s_add_u32 s64, s45, s43\n"
    "buffer_load_dwordx4 v202, s[24:27], s64 offen lds\n"
    "s_add_u32 m0, s50, 2048\n"
    "s_add_u32 s64, s46, s43\n"
    "buffer_load_dwordx4 v202, s[24:27], s64 offen lds\n"
    "s_add_u32 m0, s50, 3072\n"
    "s_add_u32 s64, s47, s43\n"
    "buffer_load_dwordx4 v202, s[24:27], s64 offen lds\n"
    "s_add_u32 m0, s50, 4096\n"
    "s_add_u32 s64, s48, s43\n"
    "buffer_load_dwordx4 v202, s[24:27], s64 offen lds\n"
    "s_add_u32 m0, s50, 5120\n"
    "s_add_u32 s64, s49, s43\n"
    "buffer_load_dwordx4 v202, s[24:27], s64 offen lds\n"
    "s_add_u32 s43, s43, 8192\n"
    "s_min_u32 s43, s43, 516096\n"
    "s_add_u32 s50, s50, 24576\n"
    "s_sub_u32 s64, s50, 122880\n"
    "s_cmp_ge_u32 s50, 139264\n"
    "s_cselect_b32 s50, s64, s50\n"
    "s_add_u32 m0, s50, 0\n"
    "s_add_u32 s64, s44, s43\n"
    "buffer_load_dwordx4 v202, s[24:27], s64 offen lds\n"
    "s_add_u32 m0, s50, 1024\n"
    "s_add_u32 s64, s45, s43\n"
    "buffer_load_dwordx4 v202, s[24:27], s64 offen lds\n"
    "s_add_u32 m0, s50, 2048\n"
    "s_add_u32 s64, s46, s43\n"
    "buffer_load_dwordx4 v202, s[24:27], s64 offen lds\n"
    "s_add_u32 m0, s50, 3072\n"
    "s_add_u32 s64, s47, s43\n"
    "buffer_load_dwordx4 v202, s[24:27], s64 offen lds\n"
    "s_add_u32 m0, s50, 4096\n"
    "s_add_u32 s64, s48, s43\n"
    "buffer_load_dwordx4 v202, s[24:27], s64 offen lds\n"
    "s_add_u32 m0, s50, 5120\n"
    "s_add_u32 s64, s49, s43\n"
    "buffer_load_dwordx4 v202, s[24:27], s64 offen lds\n"
    "s_add_u32 s43, s43, 8192\n"
    "s_min_u32 s43, s43, 516096\n"
    "s_add_u32 s50, s50, 24576\n"
    "s_sub_u32 s64, s50, 122880\n"
    "s_cmp_ge_u32 s50, 139264\n"
    "s_cselect_b32 s50, s64, s50\n"
    "s_add_u32 m0, s50, 0\n"
    "s_add_u32 s64, s44, s43\n"
    "buffer_load_dwordx4 v202, s[24:27], s64 offen lds\n"
    "s_add_u32 m0, s50, 1024\n"
    "s_add_u32 s64, s45, s43\n"
    "buffer_load_dwordx4 v202, s[24:27], s64 offen lds\n"
    "s_add_u32 m0, s50, 2048\n"
    "s_add_u32 s64, s46, s43\n"
    "buffer_load_dwordx4 v202, s[24:27], s64 offen lds\n"
    "s_add_u32 m0, s50, 3072\n"
    "s_add_u32 s64, s47, s43\n"
    "buffer_load_dwordx4 v202, s[24:27], s64 offen lds\n"
    "s_add_u32 m0, s50, 4096\n"
    "s_add_u32 s64, s48, s43\n"
    "buffer_load_dwordx4 v202, s[24:27], s64 offen lds\n"
    "s_add_u32 m0, s50, 5120\n"
    "s_add_u32 s64, s49, s43\n"
    "buffer_load_dwordx4 v202, s[24:27], s64 offen lds\n"
    "s_add_u32 s43, s43, 8192\n"
    "s_min_u32 s43, s43, 516096\n"
    "s_add_u32 s50, s50, 24576\n"
    "s_sub_u32 s64, s50, 122880\n"
    "s_cmp_ge_u32 s50, 139264\n"
    "s_cselect_b32 s50, s64, s50\n"
    "s_waitcnt vmcnt(12)\n"
    "s_barrier\n"
    "v_add_u32 v220, s51, v202\n"
    "s_lshl_b32 s64, s73, 12\n"
    "v_add_u32 v218, s64, v220\n"
    "s_lshl_b32 s64, s74, 12\n"
    "v_add_u32 v219, s64, v220\n"
    "s_add_u32 s51, s51, 24576\n"
    "s_cmp_ge_u32 s51, 139264\n"
    "s_cselect_b32 s51, 16384, s51\n"
    "ds_read_b128 v[0:3], v218\n"
    "ds_read_b128 v[16:19], v220 offset:8192\n"
    "ds_read_b128 v[32:35], v220 offset:12288\n"
    "ds_read_b128 v[4:7], v218 offset:1024\n"
    "ds_read_b128 v[20:23], v220 offset:9216\n"
    "ds_read_b128 v[36:39], v220 offset:13312\n"
    "ds_read_b128 v[8:11], v218 offset:2048\n"
    "ds_read_b128 v[24:27], v220 offset:10240\n"
    "ds_read_b128 v[40:43], v220 offset:14336\n"
    "ds_read_b128 v[12:15], v218 offset:3072\n"
    "ds_read_b128 v[28:31], v220 offset:11264\n"
    "ds_read_b128 v[44:47], v220 offset:15360\n"
    "ds_read_b128 v[48:51], v219 offset:16384\n"
    "ds_read_b128 v[52:55], v219 offset:17408\n"
    "ds_read_b128 v[56:59], v219 offset:18432\n"
    "ds_read_b128 v[60:63], v219 offset:19456\n"
    "s_mov_b32 s75, 0\n"
    "s_mov_b32 s78, 0\n"
    "s_mov_b32 s69, 0\n"
    "ab_ww3_%=:\n"
    "buffer_load_dword v208, v204, s[20:23], 0 offen sc1\n"
    "s_add_u32 s69, s69, 1\n"
    "s_cmp_gt_u32 s69, 0x40000\n"
    "s_cselect_b32 s64, 1, 0\n"
    "s_or_b32 s70, s70, s64\n"
    "s_waitcnt vmcnt(0)\n"
    "s_cbranch_scc1 ab_wwok4_%=\n"
    "v_cmp_gt_u32 vcc, 1, v208\n"
    "s_and_b64 vcc, vcc, s[82:83]\n"
    "s_cbranch_scc0 ab_wwok4_%=\n"
    "s_sleep 1\n"
    "s_branch ab_ww3_%=\n"
    "ab_wwok4_%=:\n"
    "s_lshl_b32 s64, s72, 6\n"
    "s_add_u32 s64, s64, s75\n"
    "s_lshl_b32 s64, s64, 12\n"
    "s_lshl_b32 s65, s74, 11\n"
    "s_add_u32 s65, s65, s64\n"
    "s_add_u32 s65, s65, 0x800000\n"
    "buffer_load_dwordx4 v[64:67], v202, s[28:31], s64 offen sc1\n"
    "buffer_load_dwordx4 v[68:71], v202, s[28:31], s64 offen offset:1024 sc1\n"
    "buffer_load_dwordx4 v[72:75], v202, s[28:31], s64 offen offset:2048 sc1\n"
    "buffer_load_dwordx4 v[76:79], v202, s[28:31], s64 offen offset:3072 sc1\n"
    "buffer_load_dwordx2 v[80:81], v207, s[28:31], s65 offen sc1\n"
    "buffer_load_dwordx2 v[82:83], v207, s[28:31], s65 offen offset:1024 sc1\n"
    "ab_loop_%=:\n"
    "s_waitcnt lgkmcnt(0)\n"
    "v_mfma_f32_16x16x32_bf16 v[88:91], v[16:19], v[0:3], 0\n"
    "v_mfma_f32_16x16x32_bf16 v[92:95], v[32:35], v[0:3], 0\n"
    "v_mfma_f32_16x16x32_bf16 v[88:91], v[20:23], v[4:7], v[88:91]\n"
    "v_mfma_f32_16x16x32_bf16 v[92:95], v[36:39], v[4:7], v[92:95]\n"
    "v_mfma_f32_16x16x32_bf16 v[88:91], v[24:27], v[8:11], v[88:91]\n"
    "v_mfma_f32_16x16x32_bf16 v[92:95], v[40:43], v[8:11], v[92:95]\n"
    "v_mfma_f32_16x16x32_bf16 v[88:91], v[28:31], v[12:15], v[88:91]\n"
    "v_mfma_f32_16x16x32_bf16 v[92:95], v[44:47], v[12:15], v[92:95]\n"
    "s_cmp_lg_u32 s78, 0\n"
    "s_cbranch_scc1 ab_early_%=\n"
    "s_waitcnt vmcnt(0)\n"
    "ab_early_%=:\n"
    "s_waitcnt vmcnt(15)\n"
    "buffer_load_dword v211, v204, s[20:23], 0 offen sc1\n"
    "v_mfma_f32_16x16x32_bf16 v[84:87], v[64:67], v[0:3], 0\n"
    "v_mfma_f32_16x16x32_bf16 v[84:87], v[68:71], v[4:7], v[84:87]\n"
    "v_mfma_f32_16x16x32_bf16 v[84:87], v[72:75], v[8:11], v[84:87]\n"
    "v_mfma_f32_16x16x32_bf16 v[84:87], v[76:79], v[12:15], v[84:87]\n"
    "s_cmp_lg_u32 s73, 0\n"
    "s_cbranch_scc1 ab_tt1_%=\n"
    "v_cndmask_b32 v88, v162, v88, s[90:91]\n"
    "v_mov_b32 v92, v162\n"
    "v_cndmask_b32 v89, v162, v89, s[92:93]\n"
    "v_mov_b32 v93, v162\n"
    "v_cndmask_b32 v90, v162, v90, s[94:95]\n"
    "v_mov_b32 v94, v162\n"
    "v_cndmask_b32 v91, v162, v91, s[96:97]\n"
    "v_mov_b32 v95, v162\n"
    "s_branch ab_masked_%=\n"
    "ab_tt1_%=:\n"
    "s_nop 3\n"
    "v_cndmask_b32 v92, v162, v92, s[90:91]\n"
    "v_cndmask_b32 v93, v162, v93, s[92:93]\n"
    "v_cndmask_b32 v94, v162, v94, s[94:95]\n"
    "v_cndmask_b32 v95, v162, v95, s[96:97]\n"
    "ab_masked_%=:\n"
    "v_max3_f32 v208, v88, v89, v90\n"
    "v_max3_f32 v208, v208, v91, v92\n"
    "v_max3_f32 v208, v208, v93, v94\n"
    "v_max3_f32 v208, v208, v95, v84\n"
    "v_max3_f32 v208, v208, v85, v86\n"
    "v_max_f32 v208, v208, v87\n"
    "v_mov_b32 v209, v208\n"
    "s_nop 1\n"
    "v_permlane16_swap_b32 v208, v209\n"
    "v_max_f32 v208, v208, v209\n"
    "v_mov_b32 v209, v208\n"
    "s_nop 1\n"
    "v_permlane32_swap_b32 v208, v209\n"
    "v_max_f32 v208, v208, v209\n"
    "v_mul_f32 v208, 0xbe0293ee, v208\n"
    "v_fmamk_f32 v88, v88, 0x3e0293ee, v208\n"
    "v_fmamk_f32 v89, v89, 0x3e0293ee, v208\n"
    "v_fmamk_f32 v90, v90, 0x3e0293ee, v208\n"
    "v_fmamk_f32 v91, v91, 0x3e0293ee, v208\n"
    "v_fmamk_f32 v92, v92, 0x3e0293ee, v208\n"
    "v_fmamk_f32 v93, v93, 0x3e0293ee, v208\n"
    "v_fmamk_f32 v94, v94, 0x3e0293ee, v208\n"
    "v_fmamk_f32 v95, v95, 0x3e0293ee, v208\n"
    "v_fmamk_f32 v84, v84, 0x3e0293ee, v208\n"
    "v_fmamk_f32 v85, v85, 0x3e0293ee, v208\n"
    "v_fmamk_f32 v86, v86, 0x3e0293ee, v208\n"
    "v_fmamk_f32 v87, v87, 0x3e0293ee, v208\n"
    "v_exp_f32 v88, v88\n"
    "v_exp_f32 v89, v89\n"
    "v_exp_f32 v90, v90\n"
    "v_exp_f32 v91, v91\n"
    "v_exp_f32 v92, v92\n"
    "v_exp_f32 v93, v93\n"
    "v_exp_f32 v94, v94\n"
    "v_exp_f32 v95, v95\n"
    "v_exp_f32 v84, v84\n"
    "v_cvt_pk_bf16_f32 v168, v88, v89\n"
    "v_cvt_pk_bf16_f32 v169, v90, v91\n"
    "v_cvt_pk_bf16_f32 v170, v92, v93\n"
    "v_cvt_pk_bf16_f32 v171, v94, v95\n"
    "v_exp_f32 v85, v85\n"
    "v_exp_f32 v86, v86\n"
    "v_mfma_f32_16x16x32_bf16 v[96:99], v[156:159], v[168:171], 0\n"
    "v_exp_f32 v87, v87\n"
    "v_mfma_f32_16x16x32_bf16 v[100:103], v[48:51], v[168:171], 0\n"
    "v_cvt_pk_bf16_f32 v172, v84, v85\n"
    "v_cvt_pk_bf16_f32 v173, v86, v87\n"
    "v_mfma_f32_16x16x32_bf16 v[104:107], v[52:55], v[168:171], 0\n"
    "v_cndmask_b32 v164, 0, v80, s[80:81]\n"
    "v_cndmask_b32 v165, 0, v81, s[80:81]\n"
    "v_mfma_f32_16x16x16_bf16 v[96:99], v[160:161], v[172:173], v[96:99]\n"
    "v_cndmask_b32 v166, v80, 0, s[80:81]\n"
    "v_cndmask_b32 v167, v81, 0, s[80:81]\n"
    "v_mfma_f32_16x16x32_bf16 v[108:111], v[56:59], v[168:171], 0\n"
    "v_mfma_f32_16x16x32_bf16 v[112:115], v[60:63], v[168:171], 0\n"
    "v_mfma_f32_16x16x16_bf16 v[100:103], v[164:165], v[172:173], v[100:103]\n"
    "v_mfma_f32_16x16x16_bf16 v[104:107], v[166:167], v[172:173], v[104:107]\n"
    "v_cndmask_b32 v164, 0, v82, s[80:81]\n"
    "v_cndmask_b32 v165, 0, v83, s[80:81]\n"
    "v_cndmask_b32 v166, v82, 0, s[80:81]\n"
    "v_cndmask_b32 v167, v83, 0, s[80:81]\n"
    "v_rcp_f32 v174, v96\n"
    "v_mfma_f32_16x16x16_bf16 v[108:111], v[164:165], v[172:173], v[108:111]\n"
    "v_mfma_f32_16x16x16_bf16 v[112:115], v[166:167], v[172:173], v[112:115]\n"
    "s_lshl_b32 s64, s74, 11\n"
    "v_add_u32 v209, s64, v206\n"
    "v_pk_mul_f32 v[100:101], v[100:101], v[174:175] op_sel_hi:[1,0]\n"
    "v_pk_mul_f32 v[102:103], v[102:103], v[174:175] op_sel_hi:[1,0]\n"
    "v_cvt_pk_bf16_f32 v148, v100, v101\n"
    "v_cvt_pk_bf16_f32 v149, v102, v103\n"
    "v_pk_mul_f32 v[104:105], v[104:105], v[174:175] op_sel_hi:[1,0]\n"
    "v_pk_mul_f32 v[106:107], v[106:107], v[174:175] op_sel_hi:[1,0]\n"
    "v_cvt_pk_bf16_f32 v150, v104, v105\n"
    "v_cvt_pk_bf16_f32 v151, v106, v107\n"
    "ds_write_b128 v209, v[148:151]\n"
    "v_pk_mul_f32 v[108:109], v[108:109], v[174:175] op_sel_hi:[1,0]\n"
    "v_pk_mul_f32 v[110:111], v[110:111], v[174:175] op_sel_hi:[1,0]\n"
    "v_cvt_pk_bf16_f32 v152, v108, v109\n"
    "v_cvt_pk_bf16_f32 v153, v110, v111\n"
    "v_pk_mul_f32 v[112:113], v[112:113], v[174:175] op_sel_hi:[1,0]\n"
    "v_pk_mul_f32 v[114:115], v[114:115], v[174:175] op_sel_hi:[1,0]\n"
    "v_cvt_pk_bf16_f32 v154, v112, v113\n"
    "v_cvt_pk_bf16_f32 v155, v114, v115\n"
    "ds_write_b128 v209, v[152:155] offset:1024\n"
    "s_add_u32 s67, s75, 1\n"
    "s_add_u32 s64, s75, 2\n"
    "v_mov_b32 v208, s75\n"
    "s_waitcnt vmcnt(0)\n"
    "v_cmp_gt_u32 vcc, s64, v211\n"
    "s_and_b64 vcc, vcc, s[82:83]\n"
    "s_cselect_b32 s77, 0, 1\n"
    "s_cmp_lt_u32 s67, 64\n"
    "s_cselect_b32 s77, s77, 0\n"
    "s_min_u32 s67, s67, 63\n"
    "s_cmp_eq_u32 s78, 0\n"
    "s_cbranch_scc1 ab_nopend_%=\n"
    "buffer_store_dword v208, v203, s[20:23], 0 offen sc1\n"
    "ab_nopend_%=:\n"
    "s_waitcnt lgkmcnt(0)\n"
    "s_barrier\n"
    "ds_read_b128 v[116:119], v206\n"
    "ds_read_b128 v[120:123], v206 offset:1024\n"
    "ds_read_b128 v[124:127], v206 offset:2048\n"
    "ds_read_b128 v[128:131], v206 offset:3072\n"
    "v_xor_b32 v206, 8192, v206\n"
    "s_and_b32 s76, s75, s42\n"
    "s_lshl_b32 s76, s76, 5\n"
    "s_add_u32 s76, s76, s72\n"
    "s_mul_i32 s76, s76, 36864\n"
    "s_lshl_b32 s64, s98, 10\n"
    "s_add_u32 s76, s76, s64\n"
    "s_add_u32 s62, s76, 4096\n"
    "s_add_u32 s63, s76, 8192\n"
    "v_add_u32 v220, s51, v202\n"
    "s_lshl_b32 s64, s73, 12\n"
    "v_add_u32 v218, s64, v220\n"
    "s_lshl_b32 s64, s74, 12\n"
    "v_add_u32 v219, s64, v220\n"
    "s_add_u32 s51, s51, 24576\n"
    "s_cmp_ge_u32 s51, 139264\n"
    "s_cselect_b32 s51, 16384, s51\n"
    "s_cmp_eq_u32 s77, 0\n"
    "s_cbranch_scc1 ab_nom_%=\n"
    "s_lshl_b32 s64, s72, 6\n"
    "s_add_u32 s64, s64, s67\n"
    "s_lshl_b32 s64, s64, 12\n"
    "s_lshl_b32 s65, s74, 11\n"
    "s_add_u32 s65, s65, s64\n"
    "s_add_u32 s65, s65, 0x800000\n"
    "buffer_load_dwordx4 v[64:67], v202, s[28:31], s64 offen sc1\n"
    "buffer_load_dwordx4 v[68:71], v202, s[28:31], s64 offen offset:1024 sc1\n"
    "buffer_load_dwordx4 v[72:75], v202, s[28:31], s64 offen offset:2048 sc1\n"
    "buffer_load_dwordx4 v[76:79], v202, s[28:31], s64 offen offset:3072 sc1\n"
    "buffer_load_dwordx2 v[80:81], v207, s[28:31], s65 offen sc1\n"
    "buffer_load_dwordx2 v[82:83], v207, s[28:31], s65 offen offset:1024 sc1\n"
    "ab_nom_%=:\n"
    "s_waitcnt lgkmcnt(3)\n"
    "v_mfma_f32_16x16x32_bf16 v[132:135], a[0:3], v[116:119], 0\n"
    "v_mfma_f32_16x16x32_bf16 v[136:139], a[16:19], v[116:119], 0\n"
    "s_waitcnt lgkmcnt(2)\n"
    "v_mfma_f32_16x16x32_bf16 v[132:135], a[4:7], v[120:123], v[132:135]\n"
    "v_mfma_f32_16x16x32_bf16 v[136:139], a[20:23], v[120:123], v[136:139]\n"
    "s_waitcnt lgkmcnt(1)\n"
    "v_mfma_f32_16x16x32_bf16 v[132:135], a[8:11], v[124:127], v[132:135]\n"
    "v_mfma_f32_16x16x32_bf16 v[136:139], a[24:27], v[124:127], v[136:139]\n"
    "s_waitcnt lgkmcnt(0)\n"
    "v_mfma_f32_16x16x32_bf16 v[132:135], a[12:15], v[128:131], v[132:135]\n"
    "v_mfma_f32_16x16x32_bf16 v[136:139], a[28:31], v[128:131], v[136:139]\n"
    "v_mfma_f32_16x16x32_bf16 v[140:143], a[32:35], v[116:119], 0\n"
    "s_add_u32 m0, s50, 0\n"
    "s_add_u32 s64, s44, s43\n"
    "buffer_load_dwordx4 v202, s[24:27], s64 offen lds\n"
    "v_mfma_f32_16x16x32_bf16 v[144:147], a[48:51], v[116:119], 0\n"
    "ds_read_b128 v[0:3], v218\n"
    "v_mfma_f32_16x16x32_bf16 v[140:143], a[36:39], v[120:123], v[140:143]\n"
    "ds_read_b128 v[16:19], v220 offset:8192\n"
    "v_mfma_f32_16x16x32_bf16 v[144:147], a[52:55], v[120:123], v[144:147]\n"
    "ds_read_b128 v[32:35], v220 offset:12288\n"
    "v_mfma_f32_16x16x32_bf16 v[140:143], a[40:43], v[124:127], v[140:143]\n"
    "v_cvt_pk_bf16_f32 v148, v132, v133\n"
    "v_cvt_pk_bf16_f32 v149, v134, v135\n"
    "v_mfma_f32_16x16x32_bf16 v[144:147], a[56:59], v[124:127], v[144:147]\n"
    "v_cvt_pk_bf16_f32 v150, v136, v137\n"
    "v_mfma_f32_16x16x32_bf16 v[140:143], a[44:47], v[128:131], v[140:143]\n"
    "v_cvt_pk_bf16_f32 v151, v138, v139\n"
    "v_mfma_f32_16x16x32_bf16 v[144:147], a[60:63], v[128:131], v[144:147]\n"
    "buffer_store_dwordx4 v[148:151], v202, s[84:87], s76 offen sc1\n"
    "v_mfma_f32_16x16x32_bf16 v[132:135], a[64:67], v[116:119], 0\n"
    "s_add_u32 m0, s50, 1024\n"
    "s_add_u32 s64, s45, s43\n"
    "buffer_load_dwordx4 v202, s[24:27], s64 offen lds\n"
    "v_mfma_f32_16x16x32_bf16 v[136:139], a[80:83], v[116:119], 0\n"
    "ds_read_b128 v[4:7], v218 offset:1024\n"
    "v_mfma_f32_16x16x32_bf16 v[132:135], a[68:71], v[120:123], v[132:135]\n"
    "ds_read_b128 v[20:23], v220 offset:9216\n"
    "v_mfma_f32_16x16x32_bf16 v[136:139], a[84:87], v[120:123], v[136:139]\n"
    "ds_read_b128 v[36:39], v220 offset:13312\n"
    "v_mfma_f32_16x16x32_bf16 v[132:135], a[72:75], v[124:127], v[132:135]\n"
    "v_cvt_pk_bf16_f32 v152, v140, v141\n"
    "v_cvt_pk_bf16_f32 v153, v142, v143\n"
    "v_mfma_f32_16x16x32_bf16 v[136:139], a[88:91], v[124:127], v[136:139]\n"
    "v_cvt_pk_bf16_f32 v154, v144, v145\n"
    "v_mfma_f32_16x16x32_bf16 v[132:135], a[76:79], v[128:131], v[132:135]\n"
    "v_cvt_pk_bf16_f32 v155, v146, v147\n"
    "v_mfma_f32_16x16x32_bf16 v[136:139], a[92:95], v[128:131], v[136:139]\n"
    "buffer_store_dwordx4 v[152:155], v202, s[84:87], s76 offen offset:1024 sc1\n"
    "v_mfma_f32_16x16x32_bf16 v[140:143], a[96:99], v[116:119], 0\n"
    "s_add_u32 m0, s50, 2048\n"
    "s_add_u32 s64, s46, s43\n"
    "buffer_load_dwordx4 v202, s[24:27], s64 offen lds\n"
    "v_mfma_f32_16x16x32_bf16 v[144:147], a[112:115], v[116:119], 0\n"
    "ds_read_b128 v[8:11], v218 offset:2048\n"
    "v_mfma_f32_16x16x32_bf16 v[140:143], a[100:103], v[120:123], v[140:143]\n"
    "ds_read_b128 v[24:27], v220 offset:10240\n"
    "v_mfma_f32_16x16x32_bf16 v[144:147], a[116:119], v[120:123], v[144:147]\n"
    "ds_read_b128 v[40:43], v220 offset:14336\n"
    "v_mfma_f32_16x16x32_bf16 v[140:143], a[104:107], v[124:127], v[140:143]\n"
    "v_cvt_pk_bf16_f32 v148, v132, v133\n"
    "v_cvt_pk_bf16_f32 v149, v134, v135\n"
    "v_mfma_f32_16x16x32_bf16 v[144:147], a[120:123], v[124:127], v[144:147]\n"
    "v_cvt_pk_bf16_f32 v150, v136, v137\n"
    "v_mfma_f32_16x16x32_bf16 v[140:143], a[108:111], v[128:131], v[140:143]\n"
    "v_cvt_pk_bf16_f32 v151, v138, v139\n"
    "v_mfma_f32_16x16x32_bf16 v[144:147], a[124:127], v[128:131], v[144:147]\n"
    "buffer_store_dwordx4 v[148:151], v202, s[84:87], s76 offen offset:2048 sc1\n"
    "v_mfma_f32_16x16x32_bf16 v[132:135], a[128:131], v[116:119], 0\n"
    "s_add_u32 m0, s50, 3072\n"
    "s_add_u32 s64, s47, s43\n"
    "buffer_load_dwordx4 v202, s[24:27], s64 offen lds\n"
    "v_mfma_f32_16x16x32_bf16 v[136:139], a[144:147], v[116:119], 0\n"
    "ds_read_b128 v[12:15], v218 offset:3072\n"
    "v_mfma_f32_16x16x32_bf16 v[132:135], a[132:135], v[120:123], v[132:135]\n"
    "ds_read_b128 v[28:31], v220 offset:11264\n"
    "v_mfma_f32_16x16x32_bf16 v[136:139], a[148:151], v[120:123], v[136:139]\n"
    "ds_read_b128 v[44:47], v220 offset:15360\n"
    "v_mfma_f32_16x16x32_bf16 v[132:135], a[136:139], v[124:127], v[132:135]\n"
    "v_cvt_pk_bf16_f32 v152, v140, v141\n"
    "v_cvt_pk_bf16_f32 v153, v142, v143\n"
    "v_mfma_f32_16x16x32_bf16 v[136:139], a[152:155], v[124:127], v[136:139]\n"
    "v_cvt_pk_bf16_f32 v154, v144, v145\n"
    "v_mfma_f32_16x16x32_bf16 v[132:135], a[140:143], v[128:131], v[132:135]\n"
    "v_cvt_pk_bf16_f32 v155, v146, v147\n"
    "v_mfma_f32_16x16x32_bf16 v[136:139], a[156:159], v[128:131], v[136:139]\n"
    "buffer_store_dwordx4 v[152:155], v202, s[84:87], s76 offen offset:3072 sc1\n"
    "v_mfma_f32_16x16x32_bf16 v[140:143], a[160:163], v[116:119], 0\n"
    "s_add_u32 m0, s50, 4096\n"
    "s_add_u32 s64, s48, s43\n"
    "buffer_load_dwordx4 v202, s[24:27], s64 offen lds\n"
    "v_mfma_f32_16x16x32_bf16 v[144:147], a[176:179], v[116:119], 0\n"
    "ds_read_b128 v[48:51], v219 offset:16384\n"
    "v_mfma_f32_16x16x32_bf16 v[140:143], a[164:167], v[120:123], v[140:143]\n"
    "ds_read_b128 v[52:55], v219 offset:17408\n"
    "v_mfma_f32_16x16x32_bf16 v[144:147], a[180:183], v[120:123], v[144:147]\n"
    "ds_read_b128 v[56:59], v219 offset:18432\n"
    "v_mfma_f32_16x16x32_bf16 v[140:143], a[168:171], v[124:127], v[140:143]\n"
    "v_cvt_pk_bf16_f32 v148, v132, v133\n"
    "v_cvt_pk_bf16_f32 v149, v134, v135\n"
    "v_mfma_f32_16x16x32_bf16 v[144:147], a[184:187], v[124:127], v[144:147]\n"
    "v_cvt_pk_bf16_f32 v150, v136, v137\n"
    "v_mfma_f32_16x16x32_bf16 v[140:143], a[172:175], v[128:131], v[140:143]\n"
    "v_cvt_pk_bf16_f32 v151, v138, v139\n"
    "v_mfma_f32_16x16x32_bf16 v[144:147], a[188:191], v[128:131], v[144:147]\n"
    "buffer_store_dwordx4 v[148:151], v202, s[84:87], s62 offen sc1\n"
    "v_mfma_f32_16x16x32_bf16 v[132:135], a[192:195], v[116:119], 0\n"
    "s_add_u32 m0, s50, 5120\n"
    "s_add_u32 s64, s49, s43\n"
    "buffer_load_dwordx4 v202, s[24:27], s64 offen lds\n"
    "v_mfma_f32_16x16x32_bf16 v[136:139], a[208:211], v[116:119], 0\n"
    "ds_read_b128 v[60:63], v219 offset:19456\n"
    "v_mfma_f32_16x16x32_bf16 v[132:135], a[196:199], v[120:123], v[132:135]\n"
    "v_mfma_f32_16x16x32_bf16 v[136:139], a[212:215], v[120:123], v[136:139]\n"
    "v_mfma_f32_16x16x32_bf16 v[132:135], a[200:203], v[124:127], v[132:135]\n"
    "v_cvt_pk_bf16_f32 v152, v140, v141\n"
    "v_cvt_pk_bf16_f32 v153, v142, v143\n"
    "v_mfma_f32_16x16x32_bf16 v[136:139], a[216:219], v[124:127], v[136:139]\n"
    "v_cvt_pk_bf16_f32 v154, v144, v145\n"
    "v_mfma_f32_16x16x32_bf16 v[132:135], a[204:207], v[128:131], v[132:135]\n"
    "v_cvt_pk_bf16_f32 v155, v146, v147\n"
    "v_mfma_f32_16x16x32_bf16 v[136:139], a[220:223], v[128:131], v[136:139]\n"
    "buffer_store_dwordx4 v[152:155], v202, s[84:87], s62 offen offset:1024 sc1\n"
    "v_mfma_f32_16x16x32_bf16 v[140:143], a[224:227], v[116:119], 0\n"
    "v_mfma_f32_16x16x32_bf16 v[144:147], a[240:243], v[116:119], 0\n"
    "v_mfma_f32_16x16x32_bf16 v[140:143], a[228:231], v[120:123], v[140:143]\n"
    "v_mfma_f32_16x16x32_bf16 v[144:147], a[244:247], v[120:123], v[144:147]\n"
    "v_mfma_f32_16x16x32_bf16 v[140:143], a[232:235], v[124:127], v[140:143]\n"
    "v_cvt_pk_bf16_f32 v148, v132, v133\n"
    "v_cvt_pk_bf16_f32 v149, v134, v135\n"
    "v_mfma_f32_16x16x32_bf16 v[144:147], a[248:251], v[124:127], v[144:147]\n"
    "v_cvt_pk_bf16_f32 v150, v136, v137\n"
    "v_mfma_f32_16x16x32_bf16 v[140:143], a[236:239], v[128:131], v[140:143]\n"
    "v_cvt_pk_bf16_f32 v151, v138, v139\n"
    "v_mfma_f32_16x16x32_bf16 v[144:147], a[252:255], v[128:131], v[144:147]\n"
    "buffer_store_dwordx4 v[148:151], v202, s[84:87], s62 offen offset:2048 sc1\n"
    "v_mfma_f32_16x16x32_bf16 v[132:135], v[224:227], v[116:119], 0\n"
    "v_mfma_f32_16x16x32_bf16 v[136:139], v[240:243], v[116:119], 0\n"
    "v_mfma_f32_16x16x32_bf16 v[132:135], v[228:231], v[120:123], v[132:135]\n"
    "v_mfma_f32_16x16x32_bf16 v[136:139], v[244:247], v[120:123], v[136:139]\n"
    "v_mfma_f32_16x16x32_bf16 v[132:135], v[232:235], v[124:127], v[132:135]\n"
    "v_cvt_pk_bf16_f32 v152, v140, v141\n"
    "v_cvt_pk_bf16_f32 v153, v142, v143\n"
    "v_mfma_f32_16x16x32_bf16 v[136:139], v[248:251], v[124:127], v[136:139]\n"
    "v_cvt_pk_bf16_f32 v154, v144, v145\n"
    "v_mfma_f32_16x16x32_bf16 v[132:135], v[236:239], v[128:131], v[132:135]\n"
    "v_cvt_pk_bf16_f32 v155, v146, v147\n"
    "v_mfma_f32_16x16x32_bf16 v[136:139], v[252:255], v[128:131], v[136:139]\n"
    "buffer_store_dwordx4 v[152:155], v202, s[84:87], s62 offen offset:3072 sc1\n"
    "s_nop 4\n"
    "v_cvt_pk_bf16_f32 v148, v132, v133\n"
    "v_cvt_pk_bf16_f32 v149, v134, v135\n"
    "v_cvt_pk_bf16_f32 v150, v136, v137\n"
    "v_cvt_pk_bf16_f32 v151, v138, v139\n"
    "buffer_store_dwordx4 v[148:151], v202, s[84:87], s63 offen sc1\n"
    "s_add_u32 s43, s43, 8192\n"
    "s_min_u32 s43, s43, 516096\n"
    "s_add_u32 s50, s50, 24576\n"
    "s_sub_u32 s64, s50, 122880\n"
    "s_cmp_ge_u32 s50, 139264\n"
    "s_cselect_b32 s50, s64, s50\n"
    "s_mov_b32 s78, s77\n"
    "s_add_u32 s75, s75, 1\n"
    "s_cmp_lg_u32 s77, 0\n"
    "s_cbranch_scc1 ab_loop_%=\n"
    "buffer_load_dword v211, v204, s[20:23], 0 offen sc1\n"
    "v_mov_b32 v208, s75\n"
    "s_add_u32 s67, s75, 1\n"
    "s_waitcnt vmcnt(0)\n"
    "buffer_store_dword v208, v203, s[20:23], 0 offen sc1\n"
    "s_cmp_ge_u32 s75, 64\n"
    "s_cbranch_scc1 ab_end_%=\n"
    "v_cmp_gt_u32 vcc, s67, v211\n"
    "s_and_b64 vcc, vcc, s[82:83]\n"
    "s_cbranch_scc0 ab_gotm_%=\n"
    "s_mov_b32 s69, 0\n"
    "ab_ww5_%=:\n"
    "buffer_load_dword v209, v204, s[20:23], 0 offen sc1\n"
    "s_add_u32 s69, s69, 1\n"
    "s_cmp_gt_u32 s69, 0x40000\n"
    "s_cselect_b32 s64, 1, 0\n"
    "s_or_b32 s70, s70, s64\n"
    "s_waitcnt vmcnt(0)\n"
    "s_cbranch_scc1 ab_wwok6_%=\n"
    "v_cmp_gt_u32 vcc, s67, v209\n"
    "s_and_b64 vcc, vcc, s[82:83]\n"
    "s_cbranch_scc0 ab_wwok6_%=\n"
    "s_sleep 1\n"
    "s_branch ab_ww5_%=\n"
    "ab_wwok6_%=:\n"
    "ab_gotm_%=:\n"
    "s_lshl_b32 s64, s72, 6\n"
    "s_add_u32 s64, s64, s75\n"
    "s_lshl_b32 s64, s64, 12\n"
    "s_lshl_b32 s65, s74, 11\n"
    "s_add_u32 s65, s65, s64\n"
    "s_add_u32 s65, s65, 0x800000\n"
    "buffer_load_dwordx4 v[64:67], v202, s[28:31], s64 offen sc1\n"
    "buffer_load_dwordx4 v[68:71], v202, s[28:31], s64 offen offset:1024 sc1\n"
    "buffer_load_dwordx4 v[72:75], v202, s[28:31], s64 offen offset:2048 sc1\n"
    "buffer_load_dwordx4 v[76:79], v202, s[28:31], s64 offen offset:3072 sc1\n"
    "buffer_load_dwordx2 v[80:81], v207, s[28:31], s65 offen sc1\n"
    "buffer_load_dwordx2 v[82:83], v207, s[28:31], s65 offen offset:1024 sc1\n"
    "s_branch ab_loop_%=\n"
    "ab_end_%=:\n"
    "s_waitcnt vmcnt(0)\n"
    :: "s"(__builtin_amdgcn_kernarg_segment_ptr()), "s"(ids)
    : "v0", "v1", "v2", "v3", "v4", "v5", "v6", "v7", "v8", "v9", "v10", "v11", "v12", "v13", "v14", "v15", "v16", "v17", "v18", "v19", "v20", "v21", "v22",
      "v23", "v24", "v25", "v26", "v27", "v28", "v29", "v30", "v31", "v32", "v33", "v34", "v35", "v36", "v37", "v38", "v39", "v40", "v41", "v42", "v43",
      "v44", "v45", "v46", "v47", "v48", "v49", "v50", "v51", "v52", "v53", "v54", "v55", "v56", "v57", "v58", "v59", "v60", "v61", "v62", "v63", "v64",
      "v65", "v66", "v67", "v68", "v69", "v70", "v71", "v72", "v73", "v74", "v75", "v76", "v77", "v78", "v79", "v80", "v81", "v82", "v83", "v84", "v85",
      "v86", "v87", "v88", "v89", "v90", "v91", "v92", "v93", "v94", "v95", "v96", "v97", "v98", "v99", "v100", "v101", "v102", "v103", "v104", "v105",
      "v106", "v107", "v108", "v109", "v110", "v111", "v112", "v113", "v114", "v115", "v116", "v117", "v118", "v119", "v120", "v121", "v122", "v123", "v124",
      "v125", "v126", "v127", "v128", "v129", "v130", "v131", "v132", "v133", "v134", "v135", "v136", "v137", "v138", "v139", "v140", "v141", "v142", "v143",
      "v144", "v145", "v146", "v147", "v148", "v149", "v150", "v151", "v152", "v153", "v154", "v155", "v156", "v157", "v158", "v159", "v160", "v161", "v162",
      "v163", "v164", "v165", "v166", "v167", "v168", "v169", "v170", "v171", "v172", "v173", "v174", "v175", "v176", "v177", "v178", "v179", "v180", "v181",
      "v182", "v183", "v184", "v185", "v186", "v187", "v188", "v189", "v190", "v191", "v192", "v193", "v194", "v195", "v196", "v197", "v198", "v199", "v200",
      "v201", "v202", "v203", "v204", "v205", "v206", "v207", "v208", "v209", "v210", "v211", "v212", "v213", "v214", "v215", "v216", "v217", "v218", "v219",
      "v220", "v221", "v222", "v223", "v224", "v225", "v226", "v227", "v228", "v229", "v230", "v231", "v232", "v233", "v234", "v235", "v236", "v237", "v238",
      "v239", "v240", "v241", "v242", "v243", "v244", "v245", "v246", "v247", "v248", "v249", "v250", "v251", "v252", "v253", "v254", "v255", "a0", "a1",
      "a2", "a3", "a4", "a5", "a6", "a7", "a8", "a9", "a10", "a11", "a12", "a13", "a14", "a15", "a16", "a17", "a18", "a19", "a20", "a21", "a22", "a23",
      "a24", "a25", "a26", "a27", "a28", "a29", "a30", "a31", "a32", "a33", "a34", "a35", "a36", "a37", "a38", "a39", "a40", "a41", "a42", "a43", "a44",
      "a45", "a46", "a47", "a48", "a49", "a50", "a51", "a52", "a53", "a54", "a55", "a56", "a57", "a58", "a59", "a60", "a61", "a62", "a63", "a64", "a65",
      "a66", "a67", "a68", "a69", "a70", "a71", "a72", "a73", "a74", "a75", "a76", "a77", "a78", "a79", "a80", "a81", "a82", "a83", "a84", "a85", "a86",
      "a87", "a88", "a89", "a90", "a91", "a92", "a93", "a94", "a95", "a96", "a97", "a98", "a99", "a100", "a101", "a102", "a103", "a104", "a105", "a106",
      "a107", "a108", "a109", "a110", "a111", "a112", "a113", "a114", "a115", "a116", "a117", "a118", "a119", "a120", "a121", "a122", "a123", "a124", "a125",
      "a126", "a127", "a128", "a129", "a130", "a131", "a132", "a133", "a134", "a135", "a136", "a137", "a138", "a139", "a140", "a141", "a142", "a143", "a144",
      "a145", "a146", "a147", "a148", "a149", "a150", "a151", "a152", "a153", "a154", "a155", "a156", "a157", "a158", "a159", "a160", "a161", "a162", "a163",
      "a164", "a165", "a166", "a167", "a168", "a169", "a170", "a171", "a172", "a173", "a174", "a175", "a176", "a177", "a178", "a179", "a180", "a181", "a182",
      "a183", "a184", "a185", "a186", "a187", "a188", "a189", "a190", "a191", "a192", "a193", "a194", "a195", "a196", "a197", "a198", "a199", "a200", "a201",
      "a202", "a203", "a204", "a205", "a206", "a207", "a208", "a209", "a210", "a211", "a212", "a213", "a214", "a215", "a216", "a217", "a218", "a219", "a220",
      "a221", "a222", "a223", "a224", "a225", "a226", "a227", "a228", "a229", "a230", "a231", "a232", "a233", "a234", "a235", "a236", "a237", "a238", "a239",
      "a240", "a241", "a242", "a243", "a244", "a245", "a246", "a247", "a248", "a249", "a250", "a251", "a252", "a253", "a254", "a255", "s20", "s21", "s22",
      "s23", "s24", "s25", "s26", "s27", "s28", "s29", "s30", "s31", "s36", "s37", "s38", "s39", "s40", "s41", "s42", "s43", "s44", "s45", "s46", "s47",
      "s48", "s49", "s50", "s51", "s52", "s53", "s54", "s55", "s56", "s57", "s58", "s59", "s60", "s61", "s62", "s63", "s64", "s65", "s66", "s67", "s68",
      "s69", "s70", "s71", "s72", "s73", "s74", "s75", "s76", "s77", "s78", "s79", "s80", "s81", "s82", "s83", "s84", "s85", "s86", "s87", "s88", "s89",
      "s90", "s91", "s92", "s93", "s94", "s95", "s96", "s97", "s98", "s99", "vcc", "scc", "memory");
}

DEV u4 pick(int k, int l) {
    u4 r;
#pragma unroll
    for (int i = 0; i < 4; ++i) r[i] = (k >> 1) == 4 * (l >> 4) + i ? ((k & 1) ? 0x3f800000u : 0x00003f80u) : 0u;
    return r;
}
template <int N> DEV void sum_blocks(rsrc_t rp, u32 base, int l, u4 sa, u4 sb, f4& a, f4& b) {
    u4 x[N];
#pragma unroll
    for (int i = 0; i < N; ++i) x[i] = ld4(rp, l * 16, base + i * TILEB);
    sched();
#pragma unroll
    for (int i = 0; i < N; ++i) { a = mma32(sa, x[i], a); b = mma32(sb, x[i], b); }
}

DEV void take_turn(volatile LAS u32* turn, u32 seg, bool& dead) {
    u32 spins = 0;
    while (!dead && (u32)__builtin_amdgcn_readfirstlane(*turn) < seg) {
        if (++spins > (SPIN_CAP << 4)) dead = true;
        __builtin_amdgcn_s_sleep(1);
    }
}

DEV void role_c(const Params& P, volatile LAS u32* turn, int u, int w, int l) {
    const rsrc_t ctl = mk(P.ws, CTL_BYTES);
    const rsrc_t rp = mk(P.ws + OFF_PAB, NSEG * NH * TILEB);
    const rsrc_t rh = mk(P.ws + OFF_HN, NSEG * HSEG);
    const rsrc_t rx = mk(P.x, SEQ * ROWB);
    const int i16 = l & 15, tt = u / 18, pr = u % 18;
    const int ka = 8 * (i16 >> 2) + (i16 & 3);
    const u4 sa = pick(ka, l), sb = pick(ka + 4, l), xa = pick(i16, l), xb = pick(16 + i16, l);
    bool dead = false;
#pragma unroll 1
    for (int seg = w; seg < NSEG; seg += 4) {
        const u4 xr = __builtin_amdgcn_raw_buffer_load_b128(rx, i16 * ROWB + (l >> 4) * 16, (u32)(seg * 32 + tt * 16) * ROWB + pr * 64, 0);
        const f4 z = {0, 0, 0, 0};
        f4 a = mma32(xa, xr, z), b = mma32(xb, xr, z);
        take_turn(turn, seg, dead);
        wait_words(ctl, OFF_MBAB + (u * 32 + l) * 64, l < 32, seg + 1, dead);
        *turn = seg + 1;
        STAMP(8 + seg);
        sum_blocks<32>(rp, (u32)(seg & P.ring) * NH * TILEB + u * 1024, l, sa, sb, a, b);
        const u4 h = cat(pk4(a), pk4(b));
        if (seg >= 56) STAMP(72 + (seg - 56) * 6 + 1);
        st4(h, rh, l * 16, (u32)seg * HSEG + u * 1024);
        float v[8], s = 0.f, q = 0.f;
#pragma unroll
        for (int i = 0; i < 4; ++i) { v[2 * i] = blo(h[i]); v[2 * i + 1] = bhi(h[i]); s += v[2 * i] + v[2 * i + 1]; }
        s = sum_x32(sum_x16(s));
        const float m = s * (1.f / 32);
#pragma unroll
        for (int i = 0; i < 8; ++i) q += (v[i] - m) * (v[i] - m);
        q = sum_x32(sum_x16(q));
        const u2 st = {fb(s), fb(q)};
        st2(st, rh, l < 16 ? l * 8 : OOB, (u32)seg * HSEG + TILEB + u * 128);
        waitv<0>();
        if (seg >= 56) STAMP(72 + (seg - 56) * 6 + 3);
        st1((u32)seg + 1, ctl, l < 36 ? OFF_MBC + ((((seg & 1) * 36 + l) * 2 + ((seg >> 1) & 1)) * 64 + u) * 64 : OOB, 0);
    }
}

DEV void role_de(u32 ids) {
    asm volatile(
    "s_nop 4\n"
    "s_load_dwordx16 s[40:55], %0, 0x0\n"
    "s_load_dwordx4 s[56:59], %0, 0x40\n"
    "s_load_dwordx2 s[60:61], %0, 0x50\n"
    "s_lshr_b32 s71, %1, 24\n"
    "s_mov_b32 s70, 0\n"
    "s_waitcnt lgkmcnt(0)\n"
    "s_load_dword s42, %0, 0x6c\n"
    "s_waitcnt lgkmcnt(0)\n"
    "s_add_u32 s20, s60, 0\n"
    "s_addc_u32 s21, s61, 0\n"
    "s_and_b32 s21, s21, 0xffff\n"
    "s_mov_b32 s22, 0x300000\n"
    "s_mov_b32 s23, 0x20000\n"
    "s_and_b32 s72, %1, 0xff\n"
    "s_bfe_u32 s73, %1, 0x40008\n"
    "v_mbcnt_lo_u32_b32 v140, -1, 0\n"
    "v_mbcnt_hi_u32_b32 v140, -1, v140\n"
    "s_lshr_b32 s66, s71, 1\n"
    "v_lshrrev_b32 v144, 3, v140\n"
    "v_and_b32 v145, 6, v144\n"
    "v_and_b32 v141, 7, v140\n"
    "v_xor_b32 v141, v141, v145\n"
    "s_mul_i32 s65, s66, 6\n"
    "v_xor_b32 v141, s65, v141\n"
    "v_lshlrev_b32 v141, 4, v141\n"
    "s_mov_b32 s65, 9216\n"
    "v_mad_u32_u24 v141, v144, s65, v141\n"
    "s_and_b32 s66, s71, 1\n"
    "v_lshrrev_b32 v144, 3, v140\n"
    "v_and_b32 v145, 6, v144\n"
    "v_and_b32 v142, 7, v140\n"
    "v_xor_b32 v142, v142, v145\n"
    "s_mul_i32 s65, s66, 6\n"
    "v_xor_b32 v142, s65, v142\n"
    "v_lshlrev_b32 v142, 4, v142\n"
    "s_mov_b32 s65, 1152\n"
    "v_mad_u32_u24 v142, v144, s65, v142\n"
    "s_lshl_b32 s78, s72, 8\n"
    "s_add_u32 s36, s52, 0\n"
    "s_addc_u32 s37, s53, 0\n"
    "s_and_b32 s37, s37, 0xffff\n"
    "s_mov_b32 s38, 0x510000\n"
    "s_mov_b32 s39, 0x20000\n"
    "s_lshr_b32 s64, s71, 1\n"
    "s_add_u32 s64, s64, 0\n"
    "s_mul_i32 s64, s64, 73728\n"
    "s_and_b32 s65, s71, 1\n"
    "s_lshl_b32 s65, s65, 7\n"
    "s_add_u32 s64, s64, s65\n"
    "s_add_u32 s64, s64, s78\n"
    "s_lshl_b32 s65, s71, 10\n"
    "s_add_u32 m0, s65, 0\n"
    "s_nop 0\n"
    "buffer_load_dwordx4 v141, s[36:39], s64 offen lds\n"
    "s_add_u32 s64, s64, 147456\n"
    "s_add_u32 m0, m0, 4096\n"
    "s_nop 0\n"
    "buffer_load_dwordx4 v141, s[36:39], s64 offen lds\n"
    "s_add_u32 s64, s64, 147456\n"
    "s_add_u32 m0, m0, 4096\n"
    "s_nop 0\n"
    "buffer_load_dwordx4 v141, s[36:39], s64 offen lds\n"
    "s_add_u32 s64, s64, 147456\n"
    "s_add_u32 m0, m0, 4096\n"
    "s_nop 0\n"
    "buffer_load_dwordx4 v141, s[36:39], s64 offen lds\n"
    "s_add_u32 s64, s64, 147456\n"
    "s_add_u32 m0, m0, 4096\n"
    "s_nop 0\n"
    "buffer_load_dwordx4 v141, s[36:39], s64 offen lds\n"
    "s_add_u32 s64, s64, 147456\n"
    "s_add_u32 m0, m0, 4096\n"
    "s_nop 0\n"
    "buffer_load_dwordx4 v141, s[36:39], s64 offen lds\n"
    "s_add_u32 s64, s64, 147456\n"
    "s_add_u32 m0, m0, 4096\n"
    "s_nop 0\n"
    "buffer_load_dwordx4 v141, s[36:39], s64 offen lds\n"
    "s_add_u32 s64, s64, 147456\n"
    "s_add_u32 m0, m0, 4096\n"
    "s_nop 0\n"
    "buffer_load_dwordx4 v141, s[36:39], s64 offen lds\n"
    "s_add_u32 s64, s64, 147456\n"
    "s_add_u32 m0, m0, 4096\n"
    "s_nop 0\n"
    "buffer_load_dwordx4 v141, s[36:39], s64 offen lds\n"
    "s_add_u32 s64, s64, 147456\n"
    "s_add_u32 m0, m0, 4096\n"
    "s_nop 0\n"
    "buffer_load_dwordx4 v141, s[36:39], s64 offen lds\n"
    "s_add_u32 s64, s64, 147456\n"
    "s_add_u32 m0, m0, 4096\n"
    "s_nop 0\n"
    "buffer_load_dwordx4 v141, s[36:39], s64 offen lds\n"
    "s_add_u32 s64, s64, 147456\n"
    "s_add_u32 m0, m0, 4096\n"
    "s_nop 0\n"
    "buffer_load_dwordx4 v141, s[36:39], s64 offen lds\n"
    "s_add_u32 s64, s64, 147456\n"
    "s_add_u32 m0, m0, 4096\n"
    "s_nop 0\n"
    "buffer_load_dwordx4 v141, s[36:39], s64 offen lds\n"
    "s_add_u32 s64, s64, 147456\n"
    "s_add_u32 m0, m0, 4096\n"
    "s_nop 0\n"
    "buffer_load_dwordx4 v141, s[36:39], s64 offen lds\n"
    "s_add_u32 s64, s64, 147456\n"
    "s_add_u32 m0, m0, 4096\n"
    "s_nop 0\n"
    "buffer_load_dwordx4 v141, s[36:39], s64 offen lds\n"
    "s_add_u32 s64, s64, 147456\n"
    "s_add_u32 m0, m0, 4096\n"
    "s_nop 0\n"
    "buffer_load_dwordx4 v141, s[36:39], s64 offen lds\n"
    "s_add_u32 s64, s64, 147456\n"
    "s_add_u32 m0, m0, 4096\n"
    "s_nop 0\n"
    "buffer_load_dwordx4 v141, s[36:39], s64 offen lds\n"
    "s_add_u32 s64, s64, 147456\n"
    "s_add_u32 m0, m0, 4096\n"
    "s_nop 0\n"
    "buffer_load_dwordx4 v141, s[36:39], s64 offen lds\n"
    "s_add_u32 s64, s64, 147456\n"
    "s_add_u32 m0, m0, 4096\n"
    "s_nop 0\n"
    "buffer_load_dwordx4 v141, s[36:39], s64 offen lds\n"
    "s_add_u32 s64, s64, 147456\n"
    "s_add_u32 m0, m0, 4096\n"
    "s_nop 0\n"
    "buffer_load_dwordx4 v141, s[36:39], s64 offen lds\n"
    "s_add_u32 s64, s64, 147456\n"
    "s_add_u32 m0, m0, 4096\n"
    "s_add_u32 s36, s52, 0\n"
    "s_addc_u32 s37, s53, 0\n"
    "s_and_b32 s37, s37, 0xffff\n"
    "s_mov_b32 s38, 0x510000\n"
    "s_mov_b32 s39, 0x20000\n"
    "s_lshr_b32 s64, s71, 1\n"
    "s_add_u32 s64, s64, 40\n"
    "s_mul_i32 s64, s64, 73728\n"
    "s_and_b32 s65, s71, 1\n"
    "s_lshl_b32 s65, s65, 7\n"
    "s_add_u32 s64, s64, s65\n"
    "s_add_u32 s64, s64, s78\n"
    "s_lshl_b32 s65, s71, 10\n"
    "s_add_u32 m0, s65, 81920\n"
    "s_nop 0\n"
    "buffer_load_dwordx4 v141, s[36:39], s64 offen lds\n"
    "s_add_u32 s64, s64, 147456\n"
    "s_add_u32 m0, m0, 4096\n"
    "s_nop 0\n"
    "buffer_load_dwordx4 v141, s[36:39], s64 offen lds\n"
    "s_add_u32 s64, s64, 147456\n"
    "s_add_u32 m0, m0, 4096\n"
    "s_nop 0\n"
    "buffer_load_dwordx4 v141, s[36:39], s64 offen lds\n"
    "s_add_u32 s64, s64, 147456\n"
    "s_add_u32 m0, m0, 4096\n"
    "s_nop 0\n"
    "buffer_load_dwordx4 v141, s[36:39], s64 offen lds\n"
    "s_add_u32 s64, s64, 147456\n"
    "s_add_u32 m0, m0, 4096\n"
    "s_nop 0\n"
    "buffer_load_dwordx4 v141, s[36:39], s64 offen lds\n"
    "s_add_u32 s64, s64, 147456\n"
    "s_add_u32 m0, m0, 4096\n"
    "s_nop 0\n"
    "buffer_load_dwordx4 v141, s[36:39], s64 offen lds\n"
    "s_add_u32 s64, s64, 147456\n"
    "s_add_u32 m0, m0, 4096\n"
    "s_nop 0\n"
    "buffer_load_dwordx4 v141, s[36:39], s64 offen lds\n"
    "s_add_u32 s64, s64, 147456\n"
    "s_add_u32 m0, m0, 4096\n"
    "s_nop 0\n"
    "buffer_load_dwordx4 v141, s[36:39], s64 offen lds\n"
    "s_add_u32 s64, s64, 147456\n"
    "s_add_u32 m0, m0, 4096\n"
    "s_nop 0\n"
    "buffer_load_dwordx4 v141, s[36:39], s64 offen lds\n"
    "s_add_u32 s64, s64, 147456\n"
    "s_add_u32 m0, m0, 4096\n"
    "s_nop 0\n"
    "buffer_load_dwordx4 v141, s[36:39], s64 offen lds\n"
    "s_add_u32 s64, s64, 147456\n"
    "s_add_u32 m0, m0, 4096\n"
    "s_nop 0\n"
    "buffer_load_dwordx4 v141, s[36:39], s64 offen lds\n"
    "s_add_u32 s64, s64, 147456\n"
    "s_add_u32 m0, m0, 4096\n"
    "s_nop 0\n"
    "buffer_load_dwordx4 v141, s[36:39], s64 offen lds\n"
    "s_add_u32 s64, s64, 147456\n"
    "s_add_u32 m0, m0, 4096\n"
    "s_nop 0\n"
    "buffer_load_dwordx4 v141, s[36:39], s64 offen lds\n"
    "s_add_u32 s64, s64, 147456\n"
    "s_add_u32 m0, m0, 4096\n"
    "s_nop 0\n"
    "buffer_load_dwordx4 v141, s[36:39], s64 offen lds\n"
    "s_add_u32 s64, s64, 147456\n"
    "s_add_u32 m0, m0, 4096\n"
    "s_nop 0\n"
    "buffer_load_dwordx4 v141, s[36:39], s64 offen lds\n"
    "s_add_u32 s64, s64, 147456\n"
    "s_add_u32 m0, m0, 4096\n"
    "s_nop 0\n"
    "buffer_load_dwordx4 v141, s[36:39], s64 offen lds\n"
    "s_add_u32 s64, s64, 147456\n"
    "s_add_u32 m0, m0, 4096\n"
    "s_add_u32 s24, s60, 0x8d40000\n"
    "s_addc_u32 s25, s61, 0\n"
    "s_and_b32 s25, s25, 0xffff\n"
    "s_mov_b32 s26, 0x288000\n"
    "s_mov_b32 s27, 0x20000\n"
    "s_add_u32 s28, s60, 0x8fc8000\n"
    "s_addc_u32 s29, s61, 0\n"
    "s_and_b32 s29, s29, 0xffff\n"
    "s_mov_b32 s30, 0x5100000\n"
    "s_mov_b32 s31, 0x20000\n"
    "v_bfe_u32 v145, v140, 3, 1\n"
    "v_bfe_u32 v146, v140, 4, 1\n"
    "v_and_b32 v150, 3, v140\n"
    "v_lshlrev_b32 v150, 3, v150\n"
    "v_bfe_u32 v144, v140, 2, 2\n"
    "v_lshl_add_u32 v150, v144, 7, v150\n"
    "s_mov_b32 s64, 2048\n"
    "v_lshrrev_b32 v144, 5, v140\n"
    "v_xor_b32 v145, v145, v144\n"
    "v_lshlrev_b32 v145, 5, v145\n"
    "v_xor_b32 v149, v146, v144\n"
    "v_lshl_or_b32 v148, v149, 6, v145\n"
    "v_mov_b32 v149, v148\n"
    "v_mad_u32_u24 v150, v144, s64, v150\n"
    "v_lshl_add_u32 v150, v146, 9, v150\n"
    "v_add_u32 v151, 4096, v150\n"
    "s_lshl_b32 s64, s71, 1\n"
    "s_add_u32 s64, s64, 0\n"
    "s_and_b32 s65, s64, 3\n"
    "s_lshl_b32 s65, s65, 5\n"
    "v_xor_b32 v152, s65, v148\n"
    "s_lshr_b32 s65, s64, 2\n"
    "s_lshl_b32 s65, s65, 10\n"
    "s_add_u32 s65, s65, 0\n"
    "v_add3_u32 v152, v152, v150, s65\n"
    "s_and_b32 s65, s64, 3\n"
    "s_lshl_b32 s65, s65, 5\n"
    "v_xor_b32 v153, s65, v149\n"
    "s_lshr_b32 s65, s64, 2\n"
    "s_lshl_b32 s65, s65, 10\n"
    "s_add_u32 s65, s65, 0\n"
    "v_add3_u32 v153, v153, v151, s65\n"
    "s_lshl_b32 s64, s71, 1\n"
    "s_add_u32 s64, s64, 1\n"
    "s_and_b32 s65, s64, 3\n"
    "s_lshl_b32 s65, s65, 5\n"
    "v_xor_b32 v154, s65, v148\n"
    "s_lshr_b32 s65, s64, 2\n"
    "s_lshl_b32 s65, s65, 10\n"
    "s_add_u32 s65, s65, 0\n"
    "v_add3_u32 v154, v154, v150, s65\n"
    "s_and_b32 s65, s64, 3\n"
    "s_lshl_b32 s65, s65, 5\n"
    "v_xor_b32 v155, s65, v149\n"
    "s_lshr_b32 s65, s64, 2\n"
    "s_lshl_b32 s65, s65, 10\n"
    "s_add_u32 s65, s65, 0\n"
    "v_add3_u32 v155, v155, v151, s65\n"
    "s_mul_i32 s75, s72, 147456\n"
    "s_waitcnt vmcnt(16)\n"
    "s_waitcnt lgkmcnt(0)\n"
    "s_barrier\n"
    "ds_read_b64_tr_b16 a[0:1], v152\n"
    "ds_read_b64_tr_b16 a[2:3], v153\n"
    "ds_read_b64_tr_b16 a[72:73], v154\n"
    "ds_read_b64_tr_b16 a[74:75], v155\n"
    "ds_read_b64_tr_b16 a[4:5], v152 offset:8192\n"
    "ds_read_b64_tr_b16 a[6:7], v153 offset:8192\n"
    "ds_read_b64_tr_b16 a[76:77], v154 offset:8192\n"
    "ds_read_b64_tr_b16 a[78:79], v155 offset:8192\n"
    "ds_read_b64_tr_b16 a[8:9], v152 offset:16384\n"
    "ds_read_b64_tr_b16 a[10:11], v153 offset:16384\n"
    "ds_read_b64_tr_b16 a[80:81], v154 offset:16384\n"
    "ds_read_b64_tr_b16 a[82:83], v155 offset:16384\n"
    "ds_read_b64_tr_b16 a[12:13], v152 offset:24576\n"
    "ds_read_b64_tr_b16 a[14:15], v153 offset:24576\n"
    "ds_read_b64_tr_b16 a[84:85], v154 offset:24576\n"
    "ds_read_b64_tr_b16 a[86:87], v155 offset:24576\n"
    "ds_read_b64_tr_b16 a[16:17], v152 offset:32768\n"
    "ds_read_b64_tr_b16 a[18:19], v153 offset:32768\n"
    "ds_read_b64_tr_b16 a[88:89], v154 offset:32768\n"
    "ds_read_b64_tr_b16 a[90:91], v155 offset:32768\n"
    "ds_read_b64_tr_b16 a[20:21], v152 offset:40960\n"
    "ds_read_b64_tr_b16 a[22:23], v153 offset:40960\n"
    "ds_read_b64_tr_b16 a[92:93], v154 offset:40960\n"
    "ds_read_b64_tr_b16 a[94:95], v155 offset:40960\n"
    "ds_read_b64_tr_b16 a[24:25], v152 offset:49152\n"
    "ds_read_b64_tr_b16 a[26:27], v153 offset:49152\n"
    "ds_read_b64_tr_b16 a[96:97], v154 offset:49152\n"
    "ds_read_b64_tr_b16 a[98:99], v155 offset:49152\n"
    "ds_read_b64_tr_b16 a[28:29], v152 offset:57344\n"
    "ds_read_b64_tr_b16 a[30:31], v153 offset:57344\n"
    "ds_read_b64_tr_b16 a[100:101], v154 offset:57344\n"
    "ds_read_b64_tr_b16 a[102:103], v155 offset:57344\n"
    "v_add_u32 v152, 0x10000, v152\n"
    "v_add_u32 v153, 0x10000, v153\n"
    "v_add_u32 v154, 0x10000, v154\n"
    "v_add_u32 v155, 0x10000, v155\n"
    "ds_read_b64_tr_b16 a[32:33], v152\n"
    "ds_read_b64_tr_b16 a[34:35], v153\n"
    "ds_read_b64_tr_b16 a[104:105], v154\n"
    "ds_read_b64_tr_b16 a[106:107], v155\n"
    "ds_read_b64_tr_b16 a[36:37], v152 offset:8192\n"
    "ds_read_b64_tr_b16 a[38:39], v153 offset:8192\n"
    "ds_read_b64_tr_b16 a[108:109], v154 offset:8192\n"
    "ds_read_b64_tr_b16 a[110:111], v155 offset:8192\n"
    "s_waitcnt vmcnt(0)\n"
    "s_waitcnt lgkmcnt(0)\n"
    "s_barrier\n"
    "s_add_u32 s36, s54, 0\n"
    "s_addc_u32 s37, s55, 0\n"
    "s_and_b32 s37, s37, 0xffff\n"
    "s_mov_b32 s38, 0x510000\n"
    "s_mov_b32 s39, 0x20000\n"
    "s_lshr_b32 s64, s71, 1\n"
    "s_add_u32 s64, s64, 0\n"
    "s_mul_i32 s64, s64, 73728\n"
    "s_and_b32 s65, s71, 1\n"
    "s_lshl_b32 s65, s65, 7\n"
    "s_add_u32 s64, s64, s65\n"
    "s_add_u32 s64, s64, s78\n"
    "s_lshl_b32 s65, s71, 10\n"
    "s_add_u32 m0, s65, 0\n"
    "s_nop 0\n"
    "buffer_load_dwordx4 v141, s[36:39], s64 offen lds\n"
    "s_add_u32 s64, s64, 147456\n"
    "s_add_u32 m0, m0, 4096\n"
    "s_nop 0\n"
    "buffer_load_dwordx4 v141, s[36:39], s64 offen lds\n"
    "s_add_u32 s64, s64, 147456\n"
    "s_add_u32 m0, m0, 4096\n"
    "s_nop 0\n"
    "buffer_load_dwordx4 v141, s[36:39], s64 offen lds\n"
    "s_add_u32 s64, s64, 147456\n"
    "s_add_u32 m0, m0, 4096\n"
    "s_nop 0\n"
    "buffer_load_dwordx4 v141, s[36:39], s64 offen lds\n"
    "s_add_u32 s64, s64, 147456\n"
    "s_add_u32 m0, m0, 4096\n"
    "s_nop 0\n"
    "buffer_load_dwordx4 v141, s[36:39], s64 offen lds\n"
    "s_add_u32 s64, s64, 147456\n"
    "s_add_u32 m0, m0, 4096\n"
    "s_nop 0\n"
    "buffer_load_dwordx4 v141, s[36:39], s64 offen lds\n"
    "s_add_u32 s64, s64, 147456\n"
    "s_add_u32 m0, m0, 4096\n"
    "s_nop 0\n"
    "buffer_load_dwordx4 v141, s[36:39], s64 offen lds\n"
    "s_add_u32 s64, s64, 147456\n"
    "s_add_u32 m0, m0, 4096\n"
    "s_nop 0\n"
    "buffer_load_dwordx4 v141, s[36:39], s64 offen lds\n"
    "s_add_u32 s64, s64, 147456\n"
    "s_add_u32 m0, m0, 4096\n"
    "s_nop 0\n"
    "buffer_load_dwordx4 v141, s[36:39], s64 offen lds\n"
    "s_add_u32 s64, s64, 147456\n"
    "s_add_u32 m0, m0, 4096\n"
    "s_nop 0\n"
    "buffer_load_dwordx4 v141, s[36:39], s64 offen lds\n"
    "s_add_u32 s64, s64, 147456\n"
    "s_add_u32 m0, m0, 4096\n"
    "s_nop 0\n"
    "buffer_load_dwordx4 v141, s[36:39], s64 offen lds\n"
    "s_add_u32 s64, s64, 147456\n"
    "s_add_u32 m0, m0, 4096\n"
    "s_nop 0\n"
    "buffer_load_dwordx4 v141, s[36:39], s64 offen lds\n"
    "s_add_u32 s64, s64, 147456\n"
    "s_add_u32 m0, m0, 4096\n"
    "s_nop 0\n"
    "buffer_load_dwordx4 v141, s[36:39], s64 offen lds\n"
    "s_add_u32 s64, s64, 147456\n"
    "s_add_u32 m0, m0, 4096\n"
    "s_nop 0\n"
    "buffer_load_dwordx4 v141, s[36:39], s64 offen lds\n"
    "s_add_u32 s64, s64, 147456\n"
    "s_add_u32 m0, m0, 4096\n"
    "s_nop 0\n"
    "buffer_load_dwordx4 v141, s[36:39], s64 offen lds\n"
    "s_add_u32 s64, s64, 147456\n"
    "s_add_u32 m0, m0, 4096\n"
    "s_nop 0\n"
    "buffer_load_dwordx4 v141, s[36:39], s64 offen lds\n"
    "s_add_u32 s64, s64, 147456\n"
    "s_add_u32 m0, m0, 4096\n"
    "s_nop 0\n"
    "buffer_load_dwordx4 v141, s[36:39], s64 offen lds\n"
    "s_add_u32 s64, s64, 147456\n"
    "s_add_u32 m0, m0, 4096\n"
    "s_nop 0\n"
    "buffer_load_dwordx4 v141, s[36:39], s64 offen lds\n"
    "s_add_u32 s64, s64, 147456\n"
    "s_add_u32 m0, m0, 4096\n"
    "s_nop 0\n"
    "buffer_load_dwordx4 v141, s[36:39], s64 offen lds\n"
    "s_add_u32 s64, s64, 147456\n"
    "s_add_u32 m0, m0, 4096\n"
    "s_nop 0\n"
    "buffer_load_dwordx4 v141, s[36:39], s64 offen lds\n"
    "s_add_u32 s64, s64, 147456\n"
    "s_add_u32 m0, m0, 4096\n"
    "ds_read_b64_tr_b16 a[40:41], v152 offset:16384\n"
    "ds_read_b64_tr_b16 a[42:43], v153 offset:16384\n"
    "ds_read_b64_tr_b16 a[112:113], v154 offset:16384\n"
    "ds_read_b64_tr_b16 a[114:115], v155 offset:16384\n"
    "ds_read_b64_tr_b16 a[44:45], v152 offset:24576\n"
    "ds_read_b64_tr_b16 a[46:47], v153 offset:24576\n"
    "ds_read_b64_tr_b16 a[116:117], v154 offset:24576\n"
    "ds_read_b64_tr_b16 a[118:119], v155 offset:24576\n"
    "ds_read_b64_tr_b16 a[48:49], v152 offset:32768\n"
    "ds_read_b64_tr_b16 a[50:51], v153 offset:32768\n"
    "ds_read_b64_tr_b16 a[120:121], v154 offset:32768\n"
    "ds_read_b64_tr_b16 a[122:123], v155 offset:32768\n"
    "ds_read_b64_tr_b16 a[52:53], v152 offset:40960\n"
    "ds_read_b64_tr_b16 a[54:55], v153 offset:40960\n"
    "ds_read_b64_tr_b16 a[124:125], v154 offset:40960\n"
    "ds_read_b64_tr_b16 a[126:127], v155 offset:40960\n"
    "ds_read_b64_tr_b16 a[56:57], v152 offset:49152\n"
    "ds_read_b64_tr_b16 a[58:59], v153 offset:49152\n"
    "ds_read_b64_tr_b16 a[128:129], v154 offset:49152\n"
    "ds_read_b64_tr_b16 a[130:131], v155 offset:49152\n"
    "ds_read_b64_tr_b16 a[60:61], v152 offset:57344\n"
    "ds_read_b64_tr_b16 a[62:63], v153 offset:57344\n"
    "ds_read_b64_tr_b16 a[132:133], v154 offset:57344\n"
    "ds_read_b64_tr_b16 a[134:135], v155 offset:57344\n"
    "v_add_u32 v152, 0x10000, v152\n"
    "v_add_u32 v153, 0x10000, v153\n"
    "v_add_u32 v154, 0x10000, v154\n"
    "v_add_u32 v155, 0x10000, v155\n"
    "ds_read_b64_tr_b16 a[64:65], v152\n"
    "ds_read_b64_tr_b16 a[66:67], v153\n"
    "ds_read_b64_tr_b16 a[136:137], v154\n"
    "ds_read_b64_tr_b16 a[138:139], v155\n"
    "ds_read_b64_tr_b16 a[68:69], v152 offset:8192\n"
    "ds_read_b64_tr_b16 a[70:71], v153 offset:8192\n"
    "ds_read_b64_tr_b16 a[140:141], v154 offset:8192\n"
    "ds_read_b64_tr_b16 a[142:143], v155 offset:8192\n"
    "s_waitcnt lgkmcnt(0)\n"
    "s_barrier\n"
    "s_add_u32 s36, s54, 0\n"
    "s_addc_u32 s37, s55, 0\n"
    "s_and_b32 s37, s37, 0xffff\n"
    "s_mov_b32 s38, 0x510000\n"
    "s_mov_b32 s39, 0x20000\n"
    "s_lshr_b32 s64, s71, 1\n"
    "s_add_u32 s64, s64, 40\n"
    "s_mul_i32 s64, s64, 73728\n"
    "s_and_b32 s65, s71, 1\n"
    "s_lshl_b32 s65, s65, 7\n"
    "s_add_u32 s64, s64, s65\n"
    "s_add_u32 s64, s64, s78\n"
    "s_lshl_b32 s65, s71, 10\n"
    "s_add_u32 m0, s65, 81920\n"
    "s_nop 0\n"
    "buffer_load_dwordx4 v141, s[36:39], s64 offen lds\n"
    "s_add_u32 s64, s64, 147456\n"
    "s_add_u32 m0, m0, 4096\n"
    "s_nop 0\n"
    "buffer_load_dwordx4 v141, s[36:39], s64 offen lds\n"
    "s_add_u32 s64, s64, 147456\n"
    "s_add_u32 m0, m0, 4096\n"
    "s_nop 0\n"
    "buffer_load_dwordx4 v141, s[36:39], s64 offen lds\n"
    "s_add_u32 s64, s64, 147456\n"
    "s_add_u32 m0, m0, 4096\n"
    "s_nop 0\n"
    "buffer_load_dwordx4 v141, s[36:39], s64 offen lds\n"
    "s_add_u32 s64, s64, 147456\n"
    "s_add_u32 m0, m0, 4096\n"
    "s_nop 0\n"
    "buffer_load_dwordx4 v141, s[36:39], s64 offen lds\n"
    "s_add_u32 s64, s64, 147456\n"
    "s_add_u32 m0, m0, 4096\n"
    "s_nop 0\n"
    "buffer_load_dwordx4 v141, s[36:39], s64 offen lds\n"
    "s_add_u32 s64, s64, 147456\n"
    "s_add_u32 m0, m0, 4096\n"
    "s_nop 0\n"
    "buffer_load_dwordx4 v141, s[36:39], s64 offen lds\n"
    "s_add_u32 s64, s64, 147456\n"
    "s_add_u32 m0, m0, 4096\n"
    "s_nop 0\n"
    "buffer_load_dwordx4 v141, s[36:39], s64 offen lds\n"
    "s_add_u32 s64, s64, 147456\n"
    "s_add_u32 m0, m0, 4096\n"
    "s_nop 0\n"
    "buffer_load_dwordx4 v141, s[36:39], s64 offen lds\n"
    "s_add_u32 s64, s64, 147456\n"
    "s_add_u32 m0, m0, 4096\n"
    "s_nop 0\n"
    "buffer_load_dwordx4 v141, s[36:39], s64 offen lds\n"
    "s_add_u32 s64, s64, 147456\n"
    "s_add_u32 m0, m0, 4096\n"
    "s_nop 0\n"
    "buffer_load_dwordx4 v141, s[36:39], s64 offen lds\n"
    "s_add_u32 s64, s64, 147456\n"
    "s_add_u32 m0, m0, 4096\n"
    "s_nop 0\n"
    "buffer_load_dwordx4 v141, s[36:39], s64 offen lds\n"
    "s_add_u32 s64, s64, 147456\n"
    "s_add_u32 m0, m0, 4096\n"
    "s_nop 0\n"
    "buffer_load_dwordx4 v141, s[36:39], s64 offen lds\n"
    "s_add_u32 s64, s64, 147456\n"
    "s_add_u32 m0, m0, 4096\n"
    "s_nop 0\n"
    "buffer_load_dwordx4 v141, s[36:39], s64 offen lds\n"
    "s_add_u32 s64, s64, 147456\n"
    "s_add_u32 m0, m0, 4096\n"
    "s_nop 0\n"
    "buffer_load_dwordx4 v141, s[36:39], s64 offen lds\n"
    "s_add_u32 s64, s64, 147456\n"
    "s_add_u32 m0, m0, 4096\n"
    "s_nop 0\n"
    "buffer_load_dwordx4 v141, s[36:39], s64 offen lds\n"
    "s_add_u32 s64, s64, 147456\n"
    "s_add_u32 m0, m0, 4096\n"
    "s_waitcnt vmcnt(16)\n"
    "s_waitcnt lgkmcnt(0)\n"
    "s_barrier\n"
    "v_add_u32 v152, 0xfffe0000, v152\n"
    "v_add_u32 v153, 0xfffe0000, v153\n"
    "v_add_u32 v154, 0xfffe0000, v154\n"
    "v_add_u32 v155, 0xfffe0000, v155\n"
    "ds_read_b64_tr_b16 a[144:145], v152\n"
    "ds_read_b64_tr_b16 a[146:147], v153\n"
    "ds_read_b64_tr_b16 a[216:217], v154\n"
    "ds_read_b64_tr_b16 a[218:219], v155\n"
    "ds_read_b64_tr_b16 a[148:149], v152 offset:8192\n"
    "ds_read_b64_tr_b16 a[150:151], v153 offset:8192\n"
    "ds_read_b64_tr_b16 a[220:221], v154 offset:8192\n"
    "ds_read_b64_tr_b16 a[222:223], v155 offset:8192\n"
    "ds_read_b64_tr_b16 a[152:153], v152 offset:16384\n"
    "ds_read_b64_tr_b16 a[154:155], v153 offset:16384\n"
    "ds_read_b64_tr_b16 a[224:225], v154 offset:16384\n"
    "ds_read_b64_tr_b16 a[226:227], v155 offset:16384\n"
    "ds_read_b64_tr_b16 a[156:157], v152 offset:24576\n"
    "ds_read_b64_tr_b16 a[158:159], v153 offset:24576\n"
    "ds_read_b64_tr_b16 a[228:229], v154 offset:24576\n"
    "ds_read_b64_tr_b16 a[230:231], v155 offset:24576\n"
    "ds_read_b64_tr_b16 a[160:161], v152 offset:32768\n"
    "ds_read_b64_tr_b16 a[162:163], v153 offset:32768\n"
    "ds_read_b64_tr_b16 a[232:233], v154 offset:32768\n"
    "ds_read_b64_tr_b16 a[234:235], v155 offset:32768\n"
    "ds_read_b64_tr_b16 a[164:165], v152 offset:40960\n"
    "ds_read_b64_tr_b16 a[166:167], v153 offset:40960\n"
    "ds_read_b64_tr_b16 a[236:237], v154 offset:40960\n"
    "ds_read_b64_tr_b16 a[238:239], v155 offset:40960\n"
    "ds_read_b64_tr_b16 a[168:169], v152 offset:49152\n"
    "ds_read_b64_tr_b16 a[170:171], v153 offset:49152\n"
    "ds_read_b64_tr_b16 a[240:241], v154 offset:49152\n"
    "ds_read_b64_tr_b16 a[242:243], v155 offset:49152\n"
    "ds_read_b64_tr_b16 a[172:173], v152 offset:57344\n"
    "ds_read_b64_tr_b16 a[174:175], v153 offset:57344\n"
    "ds_read_b64_tr_b16 a[244:245], v154 offset:57344\n"
    "ds_read_b64_tr_b16 a[246:247], v155 offset:57344\n"
    "v_add_u32 v152, 0x10000, v152\n"
    "v_add_u32 v153, 0x10000, v153\n"
    "v_add_u32 v154, 0x10000, v154\n"
    "v_add_u32 v155, 0x10000, v155\n"
    "ds_read_b64_tr_b16 a[176:177], v152\n"
    "ds_read_b64_tr_b16 a[178:179], v153\n"
    "ds_read_b64_tr_b16 a[248:249], v154\n"
    "ds_read_b64_tr_b16 a[250:251], v155\n"
    "ds_read_b64_tr_b16 a[180:181], v152 offset:8192\n"
    "ds_read_b64_tr_b16 a[182:183], v153 offset:8192\n"
    "ds_read_b64_tr_b16 a[252:253], v154 offset:8192\n"
    "ds_read_b64_tr_b16 a[254:255], v155 offset:8192\n"
    "s_waitcnt vmcnt(0)\n"
    "s_waitcnt lgkmcnt(0)\n"
    "s_barrier\n"
    "s_add_u32 s36, s56, 0\n"
    "s_addc_u32 s37, s57, 0\n"
    "s_and_b32 s37, s37, 0xffff\n"
    "s_mov_b32 s38, 0x510000\n"
    "s_mov_b32 s39, 0x20000\n"
    "s_add_u32 s64, s71, 0\n"
    "s_mul_i32 s64, s64, 9216\n"
    "s_add_u32 s64, s64, s75\n"
    "s_mul_i32 s65, s71, 9216\n"
    "s_add_u32 m0, s65, 0\n"
    "s_nop 0\n"
    "buffer_load_dwordx4 v142, s[36:39], s64 offen lds\n"
    "s_add_u32 s64, s64, 128\n"
    "s_add_u32 m0, m0, 1024\n"
    "s_nop 0\n"
    "buffer_load_dwordx4 v142, s[36:39], s64 offen lds\n"
    "s_add_u32 s64, s64, 128\n"
    "s_add_u32 m0, m0, 1024\n"
    "s_nop 0\n"
    "buffer_load_dwordx4 v142, s[36:39], s64 offen lds\n"
    "s_add_u32 s64, s64, 128\n"
    "s_add_u32 m0, m0, 1024\n"
    "s_nop 0\n"
    "buffer_load_dwordx4 v142, s[36:39], s64 offen lds\n"
    "s_add_u32 s64, s64, 128\n"
    "s_add_u32 m0, m0, 1024\n"
    "s_nop 0\n"
    "buffer_load_dwordx4 v142, s[36:39], s64 offen lds\n"
    "s_add_u32 s64, s64, 128\n"
    "s_add_u32 m0, m0, 1024\n"
    "s_nop 0\n"
    "buffer_load_dwordx4 v142, s[36:39], s64 offen lds\n"
    "s_add_u32 s64, s64, 128\n"
    "s_add_u32 m0, m0, 1024\n"
    "s_nop 0\n"
    "buffer_load_dwordx4 v142, s[36:39], s64 offen lds\n"
    "s_add_u32 s64, s64, 128\n"
    "s_add_u32 m0, m0, 1024\n"
    "s_nop 0\n"
    "buffer_load_dwordx4 v142, s[36:39], s64 offen lds\n"
    "s_add_u32 s64, s64, 128\n"
    "s_add_u32 m0, m0, 1024\n"
    "s_nop 0\n"
    "buffer_load_dwordx4 v142, s[36:39], s64 offen lds\n"
    "s_add_u32 s64, s64, 35840\n"
    "s_add_u32 m0, m0, 28672\n"
    "s_nop 0\n"
    "buffer_load_dwordx4 v142, s[36:39], s64 offen lds\n"
    "s_add_u32 s64, s64, 128\n"
    "s_add_u32 m0, m0, 1024\n"
    "s_nop 0\n"
    "buffer_load_dwordx4 v142, s[36:39], s64 offen lds\n"
    "s_add_u32 s64, s64, 128\n"
    "s_add_u32 m0, m0, 1024\n"
    "s_nop 0\n"
    "buffer_load_dwordx4 v142, s[36:39], s64 offen lds\n"
    "s_add_u32 s64, s64, 128\n"
    "s_add_u32 m0, m0, 1024\n"
    "s_nop 0\n"
    "buffer_load_dwordx4 v142, s[36:39], s64 offen lds\n"
    "s_add_u32 s64, s64, 128\n"
    "s_add_u32 m0, m0, 1024\n"
    "s_nop 0\n"
    "buffer_load_dwordx4 v142, s[36:39], s64 offen lds\n"
    "s_add_u32 s64, s64, 128\n"
    "s_add_u32 m0, m0, 1024\n"
    "s_nop 0\n"
    "buffer_load_dwordx4 v142, s[36:39], s64 offen lds\n"
    "s_add_u32 s64, s64, 128\n"
    "s_add_u32 m0, m0, 1024\n"
    "s_nop 0\n"
    "buffer_load_dwordx4 v142, s[36:39], s64 offen lds\n"
    "s_add_u32 s64, s64, 128\n"
    "s_add_u32 m0, m0, 1024\n"
    "s_nop 0\n"
    "buffer_load_dwordx4 v142, s[36:39], s64 offen lds\n"
    "s_add_u32 s64, s64, 128\n"
    "s_add_u32 m0, m0, 1024\n"
    "s_nop 0\n"
    "buffer_load_dwordx4 v142, s[36:39], s64 offen lds\n"
    "s_add_u32 s64, s64, 35840\n"
    "s_add_u32 m0, m0, 28672\n"
    "ds_read_b64_tr_b16 a[184:185], v152 offset:16384\n"
    "ds_read_b64_tr_b16 a[186:187], v153 offset:16384\n"
    "ds_read_b64_tr_b16 v[160:161], v154 offset:16384\n"
    "ds_read_b64_tr_b16 v[162:163], v155 offset:16384\n"
    "ds_read_b64_tr_b16 a[188:189], v152 offset:24576\n"
    "ds_read_b64_tr_b16 a[190:191], v153 offset:24576\n"
    "ds_read_b64_tr_b16 v[164:165], v154 offset:24576\n"
    "ds_read_b64_tr_b16 v[166:167], v155 offset:24576\n"
    "ds_read_b64_tr_b16 a[192:193], v152 offset:32768\n"
    "ds_read_b64_tr_b16 a[194:195], v153 offset:32768\n"
    "ds_read_b64_tr_b16 v[168:169], v154 offset:32768\n"
    "ds_read_b64_tr_b16 v[170:171], v155 offset:32768\n"
    "ds_read_b64_tr_b16 a[196:197], v152 offset:40960\n"
    "ds_read_b64_tr_b16 a[198:199], v153 offset:40960\n"
    "ds_read_b64_tr_b16 v[172:173], v154 offset:40960\n"
    "ds_read_b64_tr_b16 v[174:175], v155 offset:40960\n"
    "ds_read_b64_tr_b16 a[200:201], v152 offset:49152\n"
    "ds_read_b64_tr_b16 a[202:203], v153 offset:49152\n"
    "ds_read_b64_tr_b16 v[176:177], v154 offset:49152\n"
    "ds_read_b64_tr_b16 v[178:179], v155 offset:49152\n"
    "ds_read_b64_tr_b16 a[204:205], v152 offset:57344\n"
    "ds_read_b64_tr_b16 a[206:207], v153 offset:57344\n"
    "ds_read_b64_tr_b16 v[180:181], v154 offset:57344\n"
    "ds_read_b64_tr_b16 v[182:183], v155 offset:57344\n"
    "v_add_u32 v152, 0x10000, v152\n"
    "v_add_u32 v153, 0x10000, v153\n"
    "v_add_u32 v154, 0x10000, v154\n"
    "v_add_u32 v155, 0x10000, v155\n"
    "ds_read_b64_tr_b16 a[208:209], v152\n"
    "ds_read_b64_tr_b16 a[210:211], v153\n"
    "ds_read_b64_tr_b16 v[184:185], v154\n"
    "ds_read_b64_tr_b16 v[186:187], v155\n"
    "ds_read_b64_tr_b16 a[212:213], v152 offset:8192\n"
    "ds_read_b64_tr_b16 a[214:215], v153 offset:8192\n"
    "ds_read_b64_tr_b16 v[188:189], v154 offset:8192\n"
    "ds_read_b64_tr_b16 v[190:191], v155 offset:8192\n"
    "s_waitcnt lgkmcnt(0)\n"
    "s_barrier\n"
    "s_add_u32 s36, s56, 0\n"
    "s_addc_u32 s37, s57, 0\n"
    "s_and_b32 s37, s37, 0xffff\n"
    "s_mov_b32 s38, 0x510000\n"
    "s_mov_b32 s39, 0x20000\n"
    "s_add_u32 s64, s71, 8\n"
    "s_mul_i32 s64, s64, 9216\n"
    "s_add_u32 s64, s64, s75\n"
    "s_mul_i32 s65, s71, 9216\n"
    "s_add_u32 m0, s65, 81920\n"
    "s_nop 0\n"
    "buffer_load_dwordx4 v142, s[36:39], s64 offen lds\n"
    "s_add_u32 s64, s64, 128\n"
    "s_add_u32 m0, m0, 1024\n"
    "s_nop 0\n"
    "buffer_load_dwordx4 v142, s[36:39], s64 offen lds\n"
    "s_add_u32 s64, s64, 128\n"
    "s_add_u32 m0, m0, 1024\n"
    "s_nop 0\n"
    "buffer_load_dwordx4 v142, s[36:39], s64 offen lds\n"
    "s_add_u32 s64, s64, 128\n"
    "s_add_u32 m0, m0, 1024\n"
    "s_nop 0\n"
    "buffer_load_dwordx4 v142, s[36:39], s64 offen lds\n"
    "s_add_u32 s64, s64, 128\n"
    "s_add_u32 m0, m0, 1024\n"
    "s_nop 0\n"
    "buffer_load_dwordx4 v142, s[36:39], s64 offen lds\n"
    "s_add_u32 s64, s64, 128\n"
    "s_add_u32 m0, m0, 1024\n"
    "s_nop 0\n"
    "buffer_load_dwordx4 v142, s[36:39], s64 offen lds\n"
    "s_add_u32 s64, s64, 128\n"
    "s_add_u32 m0, m0, 1024\n"
    "s_nop 0\n"
    "buffer_load_dwordx4 v142, s[36:39], s64 offen lds\n"
    "s_add_u32 s64, s64, 128\n"
    "s_add_u32 m0, m0, 1024\n"
    "s_nop 0\n"
    "buffer_load_dwordx4 v142, s[36:39], s64 offen lds\n"
    "s_add_u32 s64, s64, 128\n"
    "s_add_u32 m0, m0, 1024\n"
    "s_nop 0\n"
    "buffer_load_dwordx4 v142, s[36:39], s64 offen lds\n"
    "s_add_u32 s64, s64, 35840\n"
    "s_add_u32 m0, m0, 28672\n"
    "s_nop 0\n"
    "buffer_load_dwordx4 v142, s[36:39], s64 offen lds\n"
    "s_add_u32 s64, s64, 128\n"
    "s_add_u32 m0, m0, 1024\n"
    "s_nop 0\n"
    "buffer_load_dwordx4 v142, s[36:39], s64 offen lds\n"
    "s_add_u32 s64, s64, 128\n"
    "s_add_u32 m0, m0, 1024\n"
    "s_nop 0\n"
    "buffer_load_dwordx4 v142, s[36:39], s64 offen lds\n"
    "s_add_u32 s64, s64, 128\n"
    "s_add_u32 m0, m0, 1024\n"
    "s_nop 0\n"
    "buffer_load_dwordx4 v142, s[36:39], s64 offen lds\n"
    "s_add_u32 s64, s64, 128\n"
    "s_add_u32 m0, m0, 1024\n"
    "s_nop 0\n"
    "buffer_load_dwordx4 v142, s[36:39], s64 offen lds\n"
    "s_add_u32 s64, s64, 128\n"
    "s_add_u32 m0, m0, 1024\n"
    "s_nop 0\n"
    "buffer_load_dwordx4 v142, s[36:39], s64 offen lds\n"
    "s_add_u32 s64, s64, 128\n"
    "s_add_u32 m0, m0, 1024\n"
    "s_nop 0\n"
    "buffer_load_dwordx4 v142, s[36:39], s64 offen lds\n"
    "s_add_u32 s64, s64, 128\n"
    "s_add_u32 m0, m0, 1024\n"
    "s_nop 0\n"
    "buffer_load_dwordx4 v142, s[36:39], s64 offen lds\n"
    "s_add_u32 s64, s64, 128\n"
    "s_add_u32 m0, m0, 1024\n"
    "s_nop 0\n"
    "buffer_load_dwordx4 v142, s[36:39], s64 offen lds\n"
    "s_add_u32 s64, s64, 35840\n"
    "s_add_u32 m0, m0, 28672\n"
    "v_bfe_u32 v145, v140, 3, 1\n"
    "v_bfe_u32 v146, v140, 4, 1\n"
    "v_and_b32 v150, 3, v140\n"
    "v_lshlrev_b32 v150, 3, v150\n"
    "v_bfe_u32 v144, v140, 2, 2\n"
    "v_lshl_add_u32 v150, v144, 7, v150\n"
    "s_mov_b32 s64, 9216\n"
    "v_lshrrev_b32 v144, 5, v140\n"
    "v_xor_b32 v145, v145, v144\n"
    "v_lshlrev_b32 v145, 5, v145\n"
    "v_xor_b32 v149, v146, v144\n"
    "v_lshl_or_b32 v148, v149, 6, v145\n"
    "v_mov_b32 v149, v148\n"
    "v_mad_u32_u24 v150, v144, s64, v150\n"
    "v_lshl_add_u32 v150, v146, 9, v150\n"
    "v_add_u32 v151, 18432, v150\n"
    "s_mul_i32 s64, s71, 5\n"
    "s_lshl_b32 s65, s71, 2\n"
    "s_add_u32 s65, s65, 2\n"
    "s_cmp_lt_u32 s71, 2\n"
    "s_cselect_b32 s77, s64, s65\n"
    "s_waitcnt vmcnt(18)\n"
    "s_waitcnt lgkmcnt(0)\n"
    "s_barrier\n"
    "s_lshl_b32 s66, s77, 1\n"
    "s_and_b32 s65, s66, 3\n"
    "s_lshl_b32 s65, s65, 5\n"
    "v_xor_b32 v152, s65, v148\n"
    "s_lshr_b32 s65, s66, 2\n"
    "s_lshl_b32 s65, s65, 10\n"
    "s_add_u32 s65, s65, 0\n"
    "v_add3_u32 v152, v152, v150, s65\n"
    "s_and_b32 s65, s66, 3\n"
    "s_lshl_b32 s65, s65, 5\n"
    "v_xor_b32 v153, s65, v149\n"
    "s_lshr_b32 s65, s66, 2\n"
    "s_lshl_b32 s65, s65, 10\n"
    "s_add_u32 s65, s65, 0\n"
    "v_add3_u32 v153, v153, v151, s65\n"
    "ds_read_b64_tr_b16 v[0:1], v152\n"
    "ds_read_b64_tr_b16 v[2:3], v153\n"
    "ds_read_b64_tr_b16 v[4:5], v152 offset:36864\n"
    "ds_read_b64_tr_b16 v[6:7], v153 offset:36864\n"
    "s_add_u32 s66, s66, 1\n"
    "s_min_u32 s66, s66, 35\n"
    "s_and_b32 s65, s66, 3\n"
    "s_lshl_b32 s65, s65, 5\n"
    "v_xor_b32 v152, s65, v148\n"
    "s_lshr_b32 s65, s66, 2\n"
    "s_lshl_b32 s65, s65, 10\n"
    "s_add_u32 s65, s65, 0\n"
    "v_add3_u32 v152, v152, v150, s65\n"
    "s_and_b32 s65, s66, 3\n"
    "s_lshl_b32 s65, s65, 5\n"
    "v_xor_b32 v153, s65, v149\n"
    "s_lshr_b32 s65, s66, 2\n"
    "s_lshl_b32 s65, s65, 10\n"
    "s_add_u32 s65, s65, 0\n"
    "v_add3_u32 v153, v153, v151, s65\n"
    "ds_read_b64_tr_b16 v[16:17], v152\n"
    "ds_read_b64_tr_b16 v[18:19], v153\n"
    "ds_read_b64_tr_b16 v[20:21], v152 offset:36864\n"
    "ds_read_b64_tr_b16 v[22:23], v153 offset:36864\n"
    "s_add_u32 s66, s66, 1\n"
    "s_min_u32 s66, s66, 35\n"
    "s_and_b32 s65, s66, 3\n"
    "s_lshl_b32 s65, s65, 5\n"
    "v_xor_b32 v152, s65, v148\n"
    "s_lshr_b32 s65, s66, 2\n"
    "s_lshl_b32 s65, s65, 10\n"
    "s_add_u32 s65, s65, 0\n"
    "v_add3_u32 v152, v152, v150, s65\n"
    "s_and_b32 s65, s66, 3\n"
    "s_lshl_b32 s65, s65, 5\n"
    "v_xor_b32 v153, s65, v149\n"
    "s_lshr_b32 s65, s66, 2\n"
    "s_lshl_b32 s65, s65, 10\n"
    "s_add_u32 s65, s65, 0\n"
    "v_add3_u32 v153, v153, v151, s65\n"
    "ds_read_b64_tr_b16 v[32:33], v152\n"
    "ds_read_b64_tr_b16 v[34:35], v153\n"
    "ds_read_b64_tr_b16 v[36:37], v152 offset:36864\n"
    "ds_read_b64_tr_b16 v[38:39], v153 offset:36864\n"
    "s_add_u32 s66, s66, 1\n"
    "s_min_u32 s66, s66, 35\n"
    "s_and_b32 s65, s66, 3\n"
    "s_lshl_b32 s65, s65, 5\n"
    "v_xor_b32 v152, s65, v148\n"
    "s_lshr_b32 s65, s66, 2\n"
    "s_lshl_b32 s65, s65, 10\n"
    "s_add_u32 s65, s65, 0\n"
    "v_add3_u32 v152, v152, v150, s65\n"
    "s_and_b32 s65, s66, 3\n"
    "s_lshl_b32 s65, s65, 5\n"
    "v_xor_b32 v153, s65, v149\n"
    "s_lshr_b32 s65, s66, 2\n"
    "s_lshl_b32 s65, s65, 10\n"
    "s_add_u32 s65, s65, 0\n"
    "v_add3_u32 v153, v153, v151, s65\n"
    "ds_read_b64_tr_b16 v[48:49], v152\n"
    "ds_read_b64_tr_b16 v[50:51], v153\n"
    "ds_read_b64_tr_b16 v[52:53], v152 offset:36864\n"
    "ds_read_b64_tr_b16 v[54:55], v153 offset:36864\n"
    "s_add_u32 s66, s66, 1\n"
    "s_min_u32 s66, s66, 35\n"
    "s_and_b32 s65, s66, 3\n"
    "s_lshl_b32 s65, s65, 5\n"
    "v_xor_b32 v152, s65, v148\n"
    "s_lshr_b32 s65, s66, 2\n"
    "s_lshl_b32 s65, s65, 10\n"
    "s_add_u32 s65, s65, 0\n"
    "v_add3_u32 v152, v152, v150, s65\n"
    "s_and_b32 s65, s66, 3\n"
    "s_lshl_b32 s65, s65, 5\n"
    "v_xor_b32 v153, s65, v149\n"
    "s_lshr_b32 s65, s66, 2\n"
    "s_lshl_b32 s65, s65, 10\n"
    "s_add_u32 s65, s65, 0\n"
    "v_add3_u32 v153, v153, v151, s65\n"
    "ds_read_b64_tr_b16 v[64:65], v152\n"
    "ds_read_b64_tr_b16 v[66:67], v153\n"
    "ds_read_b64_tr_b16 v[68:69], v152 offset:36864\n"
    "ds_read_b64_tr_b16 v[70:71], v153 offset:36864\n"
    "s_add_u32 s66, s66, 1\n"
    "s_min_u32 s66, s66, 35\n"
    "s_and_b32 s65, s66, 3\n"
    "s_lshl_b32 s65, s65, 5\n"
    "v_xor_b32 v152, s65, v148\n"
    "s_lshr_b32 s65, s66, 2\n"
    "s_lshl_b32 s65, s65, 10\n"
    "s_add_u32 s65, s65, 0\n"
    "v_add3_u32 v152, v152, v150, s65\n"
    "s_and_b32 s65, s66, 3\n"
    "s_lshl_b32 s65, s65, 5\n"
    "v_xor_b32 v153, s65, v149\n"
    "s_lshr_b32 s65, s66, 2\n"
    "s_lshl_b32 s65, s65, 10\n"
    "s_add_u32 s65, s65, 0\n"
    "v_add3_u32 v153, v153, v151, s65\n"
    "ds_read_b64_tr_b16 v[80:81], v152\n"
    "ds_read_b64_tr_b16 v[82:83], v153\n"
    "ds_read_b64_tr_b16 v[84:85], v152 offset:36864\n"
    "ds_read_b64_tr_b16 v[86:87], v153 offset:36864\n"
    "s_add_u32 s66, s66, 1\n"
    "s_min_u32 s66, s66, 35\n"
    "s_and_b32 s65, s66, 3\n"
    "s_lshl_b32 s65, s65, 5\n"
    "v_xor_b32 v152, s65, v148\n"
    "s_lshr_b32 s65, s66, 2\n"
    "s_lshl_b32 s65, s65, 10\n"
    "s_add_u32 s65, s65, 0\n"
    "v_add3_u32 v152, v152, v150, s65\n"
    "s_and_b32 s65, s66, 3\n"
    "s_lshl_b32 s65, s65, 5\n"
    "v_xor_b32 v153, s65, v149\n"
    "s_lshr_b32 s65, s66, 2\n"
    "s_lshl_b32 s65, s65, 10\n"
    "s_add_u32 s65, s65, 0\n"
    "v_add3_u32 v153, v153, v151, s65\n"
    "ds_read_b64_tr_b16 v[192:193], v152\n"
    "ds_read_b64_tr_b16 v[194:195], v153\n"
    "ds_read_b64_tr_b16 v[196:197], v152 offset:36864\n"
    "ds_read_b64_tr_b16 v[198:199], v153 offset:36864\n"
    "s_add_u32 s66, s66, 1\n"
    "s_min_u32 s66, s66, 35\n"
    "s_and_b32 s65, s66, 3\n"
    "s_lshl_b32 s65, s65, 5\n"
    "v_xor_b32 v152, s65, v148\n"
    "s_lshr_b32 s65, s66, 2\n"
    "s_lshl_b32 s65, s65, 10\n"
    "s_add_u32 s65, s65, 0\n"
    "v_add3_u32 v152, v152, v150, s65\n"
    "s_and_b32 s65, s66, 3\n"
    "s_lshl_b32 s65, s65, 5\n"
    "v_xor_b32 v153, s65, v149\n"
    "s_lshr_b32 s65, s66, 2\n"
    "s_lshl_b32 s65, s65, 10\n"
    "s_add_u32 s65, s65, 0\n"
    "v_add3_u32 v153, v153, v151, s65\n"
    "ds_read_b64_tr_b16 v[208:209], v152\n"
    "ds_read_b64_tr_b16 v[210:211], v153\n"
    "ds_read_b64_tr_b16 v[212:213], v152 offset:36864\n"
    "ds_read_b64_tr_b16 v[214:215], v153 offset:36864\n"
    "s_add_u32 s66, s66, 1\n"
    "s_min_u32 s66, s66, 35\n"
    "s_and_b32 s65, s66, 3\n"
    "s_lshl_b32 s65, s65, 5\n"
    "v_xor_b32 v152, s65, v148\n"
    "s_lshr_b32 s65, s66, 2\n"
    "s_lshl_b32 s65, s65, 10\n"
    "s_add_u32 s65, s65, 0\n"
    "v_add3_u32 v152, v152, v150, s65\n"
    "s_and_b32 s65, s66, 3\n"
    "s_lshl_b32 s65, s65, 5\n"
    "v_xor_b32 v153, s65, v149\n"
    "s_lshr_b32 s65, s66, 2\n"
    "s_lshl_b32 s65, s65, 10\n"
    "s_add_u32 s65, s65, 0\n"
    "v_add3_u32 v153, v153, v151, s65\n"
    "ds_read_b64_tr_b16 v[224:225], v152\n"
    "ds_read_b64_tr_b16 v[226:227], v153\n"
    "ds_read_b64_tr_b16 v[228:229], v152 offset:36864\n"
    "ds_read_b64_tr_b16 v[230:231], v153 offset:36864\n"
    "s_add_u32 s66, s66, 1\n"
    "s_min_u32 s66, s66, 35\n"
    "s_and_b32 s65, s66, 3\n"
    "s_lshl_b32 s65, s65, 5\n"
    "v_xor_b32 v152, s65, v148\n"
    "s_lshr_b32 s65, s66, 2\n"
    "s_lshl_b32 s65, s65, 10\n"
    "s_add_u32 s65, s65, 0\n"
    "v_add3_u32 v152, v152, v150, s65\n"
    "s_and_b32 s65, s66, 3\n"
    "s_lshl_b32 s65, s65, 5\n"
    "v_xor_b32 v153, s65, v149\n"
    "s_lshr_b32 s65, s66, 2\n"
    "s_lshl_b32 s65, s65, 10\n"
    "s_add_u32 s65, s65, 0\n"
    "v_add3_u32 v153, v153, v151, s65\n"
    "ds_read_b64_tr_b16 v[240:241], v152\n"
    "ds_read_b64_tr_b16 v[242:243], v153\n"
    "ds_read_b64_tr_b16 v[244:245], v152 offset:36864\n"
    "ds_read_b64_tr_b16 v[246:247], v153 offset:36864\n"
    "s_add_u32 s66, s66, 1\n"
    "s_min_u32 s66, s66, 35\n"
    "s_waitcnt vmcnt(0)\n"
    "s_waitcnt lgkmcnt(0)\n"
    "s_barrier\n"
    "s_lshl_b32 s66, s77, 1\n"
    "s_and_b32 s65, s66, 3\n"
    "s_lshl_b32 s65, s65, 5\n"
    "v_xor_b32 v152, s65, v148\n"
    "s_lshr_b32 s65, s66, 2\n"
    "s_lshl_b32 s65, s65, 10\n"
    "s_add_u32 s65, s65, 81920\n"
    "v_add3_u32 v152, v152, v150, s65\n"
    "s_and_b32 s65, s66, 3\n"
    "s_lshl_b32 s65, s65, 5\n"
    "v_xor_b32 v153, s65, v149\n"
    "s_lshr_b32 s65, s66, 2\n"
    "s_lshl_b32 s65, s65, 10\n"
    "s_add_u32 s65, s65, 81920\n"
    "v_add3_u32 v153, v153, v151, s65\n"
    "ds_read_b64_tr_b16 v[8:9], v152\n"
    "ds_read_b64_tr_b16 v[10:11], v153\n"
    "ds_read_b64_tr_b16 v[12:13], v152 offset:36864\n"
    "ds_read_b64_tr_b16 v[14:15], v153 offset:36864\n"
    "s_add_u32 s66, s66, 1\n"
    "s_min_u32 s66, s66, 35\n"
    "s_and_b32 s65, s66, 3\n"
    "s_lshl_b32 s65, s65, 5\n"
    "v_xor_b32 v152, s65, v148\n"
    "s_lshr_b32 s65, s66, 2\n"
    "s_lshl_b32 s65, s65, 10\n"
    "s_add_u32 s65, s65, 81920\n"
    "v_add3_u32 v152, v152, v150, s65\n"
    "s_and_b32 s65, s66, 3\n"
    "s_lshl_b32 s65, s65, 5\n"
    "v_xor_b32 v153, s65, v149\n"
    "s_lshr_b32 s65, s66, 2\n"
    "s_lshl_b32 s65, s65, 10\n"
    "s_add_u32 s65, s65, 81920\n"
    "v_add3_u32 v153, v153, v151, s65\n"
    "ds_read_b64_tr_b16 v[24:25], v152\n"
    "ds_read_b64_tr_b16 v[26:27], v153\n"
    "ds_read_b64_tr_b16 v[28:29], v152 offset:36864\n"
    "ds_read_b64_tr_b16 v[30:31], v153 offset:36864\n"
    "s_add_u32 s66, s66, 1\n"
    "s_min_u32 s66, s66, 35\n"
    "s_and_b32 s65, s66, 3\n"
    "s_lshl_b32 s65, s65, 5\n"
    "v_xor_b32 v152, s65, v148\n"
    "s_lshr_b32 s65, s66, 2\n"
    "s_lshl_b32 s65, s65, 10\n"
    "s_add_u32 s65, s65, 81920\n"
    "v_add3_u32 v152, v152, v150, s65\n"
    "s_and_b32 s65, s66, 3\n"
    "s_lshl_b32 s65, s65, 5\n"
    "v_xor_b32 v153, s65, v149\n"
    "s_lshr_b32 s65, s66, 2\n"
    "s_lshl_b32 s65, s65, 10\n"
    "s_add_u32 s65, s65, 81920\n"
    "v_add3_u32 v153, v153, v151, s65\n"
    "ds_read_b64_tr_b16 v[40:41], v152\n"
    "ds_read_b64_tr_b16 v[42:43], v153\n"
    "ds_read_b64_tr_b16 v[44:45], v152 offset:36864\n"
    "ds_read_b64_tr_b16 v[46:47], v153 offset:36864\n"
    "s_add_u32 s66, s66, 1\n"
    "s_min_u32 s66, s66, 35\n"
    "s_and_b32 s65, s66, 3\n"
    "s_lshl_b32 s65, s65, 5\n"
    "v_xor_b32 v152, s65, v148\n"
    "s_lshr_b32 s65, s66, 2\n"
    "s_lshl_b32 s65, s65, 10\n"
    "s_add_u32 s65, s65, 81920\n"
    "v_add3_u32 v152, v152, v150, s65\n"
    "s_and_b32 s65, s66, 3\n"
    "s_lshl_b32 s65, s65, 5\n"
    "v_xor_b32 v153, s65, v149\n"
    "s_lshr_b32 s65, s66, 2\n"
    "s_lshl_b32 s65, s65, 10\n"
    "s_add_u32 s65, s65, 81920\n"
    "v_add3_u32 v153, v153, v151, s65\n"
    "ds_read_b64_tr_b16 v[56:57], v152\n"
    "ds_read_b64_tr_b16 v[58:59], v153\n"
    "ds_read_b64_tr_b16 v[60:61], v152 offset:36864\n"
    "ds_read_b64_tr_b16 v[62:63], v153 offset:36864\n"
    "s_add_u32 s66, s66, 1\n"
    "s_min_u32 s66, s66, 35\n"
    "s_and_b32 s65, s66, 3\n"
    "s_lshl_b32 s65, s65, 5\n"
    "v_xor_b32 v152, s65, v148\n"
    "s_lshr_b32 s65, s66, 2\n"
    "s_lshl_b32 s65, s65, 10\n"
    "s_add_u32 s65, s65, 81920\n"
    "v_add3_u32 v152, v152, v150, s65\n"
    "s_and_b32 s65, s66, 3\n"
    "s_lshl_b32 s65, s65, 5\n"
    "v_xor_b32 v153, s65, v149\n"
    "s_lshr_b32 s65, s66, 2\n"
    "s_lshl_b32 s65, s65, 10\n"
    "s_add_u32 s65, s65, 81920\n"
    "v_add3_u32 v153, v153, v151, s65\n"
    "ds_read_b64_tr_b16 v[72:73], v152\n"
    "ds_read_b64_tr_b16 v[74:75], v153\n"
    "ds_read_b64_tr_b16 v[76:77], v152 offset:36864\n"
    "ds_read_b64_tr_b16 v[78:79], v153 offset:36864\n"
    "s_add_u32 s66, s66, 1\n"
    "s_min_u32 s66, s66, 35\n"
    "s_and_b32 s65, s66, 3\n"
    "s_lshl_b32 s65, s65, 5\n"
    "v_xor_b32 v152, s65, v148\n"
    "s_lshr_b32 s65, s66, 2\n"
    "s_lshl_b32 s65, s65, 10\n"
    "s_add_u32 s65, s65, 81920\n"
    "v_add3_u32 v152, v152, v150, s65\n"
    "s_and_b32 s65, s66, 3\n"
    "s_lshl_b32 s65, s65, 5\n"
    "v_xor_b32 v153, s65, v149\n"
    "s_lshr_b32 s65, s66, 2\n"
    "s_lshl_b32 s65, s65, 10\n"
    "s_add_u32 s65, s65, 81920\n"
    "v_add3_u32 v153, v153, v151, s65\n"
    "ds_read_b64_tr_b16 v[88:89], v152\n"
    "ds_read_b64_tr_b16 v[90:91], v153\n"
    "ds_read_b64_tr_b16 v[92:93], v152 offset:36864\n"
    "ds_read_b64_tr_b16 v[94:95], v153 offset:36864\n"
    "s_add_u32 s66, s66, 1\n"
    "s_min_u32 s66, s66, 35\n"
    "s_and_b32 s65, s66, 3\n"
    "s_lshl_b32 s65, s65, 5\n"
    "v_xor_b32 v152, s65, v148\n"
    "s_lshr_b32 s65, s66, 2\n"
    "s_lshl_b32 s65, s65, 10\n"
    "s_add_u32 s65, s65, 81920\n"
    "v_add3_u32 v152, v152, v150, s65\n"
    "s_and_b32 s65, s66, 3\n"
    "s_lshl_b32 s65, s65, 5\n"
    "v_xor_b32 v153, s65, v149\n"
    "s_lshr_b32 s65, s66, 2\n"
    "s_lshl_b32 s65, s65, 10\n"
    "s_add_u32 s65, s65, 81920\n"
    "v_add3_u32 v153, v153, v151, s65\n"
    "ds_read_b64_tr_b16 v[200:201], v152\n"
    "ds_read_b64_tr_b16 v[202:203], v153\n"
    "ds_read_b64_tr_b16 v[204:205], v152 offset:36864\n"
    "ds_read_b64_tr_b16 v[206:207], v153 offset:36864\n"
    "s_add_u32 s66, s66, 1\n"
    "s_min_u32 s66, s66, 35\n"
    "s_and_b32 s65, s66, 3\n"
    "s_lshl_b32 s65, s65, 5\n"
    "v_xor_b32 v152, s65, v148\n"
    "s_lshr_b32 s65, s66, 2\n"
    "s_lshl_b32 s65, s65, 10\n"
    "s_add_u32 s65, s65, 81920\n"
    "v_add3_u32 v152, v152, v150, s65\n"
    "s_and_b32 s65, s66, 3\n"
    "s_lshl_b32 s65, s65, 5\n"
    "v_xor_b32 v153, s65, v149\n"
    "s_lshr_b32 s65, s66, 2\n"
    "s_lshl_b32 s65, s65, 10\n"
    "s_add_u32 s65, s65, 81920\n"
    "v_add3_u32 v153, v153, v151, s65\n"
    "ds_read_b64_tr_b16 v[216:217], v152\n"
    "ds_read_b64_tr_b16 v[218:219], v153\n"
    "ds_read_b64_tr_b16 v[220:221], v152 offset:36864\n"
    "ds_read_b64_tr_b16 v[222:223], v153 offset:36864\n"
    "s_add_u32 s66, s66, 1\n"
    "s_min_u32 s66, s66, 35\n"
    "s_and_b32 s65, s66, 3\n"
    "s_lshl_b32 s65, s65, 5\n"
    "v_xor_b32 v152, s65, v148\n"
    "s_lshr_b32 s65, s66, 2\n"
    "s_lshl_b32 s65, s65, 10\n"
    "s_add_u32 s65, s65, 81920\n"
    "v_add3_u32 v152, v152, v150, s65\n"
    "s_and_b32 s65, s66, 3\n"
    "s_lshl_b32 s65, s65, 5\n"
    "v_xor_b32 v153, s65, v149\n"
    "s_lshr_b32 s65, s66, 2\n"
    "s_lshl_b32 s65, s65, 10\n"
    "s_add_u32 s65, s65, 81920\n"
    "v_add3_u32 v153, v153, v151, s65\n"
    "ds_read_b64_tr_b16 v[232:233], v152\n"
    "ds_read_b64_tr_b16 v[234:235], v153\n"
    "ds_read_b64_tr_b16 v[236:237], v152 offset:36864\n"
    "ds_read_b64_tr_b16 v[238:239], v153 offset:36864\n"
    "s_add_u32 s66, s66, 1\n"
    "s_min_u32 s66, s66, 35\n"
    "s_and_b32 s65, s66, 3\n"
    "s_lshl_b32 s65, s65, 5\n"
    "v_xor_b32 v152, s65, v148\n"
    "s_lshr_b32 s65, s66, 2\n"
    "s_lshl_b32 s65, s65, 10\n"
    "s_add_u32 s65, s65, 81920\n"
    "v_add3_u32 v152, v152, v150, s65\n"
    "s_and_b32 s65, s66, 3\n"
    "s_lshl_b32 s65, s65, 5\n"
    "v_xor_b32 v153, s65, v149\n"
    "s_lshr_b32 s65, s66, 2\n"
    "s_lshl_b32 s65, s65, 10\n"
    "s_add_u32 s65, s65, 81920\n"
    "v_add3_u32 v153, v153, v151, s65\n"
    "ds_read_b64_tr_b16 v[248:249], v152\n"
    "ds_read_b64_tr_b16 v[250:251], v153\n"
    "ds_read_b64_tr_b16 v[252:253], v152 offset:36864\n"
    "ds_read_b64_tr_b16 v[254:255], v153 offset:36864\n"
    "s_add_u32 s66, s66, 1\n"
    "s_min_u32 s66, s66, 35\n"
    "s_waitcnt lgkmcnt(0)\n"
    "s_barrier\n"
    "v_lshlrev_b32 v143, 4, v140\n"
    "s_mul_i32 s64, s71, 24576\n"
    "v_add_u32 v131, s64, v143\n"
    "ds_write_b128 v131, v[0:3]\n"
    "ds_write_b128 v131, v[4:7] offset:1024\n"
    "ds_write_b128 v131, v[8:11] offset:2048\n"
    "ds_write_b128 v131, v[12:15] offset:3072\n"
    "ds_write_b128 v131, v[16:19] offset:4096\n"
    "ds_write_b128 v131, v[20:23] offset:5120\n"
    "ds_write_b128 v131, v[24:27] offset:6144\n"
    "ds_write_b128 v131, v[28:31] offset:7168\n"
    "ds_write_b128 v131, v[32:35] offset:8192\n"
    "ds_write_b128 v131, v[36:39] offset:9216\n"
    "ds_write_b128 v131, v[40:43] offset:10240\n"
    "ds_write_b128 v131, v[44:47] offset:11264\n"
    "ds_write_b128 v131, v[48:51] offset:12288\n"
    "ds_write_b128 v131, v[52:55] offset:13312\n"
    "ds_write_b128 v131, v[56:59] offset:14336\n"
    "ds_write_b128 v131, v[60:63] offset:15360\n"
    "ds_write_b128 v131, v[64:67] offset:16384\n"
    "ds_write_b128 v131, v[68:71] offset:17408\n"
    "ds_write_b128 v131, v[72:75] offset:18432\n"
    "ds_write_b128 v131, v[76:79] offset:19456\n"
    "ds_write_b128 v131, v[80:83] offset:20480\n"
    "ds_write_b128 v131, v[84:87] offset:21504\n"
    "ds_write_b128 v131, v[88:91] offset:22528\n"
    "ds_write_b128 v131, v[92:95] offset:23552\n"
    "s_mul_i32 s64, s71, 9216\n"
    "s_add_u32 s64, s64, 98304\n"
    "v_add_u32 v132, s64, v143\n"
    "v_add_u32 v133, 98304, v143\n"
    "v_add_u32 v134, 139776, v143\n"
    "s_lshl_b32 s64, s71, 10\n"
    "v_add_u32 v135, s64, v134\n"
    "s_lshl_b32 s64, s71, 10\n"
    "s_add_u32 s64, s64, 135168\n"
    "v_add_u32 v136, s64, v143\n"
    "v_and_b32 v144, 15, v140\n"
    "s_lshl_b32 s64, s71, 7\n"
    "s_add_u32 s64, s64, 4096\n"
    "v_lshl_add_u32 v130, v144, 3, s64\n"
    "v_add_u32 v137, 135168, v130\n"
    "v_lshrrev_b32 v145, 4, v140\n"
    "v_lshlrev_b32 v138, 7, v145\n"
    "v_lshl_add_u32 v138, v144, 3, v138\n"
    "v_add_u32 v138, 135168, v138\n"
    "s_mul_i32 s64, s73, 36\n"
    "s_add_u32 s64, s64, s72\n"
    "s_lshl_b32 s65, s64, 13\n"
    "s_add_u32 s65, s65, 0x21f000\n"
    "v_lshl_add_u32 v129, v140, 6, s65\n"
    "s_mov_b32 s82, -1\n"
    "s_mov_b32 s83, 15\n"
    "s_mul_i32 s65, s77, 72\n"
    "s_add_u32 s64, s64, s65\n"
    "s_lshl_b32 s64, s64, 6\n"
    "s_add_u32 s64, s64, 0x2af000\n"
    "v_and_b32 v144, 7, v140\n"
    "v_lshrrev_b32 v145, 3, v140\n"
    "v_mad_u32_u24 v146, v145, 18, v144\n"
    "s_mov_b32 s65, 4608\n"
    "v_mov_b32 v128, s64\n"
    "v_mad_u32_u24 v128, v146, s65, v128\n"
    "s_cmp_lt_u32 s71, 2\n"
    "s_cselect_b32 s64, 5, 4\n"
    "v_cmp_gt_u32 vcc, s64, v144\n"
    "v_mov_b32 v146, 0x7fffffff\n"
    "s_nop 0\n"
    "v_cndmask_b32 v128, v146, v128, vcc\n"
    "v_cmp_gt_u32 vcc, 16, v140\n"
    "v_mov_b32 v146, 0x7fffffff\n"
    "s_nop 0\n"
    "v_cndmask_b32 v128, v146, v128, vcc\n"
    "v_mov_b32 v139, v143\n"
    "v_mov_b32 v96, 0x3f803f80\n"
    "v_mov_b32 v97, 0x3f803f80\n"
    "v_mov_b32 v98, 0x3f803f80\n"
    "v_mov_b32 v99, 0x3f803f80\n"
    "s_nop 1\n"
    "v_mfma_f32_16x16x32_bf16 v[144:147], a[0:3], v[96:99], 0\n"
    "v_mfma_f32_16x16x32_bf16 v[152:155], a[144:147], v[96:99], 0\n"
    "v_mfma_f32_16x16x32_bf16 v[148:151], a[72:75], v[96:99], 0\n"
    "v_mfma_f32_16x16x32_bf16 v[156:159], a[216:219], v[96:99], 0\n"
    "v_mfma_f32_16x16x32_bf16 v[144:147], a[4:7], v[96:99], v[144:147]\n"
    "v_mfma_f32_16x16x32_bf16 v[152:155], a[148:151], v[96:99], v[152:155]\n"
    "v_mfma_f32_16x16x32_bf16 v[148:151], a[76:79], v[96:99], v[148:151]\n"
    "v_mfma_f32_16x16x32_bf16 v[156:159], a[220:223], v[96:99], v[156:159]\n"
    "v_mfma_f32_16x16x32_bf16 v[144:147], a[8:11], v[96:99], v[144:147]\n"
    "v_mfma_f32_16x16x32_bf16 v[152:155], a[152:155], v[96:99], v[152:155]\n"
    "v_mfma_f32_16x16x32_bf16 v[148:151], a[80:83], v[96:99], v[148:151]\n"
    "v_mfma_f32_16x16x32_bf16 v[156:159], a[224:227], v[96:99], v[156:159]\n"
    "v_mfma_f32_16x16x32_bf16 v[144:147], a[12:15], v[96:99], v[144:147]\n"
    "v_mfma_f32_16x16x32_bf16 v[152:155], a[156:159], v[96:99], v[152:155]\n"
    "v_mfma_f32_16x16x32_bf16 v[148:151], a[84:87], v[96:99], v[148:151]\n"
    "v_mfma_f32_16x16x32_bf16 v[156:159], a[228:231], v[96:99], v[156:159]\n"
    "v_mfma_f32_16x16x32_bf16 v[144:147], a[16:19], v[96:99], v[144:147]\n"
    "v_mfma_f32_16x16x32_bf16 v[152:155], a[160:163], v[96:99], v[152:155]\n"
    "v_mfma_f32_16x16x32_bf16 v[148:151], a[88:91], v[96:99], v[148:151]\n"
    "v_mfma_f32_16x16x32_bf16 v[156:159], a[232:235], v[96:99], v[156:159]\n"
    "v_mfma_f32_16x16x32_bf16 v[144:147], a[20:23], v[96:99], v[144:147]\n"
    "v_mfma_f32_16x16x32_bf16 v[152:155], a[164:167], v[96:99], v[152:155]\n"
    "v_mfma_f32_16x16x32_bf16 v[148:151], a[92:95], v[96:99], v[148:151]\n"
    "v_mfma_f32_16x16x32_bf16 v[156:159], a[236:239], v[96:99], v[156:159]\n"
    "v_mfma_f32_16x16x32_bf16 v[144:147], a[24:27], v[96:99], v[144:147]\n"
    "v_mfma_f32_16x16x32_bf16 v[152:155], a[168:171], v[96:99], v[152:155]\n"
    "v_mfma_f32_16x16x32_bf16 v[148:151], a[96:99], v[96:99], v[148:151]\n"
    "v_mfma_f32_16x16x32_bf16 v[156:159], a[240:243], v[96:99], v[156:159]\n"
    "v_mfma_f32_16x16x32_bf16 v[144:147], a[28:31], v[96:99], v[144:147]\n"
    "v_mfma_f32_16x16x32_bf16 v[152:155], a[172:175], v[96:99], v[152:155]\n"
    "v_mfma_f32_16x16x32_bf16 v[148:151], a[100:103], v[96:99], v[148:151]\n"
    "v_mfma_f32_16x16x32_bf16 v[156:159], a[244:247], v[96:99], v[156:159]\n"
    "v_mfma_f32_16x16x32_bf16 v[144:147], a[32:35], v[96:99], v[144:147]\n"
    "v_mfma_f32_16x16x32_bf16 v[152:155], a[176:179], v[96:99], v[152:155]\n"
    "v_mfma_f32_16x16x32_bf16 v[148:151], a[104:107], v[96:99], v[148:151]\n"
    "v_mfma_f32_16x16x32_bf16 v[156:159], a[248:251], v[96:99], v[156:159]\n"
    "v_mfma_f32_16x16x32_bf16 v[144:147], a[36:39], v[96:99], v[144:147]\n"
    "v_mfma_f32_16x16x32_bf16 v[152:155], a[180:183], v[96:99], v[152:155]\n"
    "v_mfma_f32_16x16x32_bf16 v[148:151], a[108:111], v[96:99], v[148:151]\n"
    "v_mfma_f32_16x16x32_bf16 v[156:159], a[252:255], v[96:99], v[156:159]\n"
    "v_mfma_f32_16x16x32_bf16 v[144:147], a[40:43], v[96:99], v[144:147]\n"
    "v_mfma_f32_16x16x32_bf16 v[152:155], a[184:187], v[96:99], v[152:155]\n"
    "v_mfma_f32_16x16x32_bf16 v[148:151], a[112:115], v[96:99], v[148:151]\n"
    "v_mfma_f32_16x16x32_bf16 v[156:159], v[160:163], v[96:99], v[156:159]\n"
    "v_mfma_f32_16x16x32_bf16 v[144:147], a[44:47], v[96:99], v[144:147]\n"
    "v_mfma_f32_16x16x32_bf16 v[152:155], a[188:191], v[96:99], v[152:155]\n"
    "v_mfma_f32_16x16x32_bf16 v[148:151], a[116:119], v[96:99], v[148:151]\n"
    "v_mfma_f32_16x16x32_bf16 v[156:159], v[164:167], v[96:99], v[156:159]\n"
    "v_mfma_f32_16x16x32_bf16 v[144:147], a[48:51], v[96:99], v[144:147]\n"
    "v_mfma_f32_16x16x32_bf16 v[152:155], a[192:195], v[96:99], v[152:155]\n"
    "v_mfma_f32_16x16x32_bf16 v[148:151], a[120:123], v[96:99], v[148:151]\n"
    "v_mfma_f32_16x16x32_bf16 v[156:159], v[168:171], v[96:99], v[156:159]\n"
    "v_mfma_f32_16x16x32_bf16 v[144:147], a[52:55], v[96:99], v[144:147]\n"
    "v_mfma_f32_16x16x32_bf16 v[152:155], a[196:199], v[96:99], v[152:155]\n"
    "v_mfma_f32_16x16x32_bf16 v[148:151], a[124:127], v[96:99], v[148:151]\n"
    "v_mfma_f32_16x16x32_bf16 v[156:159], v[172:175], v[96:99], v[156:159]\n"
    "v_mfma_f32_16x16x32_bf16 v[144:147], a[56:59], v[96:99], v[144:147]\n"
    "v_mfma_f32_16x16x32_bf16 v[152:155], a[200:203], v[96:99], v[152:155]\n"
    "v_mfma_f32_16x16x32_bf16 v[148:151], a[128:131], v[96:99], v[148:151]\n"
    "v_mfma_f32_16x16x32_bf16 v[156:159], v[176:179], v[96:99], v[156:159]\n"
    "v_mfma_f32_16x16x32_bf16 v[144:147], a[60:63], v[96:99], v[144:147]\n"
    "v_mfma_f32_16x16x32_bf16 v[152:155], a[204:207], v[96:99], v[152:155]\n"
    "v_mfma_f32_16x16x32_bf16 v[148:151], a[132:135], v[96:99], v[148:151]\n"
    "v_mfma_f32_16x16x32_bf16 v[156:159], v[180:183], v[96:99], v[156:159]\n"
    "v_mfma_f32_16x16x32_bf16 v[144:147], a[64:67], v[96:99], v[144:147]\n"
    "v_mfma_f32_16x16x32_bf16 v[152:155], a[208:211], v[96:99], v[152:155]\n"
    "v_mfma_f32_16x16x32_bf16 v[148:151], a[136:139], v[96:99], v[148:151]\n"
    "v_mfma_f32_16x16x32_bf16 v[156:159], v[184:187], v[96:99], v[156:159]\n"
    "v_mfma_f32_16x16x32_bf16 v[144:147], a[68:71], v[96:99], v[144:147]\n"
    "v_mfma_f32_16x16x32_bf16 v[152:155], a[212:215], v[96:99], v[152:155]\n"
    "v_mfma_f32_16x16x32_bf16 v[148:151], a[140:143], v[96:99], v[148:151]\n"
    "v_mfma_f32_16x16x32_bf16 v[156:159], v[188:191], v[96:99], v[156:159]\n"
    "s_waitcnt lgkmcnt(0)\n"
    "s_mov_b32 s80, -1\n"
    "s_mov_b32 s81, 0\n"
    "s_mov_b32 s74, s73\n"
    "de_wait_%=:\n"
    "s_bfe_u32 s90, s74, 0x10001\n"
    "s_lshl_b32 s90, s90, 12\n"
    "s_add_u32 s67, s74, 1\n"
    "s_mov_b32 s69, 0\n"
    "de_ww1_%=:\n"
    "buffer_load_dword v127, v129, s[20:23], s90 offen sc1\n"
    "s_add_u32 s69, s69, 1\n"
    "s_cmp_gt_u32 s69, 0x40000\n"
    "s_cselect_b32 s64, 1, 0\n"
    "s_or_b32 s70, s70, s64\n"
    "s_waitcnt vmcnt(0)\n"
    "s_cbranch_scc1 de_wwok2_%=\n"
    "v_cmp_gt_u32 vcc, s67, v127\n"
    "s_and_b64 vcc, vcc, s[82:83]\n"
    "s_cbranch_scc0 de_wwok2_%=\n"
    "s_sleep 1\n"
    "s_branch de_ww1_%=\n"
    "de_wwok2_%=:\n"
    "s_mul_i32 s65, s74, 41472\n"
    "s_mul_i32 s64, s71, 9216\n"
    "s_add_u32 s64, s64, s65\n"
    "buffer_load_dwordx4 v[0:3], v139, s[24:27], s64 offen sc1\n"
    "buffer_load_dwordx4 v[4:7], v139, s[24:27], s64 offen offset:1024 sc1\n"
    "buffer_load_dwordx4 v[8:11], v139, s[24:27], s64 offen offset:2048 sc1\n"
    "buffer_load_dwordx4 v[12:15], v139, s[24:27], s64 offen offset:3072 sc1\n"
    "s_add_u32 s64, s64, 4096\n"
    "buffer_load_dwordx4 v[16:19], v139, s[24:27], s64 offen sc1\n"
    "buffer_load_dwordx4 v[20:23], v139, s[24:27], s64 offen offset:1024 sc1\n"
    "buffer_load_dwordx4 v[24:27], v139, s[24:27], s64 offen offset:2048 sc1\n"
    "buffer_load_dwordx4 v[28:31], v139, s[24:27], s64 offen offset:3072 sc1\n"
    "s_add_u32 s64, s64, 4096\n"
    "buffer_load_dwordx4 v[32:35], v139, s[24:27], s64 offen sc1\n"
    "s_add_u32 s65, s65, 36864\n"
    "s_lshl_b32 s64, s71, 10\n"
    "s_add_u32 s64, s64, s65\n"
    "buffer_load_dwordx4 v[140:143], v139, s[24:27], s64 offen sc1\n"
    "buffer_load_dwordx2 v[124:125], v130, s[24:27], s65 offen sc1\n"
    "s_waitcnt vmcnt(10)\n"
    "ds_write_b128 v132, v[0:3]\n"
    "s_waitcnt vmcnt(9)\n"
    "ds_write_b128 v132, v[4:7] offset:1024\n"
    "s_waitcnt vmcnt(8)\n"
    "ds_write_b128 v132, v[8:11] offset:2048\n"
    "s_waitcnt vmcnt(7)\n"
    "ds_write_b128 v132, v[12:15] offset:3072\n"
    "s_waitcnt vmcnt(6)\n"
    "ds_write_b128 v132, v[16:19] offset:4096\n"
    "s_waitcnt vmcnt(5)\n"
    "ds_write_b128 v132, v[20:23] offset:5120\n"
    "s_waitcnt vmcnt(4)\n"
    "ds_write_b128 v132, v[24:27] offset:6144\n"
    "s_waitcnt vmcnt(3)\n"
    "ds_write_b128 v132, v[28:31] offset:7168\n"
    "s_waitcnt vmcnt(2)\n"
    "ds_write_b128 v132, v[32:35] offset:8192\n"
    "s_waitcnt vmcnt(0)\n"
    "ds_write_b128 v136, v[140:143]\n"
    "ds_write_b64 v137, v[124:125]\n"
    "s_waitcnt lgkmcnt(0)\n"
    "s_barrier\n"
    "de_loop_%=:\n"
    "ds_read_b128 v[36:39], v133\n"
    "ds_read_b128 v[40:43], v133 offset:18432\n"
    "ds_read_b128 v[44:47], v133 offset:1024\n"
    "ds_read_b128 v[48:51], v133 offset:19456\n"
    "s_add_u32 s67, s74, 2\n"
    "s_bfe_u32 s90, s67, 0x10001\n"
    "s_lshl_b32 s90, s90, 12\n"
    "ds_read_b128 v[52:55], v133 offset:2048\n"
    "ds_read_b128 v[56:59], v133 offset:20480\n"
    "ds_read_b64 v[100:101], v138\n"
    "ds_read_b64 v[102:103], v138 offset:512\n"
    "ds_read_b64 v[104:105], v138 offset:1024\n"
    "ds_read_b64 v[106:107], v138 offset:1536\n"
    "ds_read_b64 v[108:109], v138 offset:2048\n"
    "s_waitcnt lgkmcnt(10)\n"
    "v_mfma_f32_16x16x32_bf16 v[60:63], a[0:3], v[36:39], 0\n"
    "v_mfma_f32_16x16x32_bf16 v[76:79], a[144:147], v[36:39], 0\n"
    "v_mfma_f32_16x16x32_bf16 v[64:67], a[72:75], v[36:39], 0\n"
    "v_mfma_f32_16x16x32_bf16 v[80:83], a[216:219], v[36:39], 0\n"
    "s_waitcnt lgkmcnt(9)\n"
    "v_mfma_f32_16x16x32_bf16 v[68:71], a[0:3], v[40:43], 0\n"
    "v_mfma_f32_16x16x32_bf16 v[84:87], a[144:147], v[40:43], 0\n"
    "v_mfma_f32_16x16x32_bf16 v[72:75], a[72:75], v[40:43], 0\n"
    "v_mfma_f32_16x16x32_bf16 v[88:91], a[216:219], v[40:43], 0\n"
    "ds_read_b128 v[36:39], v133 offset:3072\n"
    "ds_read_b128 v[40:43], v133 offset:21504\n"
    "ds_read_b64 v[110:111], v138 offset:2304\n"
    "ds_read_b64 v[112:113], v138 offset:2816\n"
    "ds_read_b64 v[114:115], v138 offset:3328\n"
    "ds_read_b64 v[116:117], v138 offset:3840\n"
    "ds_read_b64 v[118:119], v138 offset:4352\n"
    "s_waitcnt lgkmcnt(15)\n"
    "v_mfma_f32_16x16x32_bf16 v[60:63], a[4:7], v[44:47], v[60:63]\n"
    "v_mfma_f32_16x16x32_bf16 v[76:79], a[148:151], v[44:47], v[76:79]\n"
    "v_mfma_f32_16x16x32_bf16 v[64:67], a[76:79], v[44:47], v[64:67]\n"
    "v_mfma_f32_16x16x32_bf16 v[80:83], a[220:223], v[44:47], v[80:83]\n"
    "s_waitcnt lgkmcnt(14)\n"
    "v_mfma_f32_16x16x32_bf16 v[68:71], a[4:7], v[48:51], v[68:71]\n"
    "v_mfma_f32_16x16x32_bf16 v[84:87], a[148:151], v[48:51], v[84:87]\n"
    "v_mfma_f32_16x16x32_bf16 v[72:75], a[76:79], v[48:51], v[72:75]\n"
    "v_mfma_f32_16x16x32_bf16 v[88:91], a[220:223], v[48:51], v[88:91]\n"
    "ds_read_b128 v[44:47], v133 offset:4096\n"
    "ds_read_b128 v[48:51], v133 offset:22528\n"
    "s_waitcnt lgkmcnt(15)\n"
    "v_mfma_f32_16x16x32_bf16 v[60:63], a[8:11], v[52:55], v[60:63]\n"
    "s_waitcnt lgkmcnt(9)\n"
    "v_mfma_f32_16x16x32_bf16 v[76:79], a[152:155], v[52:55], v[76:79]\n"
    "v_cndmask_b32 v108, 0, v108, s[80:81]\n"
    "v_mfma_f32_16x16x32_bf16 v[64:67], a[80:83], v[52:55], v[64:67]\n"
    "v_cndmask_b32 v109, 0, v109, s[80:81]\n"
    "v_mfma_f32_16x16x32_bf16 v[80:83], a[224:227], v[52:55], v[80:83]\n"
    "v_add_f32 v92, v100, v102\n"
    "v_mfma_f32_16x16x32_bf16 v[68:71], a[8:11], v[56:59], v[68:71]\n"
    "v_add_f32 v93, v104, v106\n"
    "v_mfma_f32_16x16x32_bf16 v[84:87], a[152:155], v[56:59], v[84:87]\n"
    "v_add_f32 v92, v92, v108\n"
    "v_mfma_f32_16x16x32_bf16 v[72:75], a[80:83], v[56:59], v[72:75]\n"
    "v_add_f32 v92, v92, v93\n"
    "v_mfma_f32_16x16x32_bf16 v[88:91], a[224:227], v[56:59], v[88:91]\n"
    "v_mov_b32 v127, v92\n"
    "ds_read_b128 v[52:55], v133 offset:5120\n"
    "ds_read_b128 v[56:59], v133 offset:23552\n"
    "s_waitcnt lgkmcnt(10)\n"
    "v_mfma_f32_16x16x32_bf16 v[60:63], a[12:15], v[36:39], v[60:63]\n"
    "s_nop 0\n"
    "v_mfma_f32_16x16x32_bf16 v[76:79], a[156:159], v[36:39], v[76:79]\n"
    "v_permlane16_swap_b32 v92, v127\n"
    "v_mfma_f32_16x16x32_bf16 v[64:67], a[84:87], v[36:39], v[64:67]\n"
    "v_add_f32 v92, v92, v127\n"
    "v_mfma_f32_16x16x32_bf16 v[80:83], a[228:231], v[36:39], v[80:83]\n"
    "v_mov_b32 v127, v92\n"
    "s_waitcnt lgkmcnt(9)\n"
    "v_mfma_f32_16x16x32_bf16 v[68:71], a[12:15], v[40:43], v[68:71]\n"
    "s_nop 0\n"
    "v_mfma_f32_16x16x32_bf16 v[84:87], a[156:159], v[40:43], v[84:87]\n"
    "v_permlane32_swap_b32 v92, v127\n"
    "v_mfma_f32_16x16x32_bf16 v[72:75], a[84:87], v[40:43], v[72:75]\n"
    "v_add_f32 v92, v92, v127\n"
    "v_mfma_f32_16x16x32_bf16 v[88:91], a[228:231], v[40:43], v[88:91]\n"
    "v_mul_f32 v93, 0x3d638e39, v92\n"
    "ds_read_b128 v[36:39], v133 offset:6144\n"
    "ds_read_b128 v[40:43], v133 offset:24576\n"
    "s_waitcnt lgkmcnt(5)\n"
    "v_mfma_f32_16x16x32_bf16 v[60:63], a[16:19], v[44:47], v[60:63]\n"
    "v_mul_f32 v92, 0xbae38e39, v92\n"
    "v_mfma_f32_16x16x32_bf16 v[76:79], a[160:163], v[44:47], v[76:79]\n"
    "v_sub_f32 v100, v100, v93\n"
    "v_mfma_f32_16x16x32_bf16 v[64:67], a[88:91], v[44:47], v[64:67]\n"
    "v_sub_f32 v102, v102, v93\n"
    "v_mfma_f32_16x16x32_bf16 v[80:83], a[232:235], v[44:47], v[80:83]\n"
    "v_sub_f32 v104, v104, v93\n"
    "s_waitcnt lgkmcnt(4)\n"
    "v_mfma_f32_16x16x32_bf16 v[68:71], a[16:19], v[48:51], v[68:71]\n"
    "v_sub_f32 v106, v106, v93\n"
    "v_mfma_f32_16x16x32_bf16 v[84:87], a[160:163], v[48:51], v[84:87]\n"
    "v_sub_f32 v108, v108, v93\n"
    "v_mfma_f32_16x16x32_bf16 v[72:75], a[88:91], v[48:51], v[72:75]\n"
    "v_cndmask_b32 v108, 0, v108, s[80:81]\n"
    "v_mfma_f32_16x16x32_bf16 v[88:91], a[232:235], v[48:51], v[88:91]\n"
    "v_mul_f32 v100, v100, v100\n"
    "ds_read_b128 v[44:47], v133 offset:7168\n"
    "ds_read_b128 v[48:51], v133 offset:25600\n"
    "s_waitcnt lgkmcnt(5)\n"
    "v_mfma_f32_16x16x32_bf16 v[60:63], a[20:23], v[52:55], v[60:63]\n"
    "v_fmamk_f32 v101, v100, 0x3d000000, v101\n"
    "v_mfma_f32_16x16x32_bf16 v[76:79], a[164:167], v[52:55], v[76:79]\n"
    "v_mul_f32 v102, v102, v102\n"
    "v_mfma_f32_16x16x32_bf16 v[64:67], a[92:95], v[52:55], v[64:67]\n"
    "v_fmamk_f32 v103, v102, 0x3d000000, v103\n"
    "v_mfma_f32_16x16x32_bf16 v[80:83], a[236:239], v[52:55], v[80:83]\n"
    "v_mul_f32 v104, v104, v104\n"
    "s_waitcnt lgkmcnt(4)\n"
    "v_mfma_f32_16x16x32_bf16 v[68:71], a[20:23], v[56:59], v[68:71]\n"
    "v_fmamk_f32 v105, v104, 0x3d000000, v105\n"
    "v_mfma_f32_16x16x32_bf16 v[84:87], a[164:167], v[56:59], v[84:87]\n"
    "v_mul_f32 v106, v106, v106\n"
    "v_mfma_f32_16x16x32_bf16 v[72:75], a[92:95], v[56:59], v[72:75]\n"
    "v_fmamk_f32 v107, v106, 0x3d000000, v107\n"
    "v_mfma_f32_16x16x32_bf16 v[88:91], a[236:239], v[56:59], v[88:91]\n"
    "v_mul_f32 v108, v108, v108\n"
    "ds_read_b128 v[52:55], v133 offset:8192\n"
    "ds_read_b128 v[56:59], v133 offset:26624\n"
    "s_waitcnt lgkmcnt(5)\n"
    "v_mfma_f32_16x16x32_bf16 v[60:63], a[24:27], v[36:39], v[60:63]\n"
    "v_fmamk_f32 v109, v108, 0x3d000000, v109\n"
    "v_mfma_f32_16x16x32_bf16 v[76:79], a[168:171], v[36:39], v[76:79]\n"
    "v_add_f32 v96, v101, v103\n"
    "v_mfma_f32_16x16x32_bf16 v[64:67], a[96:99], v[36:39], v[64:67]\n"
    "v_add_f32 v97, v105, v107\n"
    "v_mfma_f32_16x16x32_bf16 v[80:83], a[240:243], v[36:39], v[80:83]\n"
    "v_add_f32 v96, v96, v109\n"
    "s_waitcnt lgkmcnt(4)\n"
    "v_mfma_f32_16x16x32_bf16 v[68:71], a[24:27], v[40:43], v[68:71]\n"
    "v_add_f32 v96, v96, v97\n"
    "v_mfma_f32_16x16x32_bf16 v[84:87], a[168:171], v[40:43], v[84:87]\n"
    "v_mov_b32 v127, v96\n"
    "v_mfma_f32_16x16x32_bf16 v[72:75], a[96:99], v[40:43], v[72:75]\n"
    "s_nop 0\n"
    "v_mfma_f32_16x16x32_bf16 v[88:91], a[240:243], v[40:43], v[88:91]\n"
    "v_permlane16_swap_b32 v96, v127\n"
    "ds_read_b128 v[36:39], v133 offset:9216\n"
    "ds_read_b128 v[40:43], v133 offset:27648\n"
    "s_waitcnt lgkmcnt(5)\n"
    "v_mfma_f32_16x16x32_bf16 v[60:63], a[28:31], v[44:47], v[60:63]\n"
    "v_add_f32 v96, v96, v127\n"
    "v_mfma_f32_16x16x32_bf16 v[76:79], a[172:175], v[44:47], v[76:79]\n"
    "v_mov_b32 v127, v96\n"
    "v_mfma_f32_16x16x32_bf16 v[64:67], a[100:103], v[44:47], v[64:67]\n"
    "s_nop 0\n"
    "v_mfma_f32_16x16x32_bf16 v[80:83], a[244:247], v[44:47], v[80:83]\n"
    "v_permlane32_swap_b32 v96, v127\n"
    "s_waitcnt lgkmcnt(4)\n"
    "v_mfma_f32_16x16x32_bf16 v[68:71], a[28:31], v[48:51], v[68:71]\n"
    "v_add_f32 v96, v96, v127\n"
    "v_mfma_f32_16x16x32_bf16 v[84:87], a[172:175], v[48:51], v[84:87]\n"
    "v_mov_b32 v97, 0x3727c5ac\n"
    "v_mfma_f32_16x16x32_bf16 v[72:75], a[100:103], v[48:51], v[72:75]\n"
    "v_fmamk_f32 v96, v96, 0x3ae38e39, v97\n"
    "v_mfma_f32_16x16x32_bf16 v[88:91], a[244:247], v[48:51], v[88:91]\n"
    "v_rcp_f32 v96, v96\n"
    "ds_read_b128 v[44:47], v133 offset:10240\n"
    "ds_read_b128 v[48:51], v133 offset:28672\n"
    "s_waitcnt lgkmcnt(5)\n"
    "v_mfma_f32_16x16x32_bf16 v[60:63], a[32:35], v[52:55], v[60:63]\n"
    "v_mfma_f32_16x16x32_bf16 v[76:79], a[176:179], v[52:55], v[76:79]\n"
    "v_cndmask_b32 v118, 0, v118, s[80:81]\n"
    "v_mfma_f32_16x16x32_bf16 v[64:67], a[104:107], v[52:55], v[64:67]\n"
    "v_cndmask_b32 v119, 0, v119, s[80:81]\n"
    "v_mfma_f32_16x16x32_bf16 v[80:83], a[248:251], v[52:55], v[80:83]\n"
    "v_add_f32 v94, v110, v112\n"
    "s_waitcnt lgkmcnt(4)\n"
    "v_mfma_f32_16x16x32_bf16 v[68:71], a[32:35], v[56:59], v[68:71]\n"
    "v_add_f32 v95, v114, v116\n"
    "v_mfma_f32_16x16x32_bf16 v[84:87], a[176:179], v[56:59], v[84:87]\n"
    "v_add_f32 v94, v94, v118\n"
    "v_mfma_f32_16x16x32_bf16 v[72:75], a[104:107], v[56:59], v[72:75]\n"
    "v_add_f32 v94, v94, v95\n"
    "v_mfma_f32_16x16x32_bf16 v[88:91], a[248:251], v[56:59], v[88:91]\n"
    "v_mov_b32 v127, v94\n"
    "ds_read_b128 v[52:55], v133 offset:11264\n"
    "ds_read_b128 v[56:59], v133 offset:29696\n"
    "s_waitcnt lgkmcnt(5)\n"
    "v_mfma_f32_16x16x32_bf16 v[60:63], a[36:39], v[36:39], v[60:63]\n"
    "s_nop 0\n"
    "v_mfma_f32_16x16x32_bf16 v[76:79], a[180:183], v[36:39], v[76:79]\n"
    "v_permlane16_swap_b32 v94, v127\n"
    "v_mfma_f32_16x16x32_bf16 v[64:67], a[108:111], v[36:39], v[64:67]\n"
    "v_add_f32 v94, v94, v127\n"
    "v_mfma_f32_16x16x32_bf16 v[80:83], a[252:255], v[36:39], v[80:83]\n"
    "v_mov_b32 v127, v94\n"
    "s_waitcnt lgkmcnt(4)\n"
    "v_mfma_f32_16x16x32_bf16 v[68:71], a[36:39], v[40:43], v[68:71]\n"
    "s_nop 0\n"
    "v_mfma_f32_16x16x32_bf16 v[84:87], a[180:183], v[40:43], v[84:87]\n"
    "v_permlane32_swap_b32 v94, v127\n"
    "v_mfma_f32_16x16x32_bf16 v[72:75], a[108:111], v[40:43], v[72:75]\n"
    "v_add_f32 v94, v94, v127\n"
    "v_mfma_f32_16x16x32_bf16 v[88:91], a[252:255], v[40:43], v[88:91]\n"
    "v_mul_f32 v95, 0x3d638e39, v94\n"
    "ds_read_b128 v[36:39], v133 offset:12288\n"
    "ds_read_b128 v[40:43], v133 offset:30720\n"
    "s_waitcnt lgkmcnt(5)\n"
    "v_mfma_f32_16x16x32_bf16 v[60:63], a[40:43], v[44:47], v[60:63]\n"
    "v_mul_f32 v94, 0xbae38e39, v94\n"
    "v_mfma_f32_16x16x32_bf16 v[76:79], a[184:187], v[44:47], v[76:79]\n"
    "v_sub_f32 v110, v110, v95\n"
    "v_mfma_f32_16x16x32_bf16 v[64:67], a[112:115], v[44:47], v[64:67]\n"
    "v_sub_f32 v112, v112, v95\n"
    "v_mfma_f32_16x16x32_bf16 v[80:83], v[160:163], v[44:47], v[80:83]\n"
    "v_sub_f32 v114, v114, v95\n"
    "s_waitcnt lgkmcnt(4)\n"
    "v_mfma_f32_16x16x32_bf16 v[68:71], a[40:43], v[48:51], v[68:71]\n"
    "v_sub_f32 v116, v116, v95\n"
    "v_mfma_f32_16x16x32_bf16 v[84:87], a[184:187], v[48:51], v[84:87]\n"
    "v_sub_f32 v118, v118, v95\n"
    "v_mfma_f32_16x16x32_bf16 v[72:75], a[112:115], v[48:51], v[72:75]\n"
    "v_cndmask_b32 v118, 0, v118, s[80:81]\n"
    "v_mfma_f32_16x16x32_bf16 v[88:91], v[160:163], v[48:51], v[88:91]\n"
    "v_mul_f32 v110, v110, v110\n"
    "ds_read_b128 v[44:47], v133 offset:13312\n"
    "ds_read_b128 v[48:51], v133 offset:31744\n"
    "s_waitcnt lgkmcnt(5)\n"
    "v_mfma_f32_16x16x32_bf16 v[60:63], a[44:47], v[52:55], v[60:63]\n"
    "v_fmamk_f32 v111, v110, 0x3d000000, v111\n"
    "v_mfma_f32_16x16x32_bf16 v[76:79], a[188:191], v[52:55], v[76:79]\n"
    "v_mul_f32 v112, v112, v112\n"
    "v_mfma_f32_16x16x32_bf16 v[64:67], a[116:119], v[52:55], v[64:67]\n"
    "v_fmamk_f32 v113, v112, 0x3d000000, v113\n"
    "v_mfma_f32_16x16x32_bf16 v[80:83], v[164:167], v[52:55], v[80:83]\n"
    "v_mul_f32 v114, v114, v114\n"
    "s_waitcnt lgkmcnt(4)\n"
    "v_mfma_f32_16x16x32_bf16 v[68:71], a[44:47], v[56:59], v[68:71]\n"
    "v_fmamk_f32 v115, v114, 0x3d000000, v115\n"
    "v_mfma_f32_16x16x32_bf16 v[84:87], a[188:191], v[56:59], v[84:87]\n"
    "v_mul_f32 v116, v116, v116\n"
    "v_mfma_f32_16x16x32_bf16 v[72:75], a[116:119], v[56:59], v[72:75]\n"
    "v_fmamk_f32 v117, v116, 0x3d000000, v117\n"
    "v_mfma_f32_16x16x32_bf16 v[88:91], v[164:167], v[56:59], v[88:91]\n"
    "v_mul_f32 v118, v118, v118\n"
    "ds_read_b128 v[52:55], v133 offset:14336\n"
    "ds_read_b128 v[56:59], v133 offset:32768\n"
    "buffer_load_dword v126, v129, s[20:23], s90 offen sc1\n"
    "s_waitcnt lgkmcnt(5)\n"
    "v_mfma_f32_16x16x32_bf16 v[60:63], a[48:51], v[36:39], v[60:63]\n"
    "v_fmamk_f32 v119, v118, 0x3d000000, v119\n"
    "v_mfma_f32_16x16x32_bf16 v[76:79], a[192:195], v[36:39], v[76:79]\n"
    "v_add_f32 v98, v111, v113\n"
    "v_mfma_f32_16x16x32_bf16 v[64:67], a[120:123], v[36:39], v[64:67]\n"
    "v_add_f32 v99, v115, v117\n"
    "v_mfma_f32_16x16x32_bf16 v[80:83], v[168:171], v[36:39], v[80:83]\n"
    "v_add_f32 v98, v98, v119\n"
    "s_waitcnt lgkmcnt(4)\n"
    "v_mfma_f32_16x16x32_bf16 v[68:71], a[48:51], v[40:43], v[68:71]\n"
    "v_add_f32 v98, v98, v99\n"
    "v_mfma_f32_16x16x32_bf16 v[84:87], a[192:195], v[40:43], v[84:87]\n"
    "v_mov_b32 v127, v98\n"
    "v_mfma_f32_16x16x32_bf16 v[72:75], a[120:123], v[40:43], v[72:75]\n"
    "s_nop 0\n"
    "v_mfma_f32_16x16x32_bf16 v[88:91], v[168:171], v[40:43], v[88:91]\n"
    "v_permlane16_swap_b32 v98, v127\n"
    "ds_read_b128 v[36:39], v133 offset:15360\n"
    "ds_read_b128 v[40:43], v133 offset:33792\n"
    "s_waitcnt lgkmcnt(5)\n"
    "v_mfma_f32_16x16x32_bf16 v[60:63], a[52:55], v[44:47], v[60:63]\n"
    "v_add_f32 v98, v98, v127\n"
    "v_mfma_f32_16x16x32_bf16 v[76:79], a[196:199], v[44:47], v[76:79]\n"
    "v_mov_b32 v127, v98\n"
    "v_mfma_f32_16x16x32_bf16 v[64:67], a[124:127], v[44:47], v[64:67]\n"
    "s_nop 0\n"
    "v_mfma_f32_16x16x32_bf16 v[80:83], v[172:175], v[44:47], v[80:83]\n"
    "v_permlane32_swap_b32 v98, v127\n"
    "s_waitcnt lgkmcnt(4)\n"
    "v_mfma_f32_16x16x32_bf16 v[68:71], a[52:55], v[48:51], v[68:71]\n"
    "v_add_f32 v98, v98, v127\n"
    "v_mfma_f32_16x16x32_bf16 v[84:87], a[196:199], v[48:51], v[84:87]\n"
    "v_mov_b32 v99, 0x3727c5ac\n"
    "v_mfma_f32_16x16x32_bf16 v[72:75], a[124:127], v[48:51], v[72:75]\n"
    "v_fmamk_f32 v98, v98, 0x3ae38e39, v99\n"
    "v_mfma_f32_16x16x32_bf16 v[88:91], v[172:175], v[48:51], v[88:91]\n"
    "v_rcp_f32 v98, v98\n"
    "ds_read_b128 v[44:47], v133 offset:16384\n"
    "ds_read_b128 v[48:51], v133 offset:34816\n"
    "s_waitcnt lgkmcnt(5)\n"
    "v_mfma_f32_16x16x32_bf16 v[60:63], a[56:59], v[52:55], v[60:63]\n"
    "v_mfma_f32_16x16x32_bf16 v[76:79], a[200:203], v[52:55], v[76:79]\n"
    "v_mfma_f32_16x16x32_bf16 v[64:67], a[128:131], v[52:55], v[64:67]\n"
    "v_mfma_f32_16x16x32_bf16 v[80:83], v[176:179], v[52:55], v[80:83]\n"
    "s_waitcnt lgkmcnt(4)\n"
    "v_mfma_f32_16x16x32_bf16 v[68:71], a[56:59], v[56:59], v[68:71]\n"
    "v_mfma_f32_16x16x32_bf16 v[84:87], a[200:203], v[56:59], v[84:87]\n"
    "v_mfma_f32_16x16x32_bf16 v[72:75], a[128:131], v[56:59], v[72:75]\n"
    "v_mfma_f32_16x16x32_bf16 v[88:91], v[176:179], v[56:59], v[88:91]\n"
    "ds_read_b128 v[52:55], v133 offset:17408\n"
    "ds_read_b128 v[56:59], v133 offset:35840\n"
    "s_waitcnt lgkmcnt(5)\n"
    "v_mfma_f32_16x16x32_bf16 v[60:63], a[60:63], v[36:39], v[60:63]\n"
    "v_mfma_f32_16x16x32_bf16 v[76:79], a[204:207], v[36:39], v[76:79]\n"
    "v_mfma_f32_16x16x32_bf16 v[64:67], a[132:135], v[36:39], v[64:67]\n"
    "v_mfma_f32_16x16x32_bf16 v[80:83], v[180:183], v[36:39], v[80:83]\n"
    "s_waitcnt lgkmcnt(4)\n"
    "v_mfma_f32_16x16x32_bf16 v[68:71], a[60:63], v[40:43], v[68:71]\n"
    "v_mfma_f32_16x16x32_bf16 v[84:87], a[204:207], v[40:43], v[84:87]\n"
    "v_mfma_f32_16x16x32_bf16 v[72:75], a[132:135], v[40:43], v[72:75]\n"
    "v_mfma_f32_16x16x32_bf16 v[88:91], v[180:183], v[40:43], v[88:91]\n"
    "s_waitcnt lgkmcnt(3)\n"
    "v_mfma_f32_16x16x32_bf16 v[60:63], a[64:67], v[44:47], v[60:63]\n"
    "v_mfma_f32_16x16x32_bf16 v[76:79], a[208:211], v[44:47], v[76:79]\n"
    "v_mfma_f32_16x16x32_bf16 v[64:67], a[136:139], v[44:47], v[64:67]\n"
    "v_mfma_f32_16x16x32_bf16 v[80:83], v[184:187], v[44:47], v[80:83]\n"
    "s_waitcnt lgkmcnt(2)\n"
    "v_mfma_f32_16x16x32_bf16 v[68:71], a[64:67], v[48:51], v[68:71]\n"
    "v_mfma_f32_16x16x32_bf16 v[84:87], a[208:211], v[48:51], v[84:87]\n"
    "v_mfma_f32_16x16x32_bf16 v[72:75], a[136:139], v[48:51], v[72:75]\n"
    "v_mfma_f32_16x16x32_bf16 v[88:91], v[184:187], v[48:51], v[88:91]\n"
    "s_waitcnt lgkmcnt(1)\n"
    "v_mfma_f32_16x16x32_bf16 v[60:63], a[68:71], v[52:55], v[60:63]\n"
    "v_mfma_f32_16x16x32_bf16 v[76:79], a[212:215], v[52:55], v[76:79]\n"
    "v_mfma_f32_16x16x32_bf16 v[64:67], a[140:143], v[52:55], v[64:67]\n"
    "v_mfma_f32_16x16x32_bf16 v[80:83], v[188:191], v[52:55], v[80:83]\n"
    "s_waitcnt lgkmcnt(0)\n"
    "v_mfma_f32_16x16x32_bf16 v[68:71], a[68:71], v[56:59], v[68:71]\n"
    "v_mfma_f32_16x16x32_bf16 v[84:87], a[212:215], v[56:59], v[84:87]\n"
    "v_mfma_f32_16x16x32_bf16 v[72:75], a[140:143], v[56:59], v[72:75]\n"
    "v_mfma_f32_16x16x32_bf16 v[88:91], v[188:191], v[56:59], v[88:91]\n"
    "s_add_u32 s64, s74, 3\n"
    "s_waitcnt vmcnt(0)\n"
    "v_cmp_gt_u32 vcc, s64, v126\n"
    "s_and_b64 vcc, vcc, s[82:83]\n"
    "s_cselect_b32 s76, 0, 1\n"
    "s_cmp_lt_u32 s67, 64\n"
    "s_cselect_b32 s76, s76, 0\n"
    "v_mov_b32 v127, s76\n"
    "v_pk_fma_f32 v[60:61], v[144:145], v[92:93], v[60:61] op_sel_hi:[1,0,1]\n"
    "v_pk_fma_f32 v[76:77], v[152:153], v[92:93], v[76:77] op_sel_hi:[1,0,1]\n"
    "v_pk_mul_f32 v[60:61], v[60:61], v[96:97] op_sel_hi:[1,0]\n"
    "v_pk_mul_f32 v[60:61], v[60:61], v[76:77]\n"
    "v_pk_fma_f32 v[62:63], v[146:147], v[92:93], v[62:63] op_sel_hi:[1,0,1]\n"
    "v_pk_fma_f32 v[78:79], v[154:155], v[92:93], v[78:79] op_sel_hi:[1,0,1]\n"
    "v_pk_mul_f32 v[62:63], v[62:63], v[96:97] op_sel_hi:[1,0]\n"
    "v_pk_mul_f32 v[62:63], v[62:63], v[78:79]\n"
    "v_cvt_pk_bf16_f32 v116, v60, v61\n"
    "v_cvt_pk_bf16_f32 v117, v62, v63\n"
    "v_pk_fma_f32 v[64:65], v[148:149], v[92:93], v[64:65] op_sel_hi:[1,0,1]\n"
    "v_pk_fma_f32 v[80:81], v[156:157], v[92:93], v[80:81] op_sel_hi:[1,0,1]\n"
    "v_pk_mul_f32 v[64:65], v[64:65], v[96:97] op_sel_hi:[1,0]\n"
    "v_pk_mul_f32 v[64:65], v[64:65], v[80:81]\n"
    "v_pk_fma_f32 v[66:67], v[150:151], v[92:93], v[66:67] op_sel_hi:[1,0,1]\n"
    "v_pk_fma_f32 v[82:83], v[158:159], v[92:93], v[82:83] op_sel_hi:[1,0,1]\n"
    "v_pk_mul_f32 v[66:67], v[66:67], v[96:97] op_sel_hi:[1,0]\n"
    "v_pk_mul_f32 v[66:67], v[66:67], v[82:83]\n"
    "v_cvt_pk_bf16_f32 v118, v64, v65\n"
    "v_cvt_pk_bf16_f32 v119, v66, v67\n"
    "ds_write_b128 v135, v[116:119]\n"
    "v_pk_fma_f32 v[68:69], v[144:145], v[94:95], v[68:69] op_sel_hi:[1,0,1]\n"
    "v_pk_fma_f32 v[84:85], v[152:153], v[94:95], v[84:85] op_sel_hi:[1,0,1]\n"
    "v_pk_mul_f32 v[68:69], v[68:69], v[98:99] op_sel_hi:[1,0]\n"
    "v_pk_mul_f32 v[68:69], v[68:69], v[84:85]\n"
    "v_pk_fma_f32 v[70:71], v[146:147], v[94:95], v[70:71] op_sel_hi:[1,0,1]\n"
    "v_pk_fma_f32 v[86:87], v[154:155], v[94:95], v[86:87] op_sel_hi:[1,0,1]\n"
    "v_pk_mul_f32 v[70:71], v[70:71], v[98:99] op_sel_hi:[1,0]\n"
    "v_pk_mul_f32 v[70:71], v[70:71], v[86:87]\n"
    "v_cvt_pk_bf16_f32 v120, v68, v69\n"
    "v_cvt_pk_bf16_f32 v121, v70, v71\n"
    "v_pk_fma_f32 v[72:73], v[148:149], v[94:95], v[72:73] op_sel_hi:[1,0,1]\n"
    "v_pk_fma_f32 v[88:89], v[156:157], v[94:95], v[88:89] op_sel_hi:[1,0,1]\n"
    "v_pk_mul_f32 v[72:73], v[72:73], v[98:99] op_sel_hi:[1,0]\n"
    "v_pk_mul_f32 v[72:73], v[72:73], v[88:89]\n"
    "v_pk_fma_f32 v[74:75], v[150:151], v[94:95], v[74:75] op_sel_hi:[1,0,1]\n"
    "v_pk_fma_f32 v[90:91], v[158:159], v[94:95], v[90:91] op_sel_hi:[1,0,1]\n"
    "v_pk_mul_f32 v[74:75], v[74:75], v[98:99] op_sel_hi:[1,0]\n"
    "v_pk_mul_f32 v[74:75], v[74:75], v[90:91]\n"
    "v_cvt_pk_bf16_f32 v122, v72, v73\n"
    "v_cvt_pk_bf16_f32 v123, v74, v75\n"
    "ds_write_b128 v135, v[120:123] offset:4096\n"
    "s_cmp_lg_u32 s71, 0\n"
    "s_cbranch_scc1 de_notw0_%=\n"
    "ds_write_b32 v134, v127 offset:8192\n"
    "de_notw0_%=:\n"
    "s_and_b32 s75, s74, s42\n"
    "s_mul_i32 s75, s75, 36\n"
    "s_add_u32 s75, s75, s72\n"
    "s_mul_i32 s75, s75, 36864\n"
    "s_lshl_b32 s64, s77, 10\n"
    "s_add_u32 s75, s75, s64\n"
    "s_add_u32 s78, s75, 18432\n"
    "s_add_u32 s88, s75, 4096\n"
    "s_add_u32 s89, s78, 4096\n"
    "s_waitcnt lgkmcnt(0)\n"
    "s_barrier\n"
    "ds_read_b32 v127, v134 offset:8192\n"
    "ds_read_b128 v[36:39], v134\n"
    "ds_read_b128 v[52:55], v134 offset:4096\n"
    "ds_read_b128 v[40:43], v134 offset:1024\n"
    "ds_read_b128 v[56:59], v134 offset:5120\n"
    "ds_read_b128 v[44:47], v134 offset:2048\n"
    "ds_read_b128 v[60:63], v134 offset:6144\n"
    "ds_read_b128 v[48:51], v134 offset:3072\n"
    "ds_read_b128 v[64:67], v134 offset:7168\n"
    "ds_read_b128 v[100:103], v131\n"
    "ds_read_b128 v[104:107], v131 offset:4096\n"
    "ds_read_b128 v[108:111], v131 offset:1024\n"
    "s_waitcnt lgkmcnt(11)\n"
    "v_readfirstlane_b32 s76, v127\n"
    "s_cmp_eq_u32 s76, 0\n"
    "s_cbranch_scc1 de_noh_%=\n"
    "s_mul_i32 s65, s67, 41472\n"
    "s_mul_i32 s64, s71, 9216\n"
    "s_add_u32 s64, s64, s65\n"
    "buffer_load_dwordx4 v[0:3], v139, s[24:27], s64 offen sc1\n"
    "buffer_load_dwordx4 v[4:7], v139, s[24:27], s64 offen offset:1024 sc1\n"
    "buffer_load_dwordx4 v[8:11], v139, s[24:27], s64 offen offset:2048 sc1\n"
    "buffer_load_dwordx4 v[12:15], v139, s[24:27], s64 offen offset:3072 sc1\n"
    "s_add_u32 s64, s64, 4096\n"
    "buffer_load_dwordx4 v[16:19], v139, s[24:27], s64 offen sc1\n"
    "buffer_load_dwordx4 v[20:23], v139, s[24:27], s64 offen offset:1024 sc1\n"
    "buffer_load_dwordx4 v[24:27], v139, s[24:27], s64 offen offset:2048 sc1\n"
    "buffer_load_dwordx4 v[28:31], v139, s[24:27], s64 offen offset:3072 sc1\n"
    "s_add_u32 s64, s64, 4096\n"
    "buffer_load_dwordx4 v[32:35], v139, s[24:27], s64 offen sc1\n"
    "s_add_u32 s65, s65, 36864\n"
    "s_lshl_b32 s64, s71, 10\n"
    "s_add_u32 s64, s64, s65\n"
    "buffer_load_dwordx4 v[140:143], v139, s[24:27], s64 offen sc1\n"
    "buffer_load_dwordx2 v[124:125], v130, s[24:27], s65 offen sc1\n"
    "de_noh_%=:\n"
    "s_waitcnt lgkmcnt(2)\n"
    "v_mfma_f32_16x16x32_bf16 v[68:71], v[100:103], v[36:39], 0\n"
    "v_mfma_f32_16x16x32_bf16 v[76:79], v[100:103], v[52:55], 0\n"
    "ds_read_b128 v[112:115], v131 offset:5120\n"
    "s_waitcnt lgkmcnt(2)\n"
    "v_mfma_f32_16x16x32_bf16 v[72:75], v[104:107], v[36:39], 0\n"
    "v_mfma_f32_16x16x32_bf16 v[80:83], v[104:107], v[52:55], 0\n"
    "ds_read_b128 v[100:103], v131 offset:2048\n"
    "s_waitcnt lgkmcnt(2)\n"
    "v_mfma_f32_16x16x32_bf16 v[68:71], v[108:111], v[40:43], v[68:71]\n"
    "v_mfma_f32_16x16x32_bf16 v[76:79], v[108:111], v[56:59], v[76:79]\n"
    "ds_read_b128 v[104:107], v131 offset:6144\n"
    "s_waitcnt lgkmcnt(2)\n"
    "v_mfma_f32_16x16x32_bf16 v[72:75], v[112:115], v[40:43], v[72:75]\n"
    "v_mfma_f32_16x16x32_bf16 v[80:83], v[112:115], v[56:59], v[80:83]\n"
    "ds_read_b128 v[108:111], v131 offset:3072\n"
    "s_waitcnt lgkmcnt(2)\n"
    "v_mfma_f32_16x16x32_bf16 v[68:71], v[100:103], v[44:47], v[68:71]\n"
    "v_mfma_f32_16x16x32_bf16 v[76:79], v[100:103], v[60:63], v[76:79]\n"
    "ds_read_b128 v[112:115], v131 offset:7168\n"
    "s_waitcnt lgkmcnt(2)\n"
    "v_mfma_f32_16x16x32_bf16 v[72:75], v[104:107], v[44:47], v[72:75]\n"
    "v_mfma_f32_16x16x32_bf16 v[80:83], v[104:107], v[60:63], v[80:83]\n"
    "ds_read_b128 v[100:103], v131 offset:8192\n"
    "s_waitcnt lgkmcnt(2)\n"
    "v_mfma_f32_16x16x32_bf16 v[68:71], v[108:111], v[48:51], v[68:71]\n"
    "v_mfma_f32_16x16x32_bf16 v[76:79], v[108:111], v[64:67], v[76:79]\n"
    "ds_read_b128 v[104:107], v131 offset:12288\n"
    "s_waitcnt lgkmcnt(2)\n"
    "v_mfma_f32_16x16x32_bf16 v[72:75], v[112:115], v[48:51], v[72:75]\n"
    "v_mfma_f32_16x16x32_bf16 v[80:83], v[112:115], v[64:67], v[80:83]\n"
    "ds_read_b128 v[108:111], v131 offset:9216\n"
    "s_waitcnt lgkmcnt(2)\n"
    "v_mfma_f32_16x16x32_bf16 v[84:87], v[100:103], v[36:39], 0\n"
    "v_mfma_f32_16x16x32_bf16 v[92:95], v[100:103], v[52:55], 0\n"
    "ds_read_b128 v[112:115], v131 offset:13312\n"
    "s_waitcnt lgkmcnt(2)\n"
    "v_mfma_f32_16x16x32_bf16 v[88:91], v[104:107], v[36:39], 0\n"
    "v_mfma_f32_16x16x32_bf16 v[96:99], v[104:107], v[52:55], 0\n"
    "v_cvt_pk_bf16_f32 v116, v68, v69\n"
    "v_cvt_pk_bf16_f32 v117, v70, v71\n"
    "ds_read_b128 v[100:103], v131 offset:10240\n"
    "s_waitcnt lgkmcnt(2)\n"
    "v_mfma_f32_16x16x32_bf16 v[84:87], v[108:111], v[40:43], v[84:87]\n"
    "v_mfma_f32_16x16x32_bf16 v[92:95], v[108:111], v[56:59], v[92:95]\n"
    "v_cvt_pk_bf16_f32 v118, v72, v73\n"
    "v_cvt_pk_bf16_f32 v119, v74, v75\n"
    "ds_read_b128 v[104:107], v131 offset:14336\n"
    "s_waitcnt lgkmcnt(2)\n"
    "v_mfma_f32_16x16x32_bf16 v[88:91], v[112:115], v[40:43], v[88:91]\n"
    "v_mfma_f32_16x16x32_bf16 v[96:99], v[112:115], v[56:59], v[96:99]\n"
    "buffer_store_dwordx4 v[116:119], v139, s[28:31], s75 offen sc1\n"
    "ds_read_b128 v[108:111], v131 offset:11264\n"
    "s_waitcnt lgkmcnt(2)\n"
    "v_mfma_f32_16x16x32_bf16 v[84:87], v[100:103], v[44:47], v[84:87]\n"
    "v_mfma_f32_16x16x32_bf16 v[92:95], v[100:103], v[60:63], v[92:95]\n"
    "v_cvt_pk_bf16_f32 v120, v76, v77\n"
    "v_cvt_pk_bf16_f32 v121, v78, v79\n"
    "ds_read_b128 v[112:115], v131 offset:15360\n"
    "s_waitcnt lgkmcnt(2)\n"
    "v_mfma_f32_16x16x32_bf16 v[88:91], v[104:107], v[44:47], v[88:91]\n"
    "v_mfma_f32_16x16x32_bf16 v[96:99], v[104:107], v[60:63], v[96:99]\n"
    "v_cvt_pk_bf16_f32 v122, v80, v81\n"
    "v_cvt_pk_bf16_f32 v123, v82, v83\n"
    "ds_read_b128 v[100:103], v131 offset:16384\n"
    "s_waitcnt lgkmcnt(2)\n"
    "v_mfma_f32_16x16x32_bf16 v[84:87], v[108:111], v[48:51], v[84:87]\n"
    "v_mfma_f32_16x16x32_bf16 v[92:95], v[108:111], v[64:67], v[92:95]\n"
    "buffer_store_dwordx4 v[120:123], v139, s[28:31], s78 offen sc1\n"
    "ds_read_b128 v[104:107], v131 offset:20480\n"
    "s_waitcnt lgkmcnt(2)\n"
    "v_mfma_f32_16x16x32_bf16 v[88:91], v[112:115], v[48:51], v[88:91]\n"
    "v_mfma_f32_16x16x32_bf16 v[96:99], v[112:115], v[64:67], v[96:99]\n"
    "ds_read_b128 v[108:111], v131 offset:17408\n"
    "s_waitcnt lgkmcnt(2)\n"
    "v_mfma_f32_16x16x32_bf16 v[68:71], v[100:103], v[36:39], 0\n"
    "v_mfma_f32_16x16x32_bf16 v[76:79], v[100:103], v[52:55], 0\n"
    "ds_read_b128 v[112:115], v131 offset:21504\n"
    "s_waitcnt lgkmcnt(2)\n"
    "v_mfma_f32_16x16x32_bf16 v[72:75], v[104:107], v[36:39], 0\n"
    "v_mfma_f32_16x16x32_bf16 v[80:83], v[104:107], v[52:55], 0\n"
    "v_cvt_pk_bf16_f32 v116, v84, v85\n"
    "v_cvt_pk_bf16_f32 v117, v86, v87\n"
    "ds_read_b128 v[100:103], v131 offset:18432\n"
    "s_waitcnt lgkmcnt(2)\n"
    "v_mfma_f32_16x16x32_bf16 v[68:71], v[108:111], v[40:43], v[68:71]\n"
    "v_mfma_f32_16x16x32_bf16 v[76:79], v[108:111], v[56:59], v[76:79]\n"
    "v_cvt_pk_bf16_f32 v118, v88, v89\n"
    "v_cvt_pk_bf16_f32 v119, v90, v91\n"
    "ds_read_b128 v[104:107], v131 offset:22528\n"
    "s_waitcnt lgkmcnt(2)\n"
    "v_mfma_f32_16x16x32_bf16 v[72:75], v[112:115], v[40:43], v[72:75]\n"
    "v_mfma_f32_16x16x32_bf16 v[80:83], v[112:115], v[56:59], v[80:83]\n"
    "buffer_store_dwordx4 v[116:119], v139, s[28:31], s75 offen offset:1024 sc1\n"
    "ds_read_b128 v[108:111], v131 offset:19456\n"
    "s_waitcnt lgkmcnt(2)\n"
    "v_mfma_f32_16x16x32_bf16 v[68:71], v[100:103], v[44:47], v[68:71]\n"
    "v_mfma_f32_16x16x32_bf16 v[76:79], v[100:103], v[60:63], v[76:79]\n"
    "v_cvt_pk_bf16_f32 v120, v92, v93\n"
    "v_cvt_pk_bf16_f32 v121, v94, v95\n"
    "ds_read_b128 v[112:115], v131 offset:23552\n"
    "s_waitcnt lgkmcnt(2)\n"
    "v_mfma_f32_16x16x32_bf16 v[72:75], v[104:107], v[44:47], v[72:75]\n"
    "v_mfma_f32_16x16x32_bf16 v[80:83], v[104:107], v[60:63], v[80:83]\n"
    "v_cvt_pk_bf16_f32 v122, v96, v97\n"
    "v_cvt_pk_bf16_f32 v123, v98, v99\n"
    "s_waitcnt lgkmcnt(1)\n"
    "v_mfma_f32_16x16x32_bf16 v[68:71], v[108:111], v[48:51], v[68:71]\n"
    "v_mfma_f32_16x16x32_bf16 v[76:79], v[108:111], v[64:67], v[76:79]\n"
    "buffer_store_dwordx4 v[120:123], v139, s[28:31], s78 offen offset:1024 sc1\n"
    "s_waitcnt lgkmcnt(0)\n"
    "v_mfma_f32_16x16x32_bf16 v[72:75], v[112:115], v[48:51], v[72:75]\n"
    "v_mfma_f32_16x16x32_bf16 v[80:83], v[112:115], v[64:67], v[80:83]\n"
    "v_mfma_f32_16x16x32_bf16 v[84:87], v[192:195], v[36:39], 0\n"
    "v_mfma_f32_16x16x32_bf16 v[92:95], v[192:195], v[52:55], 0\n"
    "v_mfma_f32_16x16x32_bf16 v[88:91], v[208:211], v[36:39], 0\n"
    "v_mfma_f32_16x16x32_bf16 v[96:99], v[208:211], v[52:55], 0\n"
    "v_cvt_pk_bf16_f32 v116, v68, v69\n"
    "v_cvt_pk_bf16_f32 v117, v70, v71\n"
    "v_mfma_f32_16x16x32_bf16 v[84:87], v[196:199], v[40:43], v[84:87]\n"
    "v_mfma_f32_16x16x32_bf16 v[92:95], v[196:199], v[56:59], v[92:95]\n"
    "v_cvt_pk_bf16_f32 v118, v72, v73\n"
    "v_cvt_pk_bf16_f32 v119, v74, v75\n"
    "v_mfma_f32_16x16x32_bf16 v[88:91], v[212:215], v[40:43], v[88:91]\n"
    "v_mfma_f32_16x16x32_bf16 v[96:99], v[212:215], v[56:59], v[96:99]\n"
    "buffer_store_dwordx4 v[116:119], v139, s[28:31], s75 offen offset:2048 sc1\n"
    "v_mfma_f32_16x16x32_bf16 v[84:87], v[200:203], v[44:47], v[84:87]\n"
    "v_mfma_f32_16x16x32_bf16 v[92:95], v[200:203], v[60:63], v[92:95]\n"
    "v_cvt_pk_bf16_f32 v120, v76, v77\n"
    "v_cvt_pk_bf16_f32 v121, v78, v79\n"
    "v_mfma_f32_16x16x32_bf16 v[88:91], v[216:219], v[44:47], v[88:91]\n"
    "v_mfma_f32_16x16x32_bf16 v[96:99], v[216:219], v[60:63], v[96:99]\n"
    "v_cvt_pk_bf16_f32 v122, v80, v81\n"
    "v_cvt_pk_bf16_f32 v123, v82, v83\n"
    "v_mfma_f32_16x16x32_bf16 v[84:87], v[204:207], v[48:51], v[84:87]\n"
    "v_mfma_f32_16x16x32_bf16 v[92:95], v[204:207], v[64:67], v[92:95]\n"
    "buffer_store_dwordx4 v[120:123], v139, s[28:31], s78 offen offset:2048 sc1\n"
    "v_mfma_f32_16x16x32_bf16 v[88:91], v[220:223], v[48:51], v[88:91]\n"
    "v_mfma_f32_16x16x32_bf16 v[96:99], v[220:223], v[64:67], v[96:99]\n"
    "s_cmp_ge_u32 s71, 2\n"
    "s_cbranch_scc1 de_four_%=\n"
    "v_mfma_f32_16x16x32_bf16 v[68:71], v[224:227], v[36:39], 0\n"
    "v_mfma_f32_16x16x32_bf16 v[76:79], v[224:227], v[52:55], 0\n"
    "v_mfma_f32_16x16x32_bf16 v[72:75], v[240:243], v[36:39], 0\n"
    "v_mfma_f32_16x16x32_bf16 v[80:83], v[240:243], v[52:55], 0\n"
    "v_cvt_pk_bf16_f32 v116, v84, v85\n"
    "v_cvt_pk_bf16_f32 v117, v86, v87\n"
    "v_mfma_f32_16x16x32_bf16 v[68:71], v[228:231], v[40:43], v[68:71]\n"
    "v_mfma_f32_16x16x32_bf16 v[76:79], v[228:231], v[56:59], v[76:79]\n"
    "v_cvt_pk_bf16_f32 v118, v88, v89\n"
    "v_cvt_pk_bf16_f32 v119, v90, v91\n"
    "v_mfma_f32_16x16x32_bf16 v[72:75], v[244:247], v[40:43], v[72:75]\n"
    "v_mfma_f32_16x16x32_bf16 v[80:83], v[244:247], v[56:59], v[80:83]\n"
    "buffer_store_dwordx4 v[116:119], v139, s[28:31], s75 offen offset:3072 sc1\n"
    "v_mfma_f32_16x16x32_bf16 v[68:71], v[232:235], v[44:47], v[68:71]\n"
    "v_mfma_f32_16x16x32_bf16 v[76:79], v[232:235], v[60:63], v[76:79]\n"
    "v_cvt_pk_bf16_f32 v120, v92, v93\n"
    "v_cvt_pk_bf16_f32 v121, v94, v95\n"
    "v_mfma_f32_16x16x32_bf16 v[72:75], v[248:251], v[44:47], v[72:75]\n"
    "v_mfma_f32_16x16x32_bf16 v[80:83], v[248:251], v[60:63], v[80:83]\n"
    "v_cvt_pk_bf16_f32 v122, v96, v97\n"
    "v_cvt_pk_bf16_f32 v123, v98, v99\n"
    "v_mfma_f32_16x16x32_bf16 v[68:71], v[236:239], v[48:51], v[68:71]\n"
    "v_mfma_f32_16x16x32_bf16 v[76:79], v[236:239], v[64:67], v[76:79]\n"
    "buffer_store_dwordx4 v[120:123], v139, s[28:31], s78 offen offset:3072 sc1\n"
    "v_mfma_f32_16x16x32_bf16 v[72:75], v[252:255], v[48:51], v[72:75]\n"
    "v_mfma_f32_16x16x32_bf16 v[80:83], v[252:255], v[64:67], v[80:83]\n"
    "s_nop 3\n"
    "v_cvt_pk_bf16_f32 v116, v68, v69\n"
    "v_cvt_pk_bf16_f32 v117, v70, v71\n"
    "s_nop 0\n"
    "v_cvt_pk_bf16_f32 v118, v72, v73\n"
    "v_cvt_pk_bf16_f32 v119, v74, v75\n"
    "buffer_store_dwordx4 v[116:119], v139, s[28:31], s88 offen sc1\n"
    "v_cvt_pk_bf16_f32 v120, v76, v77\n"
    "v_cvt_pk_bf16_f32 v121, v78, v79\n"
    "v_cvt_pk_bf16_f32 v122, v80, v81\n"
    "v_cvt_pk_bf16_f32 v123, v82, v83\n"
    "buffer_store_dwordx4 v[120:123], v139, s[28:31], s89 offen sc1\n"
    "s_branch de_stored_%=\n"
    "de_four_%=:\n"
    "s_nop 7\n"
    "v_cvt_pk_bf16_f32 v116, v84, v85\n"
    "v_cvt_pk_bf16_f32 v117, v86, v87\n"
    "v_cvt_pk_bf16_f32 v118, v88, v89\n"
    "v_cvt_pk_bf16_f32 v119, v90, v91\n"
    "buffer_store_dwordx4 v[116:119], v139, s[28:31], s75 offen offset:3072 sc1\n"
    "v_cvt_pk_bf16_f32 v120, v92, v93\n"
    "v_cvt_pk_bf16_f32 v121, v94, v95\n"
    "v_cvt_pk_bf16_f32 v122, v96, v97\n"
    "v_cvt_pk_bf16_f32 v123, v98, v99\n"
    "buffer_store_dwordx4 v[120:123], v139, s[28:31], s78 offen offset:3072 sc1\n"
    "de_stored_%=:\n"
    "s_nop 1\n"
    "v_add_u32 v127, 1, s74\n"
    "s_cmp_eq_u32 s76, 0\n"
    "s_cbranch_scc1 de_nput_%=\n"
    "s_waitcnt vmcnt(8)\n"
    "ds_write_b128 v132, v[0:3]\n"
    "ds_write_b128 v132, v[4:7] offset:1024\n"
    "ds_write_b128 v132, v[8:11] offset:2048\n"
    "ds_write_b128 v132, v[12:15] offset:3072\n"
    "ds_write_b128 v132, v[16:19] offset:4096\n"
    "ds_write_b128 v132, v[20:23] offset:5120\n"
    "ds_write_b128 v132, v[24:27] offset:6144\n"
    "ds_write_b128 v132, v[28:31] offset:7168\n"
    "ds_write_b128 v132, v[32:35] offset:8192\n"
    "ds_write_b128 v136, v[140:143]\n"
    "ds_write_b64 v137, v[124:125]\n"
    "de_nput_%=:\n"
    "s_waitcnt vmcnt(0)\n"
    "buffer_store_dword v127, v128, s[20:23], 0 offen sc1\n"
    "s_waitcnt lgkmcnt(0)\n"
    "s_barrier\n"
    "s_add_u32 s74, s74, 2\n"
    "s_cmp_lg_u32 s76, 0\n"
    "s_cbranch_scc1 de_loop_%=\n"
    "s_cmp_lt_u32 s74, 64\n"
    "s_cbranch_scc1 de_wait_%=\n"
    "s_waitcnt vmcnt(0)\n"
    :: "s"(__builtin_amdgcn_kernarg_segment_ptr()), "s"(ids)
    : "v0", "v1", "v2", "v3", "v4", "v5", "v6", "v7", "v8", "v9", "v10", "v11", "v12", "v13", "v14", "v15", "v16", "v17", "v18", "v19", "v20", "v21", "v22",
      "v23", "v24", "v25", "v26", "v27", "v28", "v29", "v30", "v31", "v32", "v33", "v34", "v35", "v36", "v37", "v38", "v39", "v40", "v41", "v42", "v43",
      "v44", "v45", "v46", "v47", "v48", "v49", "v50", "v51", "v52", "v53", "v54", "v55", "v56", "v57", "v58", "v59", "v60", "v61", "v62", "v63", "v64",
      "v65", "v66", "v67", "v68", "v69", "v70", "v71", "v72", "v73", "v74", "v75", "v76", "v77", "v78", "v79", "v80", "v81", "v82", "v83", "v84", "v85",
      "v86", "v87", "v88", "v89", "v90", "v91", "v92", "v93", "v94", "v95", "v96", "v97", "v98", "v99", "v100", "v101", "v102", "v103", "v104", "v105",
      "v106", "v107", "v108", "v109", "v110", "v111", "v112", "v113", "v114", "v115", "v116", "v117", "v118", "v119", "v120", "v121", "v122", "v123", "v124",
      "v125", "v126", "v127", "v128", "v129", "v130", "v131", "v132", "v133", "v134", "v135", "v136", "v137", "v138", "v139", "v140", "v141", "v142", "v143",
      "v144", "v145", "v146", "v147", "v148", "v149", "v150", "v151", "v152", "v153", "v154", "v155", "v156", "v157", "v158", "v159", "v160", "v161", "v162",
      "v163", "v164", "v165", "v166", "v167", "v168", "v169", "v170", "v171", "v172", "v173", "v174", "v175", "v176", "v177", "v178", "v179", "v180", "v181",
      "v182", "v183", "v184", "v185", "v186", "v187", "v188", "v189", "v190", "v191", "v192", "v193", "v194", "v195", "v196", "v197", "v198", "v199", "v200",
      "v201", "v202", "v203", "v204", "v205", "v206", "v207", "v208", "v209", "v210", "v211", "v212", "v213", "v214", "v215", "v216", "v217", "v218", "v219",
      "v220", "v221", "v222", "v223", "v224", "v225", "v226", "v227", "v228", "v229", "v230", "v231", "v232", "v233", "v234", "v235", "v236", "v237", "v238",
      "v239", "v240", "v241", "v242", "v243", "v244", "v245", "v246", "v247", "v248", "v249", "v250", "v251", "v252", "v253", "v254", "v255", "a0", "a1",
      "a2", "a3", "a4", "a5", "a6", "a7", "a8", "a9", "a10", "a11", "a12", "a13", "a14", "a15", "a16", "a17", "a18", "a19", "a20", "a21", "a22", "a23",
      "a24", "a25", "a26", "a27", "a28", "a29", "a30", "a31", "a32", "a33", "a34", "a35", "a36", "a37", "a38", "a39", "a40", "a41", "a42", "a43", "a44",
      "a45", "a46", "a47", "a48", "a49", "a50", "a51", "a52", "a53", "a54", "a55", "a56", "a57", "a58", "a59", "a60", "a61", "a62", "a63", "a64", "a65",
      "a66", "a67", "a68", "a69", "a70", "a71", "a72", "a73", "a74", "a75", "a76", "a77", "a78", "a79", "a80", "a81", "a82", "a83", "a84", "a85", "a86",
      "a87", "a88", "a89", "a90", "a91", "a92", "a93", "a94", "a95", "a96", "a97", "a98", "a99", "a100", "a101", "a102", "a103", "a104", "a105", "a106",
      "a107", "a108", "a109", "a110", "a111", "a112", "a113", "a114", "a115", "a116", "a117", "a118", "a119", "a120", "a121", "a122", "a123", "a124", "a125",
      "a126", "a127", "a128", "a129", "a130", "a131", "a132", "a133", "a134", "a135", "a136", "a137", "a138", "a139", "a140", "a141", "a142", "a143", "a144",
      "a145", "a146", "a147", "a148", "a149", "a150", "a151", "a152", "a153", "a154", "a155", "a156", "a157", "a158", "a159", "a160", "a161", "a162", "a163",
      "a164", "a165", "a166", "a167", "a168", "a169", "a170", "a171", "a172", "a173", "a174", "a175", "a176", "a177", "a178", "a179", "a180", "a181", "a182",
      "a183", "a184", "a185", "a186", "a187", "a188", "a189", "a190", "a191", "a192", "a193", "a194", "a195", "a196", "a197", "a198", "a199", "a200", "a201",
      "a202", "a203", "a204", "a205", "a206", "a207", "a208", "a209", "a210", "a211", "a212", "a213", "a214", "a215", "a216", "a217", "a218", "a219", "a220",
      "a221", "a222", "a223", "a224", "a225", "a226", "a227", "a228", "a229", "a230", "a231", "a232", "a233", "a234", "a235", "a236", "a237", "a238", "a239",
      "a240", "a241", "a242", "a243", "a244", "a245", "a246", "a247", "a248", "a249", "a250", "a251", "a252", "a253", "a254", "a255", "s20", "s21", "s22",
      "s23", "s24", "s25", "s26", "s27", "s28", "s29", "s30", "s31", "s36", "s37", "s38", "s39", "s40", "s41", "s42", "s43", "s44", "s45", "s46", "s47",
      "s48", "s49", "s50", "s51", "s52", "s53", "s54", "s55", "s56", "s57", "s58", "s59", "s60", "s61", "s62", "s63", "s64", "s65", "s66", "s67", "s68",
      "s69", "s70", "s71", "s72", "s73", "s74", "s75", "s76", "s77", "s78", "s79", "s80", "s81", "s82", "s83", "s84", "s85", "s86", "s87", "s88", "s89",
      "s90", "s91", "s92", "s93", "s94", "s95", "s96", "s97", "s98", "s99", "vcc", "scc", "memory");
}

DEV void role_f(const Params& P, volatile LAS u32* turn, int u, int w, int l) {
    const rsrc_t ctl = mk(P.ws, CTL_BYTES);
    const rsrc_t rp = mk(P.ws + OFF_PDE, NSEG * 36 * TILEB);
    const rsrc_t rh = mk(P.ws + OFF_HN, NSEG * HSEG);
    const rsrc_t ro = mk(P.out, SEQ * ROWB);
    const int i16 = l & 15, tt = u / 18, pr = u % 18;
    const int ka = 16 * ((i16 >> 2) & 1) + 4 * (i16 >> 3) + (i16 & 3);
    const u4 sa = pick(ka, l), sb = pick(ka + 8, l);
    bool dead = false;
#pragma unroll 1
    for (int seg = w; seg < NSEG; seg += 4) {
        take_turn(turn, seg, dead);
        wait_words(ctl, OFF_MBDE + (u * 72 + (seg & 1) * 36 + l) * 64, l < 36, seg + 1, dead);
        *turn = seg + 1;
        STAMP(8 + seg);
        const u4 h = ld4(rh, l * 16, (u32)seg * HSEG + u * 1024);
        const f4 z = {0, 0, 0, 0};
        f4 a = z, b = z;
        sum_blocks<36>(rp, (u32)(seg & P.ring) * 36 * TILEB + u * 1024, l, sa, sb, a, b);
        a = mma32(sa, h, a); b = mma32(sb, h, b);
        st4<0>(cat(pk4(a), pk4(b)), ro, i16 * ROWB + (l >> 4) * 16, (u32)(seg * 32 + tt * 16) * ROWB + pr * 64);
    }
}

DEV int fresh_lane() { int l; asm volatile("v_mbcnt_lo_u32_b32 %0, -1, 0\n\tv_mbcnt_hi_u32_b32 %0, -1, %0" : "=v"(l)); return l; }

__global__ void __launch_bounds__(256, 1) k_main(Params P) {
    const int b = blockIdx.x, xi = b & 7, bi = b >> 3;
    const int w = __builtin_amdgcn_readfirstlane(threadIdx.x >> 6);
    STAMP(0);
    int wi;
    if (P.map < 2) {
        if (xi < 4 && bi < 16) {
            if (P.mask & 2) role_chain((u32)(xi * 8 + (bi >> 1)) | (bi & 1) << 8 | xi << 12 | (bi * 4 + xi) << 16 | w << 24);
            STAMP(7);
            return;
        }
        wi = xi >= 4 ? bi * 4 + xi - 4 : 128 + (bi - 16) * 4 + xi;
        if (P.map == 1) wi = wi < 32 ? wi : (wi < 104 ? wi + 72 : (wi < 176 ? wi - 72 : wi));
    } else {
        const int other = P.map == 2 ? 2 : 1;
        if (xi == 0 || xi == other) {
            const int x2 = xi != 0;
            if (P.mask & 2) role_chain((u32)(x2 * 16 + (bi >> 1)) | (bi & 1) << 8 | (x2 * 2 + (bi >> 4)) << 12 | (bi * 2 + x2) << 16 | w << 24);
            STAMP(7);
            return;
        }
        wi = bi * 6 + (xi < other ? xi - 1 : xi - 2);
    }
    if (P.mask & 1) {
        int m, h, par;
        if (P.map == 1 && xi < 4) { const int j = bi - 16; m = 1 + (j >> 3); h = xi * 8 + (j & 7); par = 0; }
        else if (P.map == 1) { const int j = bi * 4 + xi - 4; h = j & 31; m = j < 64 ? 1 + (j >> 5) : 0; par = j < 64 ? 1 : (j >> 5) & 1; }
        else { const int u = wi % 96; m = u >> 5; h = u & 31; par = wi / 96; }
        role_proj((u32)h | m << 8 | par << 12 | w << 24);
    }
    STAMP(3);
    if (wi < 32) { if (P.mask & 4) role_ab((u32)wi | b << 16 | w << 24); }
    else if (wi < 104) {
        extern __shared__ __attribute__((aligned(16))) char smem[];
        volatile LAS u32* turn = (volatile LAS u32*)smem;
        *turn = 0;
        bar();
        if (wi < 68) { if (P.mask & 8) role_c(P, turn, wi - 32, w, fresh_lane()); }
        else if (P.mask & 32) role_f(P, turn, wi - 68, w, fresh_lane());
    }
    else if (wi < 176) { if (P.mask & 16) role_de((u32)((wi - 104) % 36) | ((wi - 104) / 36) << 8 | b << 16 | w << 24); }
    STAMP(7);
}

__global__ void __launch_bounds__(192) k_pre(const char* x, char* ws, int force) {
    __shared__ float red[2][3][16];
    const int b = blockIdx.x, l = threadIdx.x & 63, w = threadIdx.x >> 6;
    {
        const u4 z = {0, 0, 0, 0}, e = {~0u, ~0u, ~0u, ~0u};
        for (u32 o = (b * 192 + threadIdx.x) * 16; o < HOP_PROBE + 32768; o += 256 * 192 * 16)
            if ((o >> 14) != (HOP_PROBE >> 14)) *(u4*)(ws + o) = e;
        if (b < 6 && b * 192 + threadIdx.x < 1024) *(u4*)(ws + OFF_MBKV + (b * 192 + threadIdx.x) * 16) = z;
        for (u32 o = OFF_MBP1 + (b * 192 + threadIdx.x) * 16; o < CTL_END; o += 256 * 192 * 16) *(u4*)(ws + o) = z;
    }
    if (b < 128) {
        const int r16 = l & 15, g = l >> 4;
        const char* src = x + (size_t)(b * 16 + r16) * ROWB + w * 384 + g * 16;
        u4 v[6];
#pragma unroll
        for (int i = 0; i < 6; ++i) v[i] = *(const u4*)(src + i * 64);
        float s = 0.f;
#pragma unroll
        for (int i = 0; i < 6; ++i)
#pragma unroll
            for (int j = 0; j < 4; ++j) s += blo(v[i][j]) + bhi(v[i][j]);
        s = sum_x32(sum_x16(s));
        if (l < 16) red[0][w][l] = s;
        __syncthreads();
        const float mean = (red[0][0][r16] + red[0][1][r16] + red[0][2][r16]) * (1.f / DM);
        float q = 0.f;
#pragma unroll
        for (int i = 0; i < 6; ++i)
#pragma unroll
            for (int j = 0; j < 4; ++j) { const float a = blo(v[i][j]) - mean, c = bhi(v[i][j]) - mean; q += a * a + c * c; }
        q = sum_x32(sum_x16(q));
        if (l < 16) red[1][w][l] = q;
        __syncthreads();
        const float rs = 1.f / __builtin_sqrtf((red[1][0][r16] + red[1][1][r16] + red[1][2][r16]) * (1.f / DM) + 1e-5f);
        char* dst = ws + OFF_XF + (size_t)b * 18432 + w * 6144 + l * 16;
#pragma unroll
        for (int i = 0; i < 6; ++i) {
            u4 o;
#pragma unroll
            for (int j = 0; j < 4; ++j) o[j] = pk((blo(v[i][j]) - mean) * rs, (bhi(v[i][j]) - mean) * rs);
            *(u4*)(dst + i * 1024) = o;
        }
    } else if (b == 128) {
        u32 lat[2] = {~0u, ~0u};
        if (w == 0) {
#pragma unroll
            for (int r = 0; r < 6; ++r) {
                const unsigned long long t0 = __builtin_amdgcn_s_memtime();
                asm volatile("s_waitcnt lgkmcnt(0)\n\tglobal_store_dword %0, %1, off sc1\n\ts_waitcnt vmcnt(0)" :: "v"(ws + HOP_PROBE + (r & 1) * 8192 + (r >> 1) * 1024), "v"(r) : "memory");
                const u32 d = (u32)(__builtin_amdgcn_s_memtime() - t0);
                lat[r & 1] = d < lat[r & 1] ? d : lat[r & 1];
            }
        }
        if (threadIdx.x == 0) { u32* c = (u32*)(ws + OFF_CFG); c[0] = force >= 0 ? (u32)force : (lat[1] < lat[0] ? 1u : 0u); c[1] = lat[0]; c[2] = lat[1]; }
    }
}

static const int g_dbg[16] = {63, 0, 0, 0, 12, 55, 10, -1, 1, 63};

extern "C" void kernel_launch(void* const* d_in, const int* in_sizes, int n_in, void* d_out, int out_size, void* d_ws, size_t ws_size,
                              hipStream_t stream) {
    char* ws = (char*)(((uintptr_t)d_ws + (2u << 20) - 1) & ~(uintptr_t)((2u << 20) - 1));
    Params P;
    P.x = (const char*)d_in[0]; P.mem0 = (const char*)d_in[1]; P.wq = (const char*)d_in[2]; P.wk = (const char*)d_in[3];
    P.wv = (const char*)d_in[4]; P.wo = (const char*)d_in[5]; P.wg = (const char*)d_in[8]; P.wu = (const char*)d_in[9];
    P.wd = (const char*)d_in[10]; P.out = (char*)d_out; P.ws = ws;
    P.map = g_dbg[8]; P.ring = g_dbg[9];
    P.d1 = g_dbg[4]; P.d2 = g_dbg[5]; P.d3 = g_dbg[6];
    (void)hipFuncSetAttribute((const void*)k_main, hipFuncAttributeMaxDynamicSharedMemorySize, LDS_BYTES);
    k_pre<<<256, 192, 0, stream>>>(P.x, ws, g_dbg[7]);
    if (g_dbg[1] == 0) {
        P.mask = g_dbg[0];
        k_main<<<256, 256, LDS_BYTES, stream>>>(P);
    } else {
        for (int r = 0; r < 6; ++r) if ((g_dbg[0] >> r) & 1) { P.mask = 1 << r; k_main<<<256, 256, LDS_BYTES, stream>>>(P); }
    }
}
```
